```python
import torch

def _softmax(x, axis=-1):
    return torch.softmax(x, dim=axis)

def _gelu(x):
    return torch.nn.functional.gelu(x, approximate='tanh')

def _silu(x):
    return torch.nn.functional.silu(x)

def _sigmoid(x):
    return torch.sigmoid(x)

def _layernorm(x, axis=-1, eps=1e-05):
    m = x.mean(dim=axis, keepdim=True)
    v = x.var(dim=axis, keepdim=True, unbiased=False)
    return (x - m) / torch.sqrt(v + eps)

def _scatter_add(tgt, idx, src, axis=0):
    return tgt.clone().index_add_(axis, idx, src)

def _segment_sum(src, seg, n):
    out = torch.zeros((n,) + src.shape[1:], dtype=src.dtype)
    return out.index_add_(0, seg, src)

def _pad(x, pw):
    flat = tuple((v for pair in reversed(pw) for v in pair))
    return torch.nn.functional.pad(x, flat)
kernel_name = 'adaLN_cond__sliding_block_cached__geglu_ffn__decode'

def setup_inputs():

    _w = lambda a: (lambda t: t.to(torch.bfloat16) if t.is_floating_point() else t)(torch.as_tensor(a))

    def _A_setup():
        B, Sq, D = (1, 1, 576)
        Hq, Hkv, Sk, Dh = (32, 16, 65536, 128)
        assert Hq % Hkv == 0, (Hq, Hkv)
        return {'x': _w((lambda *s: torch.randn(*s, dtype=torch.bfloat16) * 0.05)(B, Sq, D)), 'k_cache': _w((lambda *s: torch.randn(*s, dtype=torch.bfloat16) * 3.2)(B, Hkv, Sk, Dh)), 'v_cache': _w((lambda *s: torch.randn(*s, dtype=torch.bfloat16) * 0.4)(B, Hkv, Sk, Dh)), 'wq': _w((lambda *s: torch.randn(*s, dtype=torch.bfloat16) * 3.2)(D, Hq * Dh) * D ** (-0.5)), 'wo': _w((lambda *s: torch.randn(*s, dtype=torch.bfloat16) * 0.4)(Hq * Dh, D) * (Hq * Dh) ** (-0.5))}
    _A_in = _A_setup()

    def _B_setup():
        B, S, D, F = (1, 1, 576, 4608)
        return {'x': _w((lambda *s: torch.randn(*s, dtype=torch.bfloat16) * 0.05)(B, S, D)), 'w_gate': _w((lambda *s: torch.randn(*s, dtype=torch.bfloat16) * 0.05)(D, F)), 'w_up': _w((lambda *s: torch.randn(*s, dtype=torch.bfloat16) * 0.05)(D, F)), 'w_down': _w((lambda *s: torch.randn(*s, dtype=torch.bfloat16) * 0.05)(F, D))}
    _B_in = _B_setup()
    return {'A_x': _A_in['x'], 'A_k_cache': _A_in['k_cache'], 'A_v_cache': _A_in['v_cache'], 'A_wq': _A_in['wq'], 'A_wo': _A_in['wo'], 'B_x': _B_in['x'], 'B_w_gate': _B_in['w_gate'], 'B_w_up': _B_in['w_up'], 'B_w_down': _B_in['w_down'], 'c': _w((lambda *s: torch.randn(*s, dtype=torch.bfloat16) * 0.1)(1, 576) * 8.0), 'w_mod': _w((lambda *s: torch.randn(*s, dtype=torch.bfloat16) * 0.1)(576, 6 * 576) * 576 ** (-0.5) * 8.0)}

def reference(A_x, A_k_cache, A_v_cache, A_wq, A_wo, B_x, B_w_gate, B_w_up, B_w_down, c, w_mod):

    def _A_ref(x, k_cache, v_cache, wq, wo):
        B, Sq, D = x.shape
        Hq, Dh = (32, 128)
        Hkv, Sk = (k_cache.shape[1], k_cache.shape[2])
        W = min(4096, max(256, Sk // 4))
        rep = Hq // Hkv
        q = (lambda x, *d: x.permute(*d))((x @ wq).reshape(B, Sq, Hq, Dh), 0, 2, 1, 3)
        k = (lambda x, n, axis: torch.repeat_interleave(x, n, dim=axis))(k_cache[:, :, Sk - W:, :], rep, axis=1)
        v = (lambda x, n, axis: torch.repeat_interleave(x, n, dim=axis))(v_cache[:, :, Sk - W:, :], rep, axis=1)
        scores = torch.einsum('bhqd,bhkd->bhqk', q, k) * Dh ** (-0.5)
        p = _softmax(scores, axis=-1)
        o = torch.einsum('bhqk,bhkd->bhqd', p, v)
        return (lambda x, *d: x.permute(*d))(o, 0, 2, 1, 3).reshape(B, Sq, Hq * Dh) @ wo

    def _B_ref(x, w_gate, w_up, w_down):
        return _gelu(x @ w_gate) * (x @ w_up) @ w_down
    x = A_x
    mod = (_silu(c) @ w_mod).reshape(c.shape[0], 6, 1, -1)
    sa, ca, ga, sb, cb, gb = (mod[:, 0].clone(), mod[:, 1].clone(), mod[:, 2].clone(), mod[:, 3].clone(), mod[:, 4].clone(), mod[:, 5].clone())
    h = _layernorm(x, axis=-1) * (1 + ca) + sa
    x = x + ga * _A_ref(h, A_k_cache, A_v_cache, A_wq, A_wo)
    h = _layernorm(x, axis=-1) * (1 + cb) + sb
    x = x + gb * _B_ref(h, B_w_gate, B_w_up, B_w_down)
    return x
```

```cpp
#include <hip/hip_runtime.h>
#include <hip/hip_bf16.h>
#include <stdint.h>

typedef unsigned short u16;
__device__ __forceinline__ float bf2f(u16 b) { return __uint_as_float(((unsigned)b) << 16); }
__device__ __forceinline__ u16 f2bf(float f) { unsigned u = __float_as_uint(f); u += 0x7FFFu + ((u >> 16) & 1u); return (u16)(u >> 16); }
__device__ __forceinline__ float rbf(float f) { return bf2f(f2bf(f)); }

constexpr int D = 576, HQ = 32, HKV = 16, SK = 65536, DH = 128, W = 4096, F = 4608;

constexpr int WS_MOD = 0;
constexpr int WS_H = 4096;
constexpr int WS_Q = 5120;
constexpr int WS_O = 10240;
constexpr int WS_X2 = 15360;
constexpr int WS_H2 = 16384;
constexpr int WS_ACT = 17408;
constexpr int WS_S = 32768;

__global__ void k_mod(const u16* c, const u16* wmod, float* ws) {
  int col = blockIdx.x * 64 + threadIdx.x;
  if (col >= 6 * D) return;
  float acc = 0.f;
  for (int i = 0; i < D; ++i) {
    float cv = bf2f(c[i]);
    float s = rbf(cv / (1.f + __expf(-cv)));
    acc += s * bf2f(wmod[(size_t)i * (6 * D) + col]);
  }
  ws[WS_MOD + col] = rbf(acc);
}

__device__ void ln_mod(const float* x, const float* shift, const float* scale, float* out, float* red) {
  int t = threadIdx.x;
  float s = 0.f;
  for (int i = 0; i < D; ++i) s += x[i];
  float m = rbf(s / D);
  float mf = s / D;
  float v = 0.f;
  for (int i = 0; i < D; ++i) { float d = x[i] - mf; v += d * d; }
  v = rbf(v / D);
  float den = rbf(sqrtf(rbf(v + 1e-5f)));
  if (t < D) {
    float ln = rbf(rbf(x[t] - m) / den);
    out[t] = rbf(rbf(ln * rbf(1.f + scale[t])) + shift[t]);
  }
}

__global__ void k_ln1(const u16* x, float* ws) {
  __shared__ float xs[D];
  int t = threadIdx.x;
  if (t < D) xs[t] = bf2f(x[t]);
  __syncthreads();
  ln_mod(xs, ws + WS_MOD + 0 * D, ws + WS_MOD + 1 * D, ws + WS_H, nullptr);
}

__global__ void k_gemv(const float* in, int K, const u16* w, int N, float* out) {
  int col = blockIdx.x * 64 + threadIdx.x;
  if (col >= N) return;
  float acc = 0.f;
  for (int i = 0; i < K; ++i) acc += in[i] * bf2f(w[(size_t)i * N + col]);
  out[col] = rbf(acc);
}

__global__ void k_scores(const u16* kc, float* ws) {
  int h = blockIdx.y, key = blockIdx.x * 64 + threadIdx.x;
  const u16* kp = kc + ((size_t)(h / 2) * SK + (SK - W) + key) * DH;
  const float* q = ws + WS_Q + h * DH;
  float acc = 0.f;
  for (int d = 0; d < DH; ++d) acc += q[d] * bf2f(kp[d]);
  ws[WS_S + h * W + key] = rbf(rbf(acc) * 0.08838834764831845f);
}

__global__ void k_softmax_pv(const u16* vc, float* ws) {
  __shared__ float p[W];
  __shared__ float red[128];
  int h = blockIdx.x, t = threadIdx.x;
  const float* s = ws + WS_S + h * W;
  float m = -INFINITY;
  for (int i = t; i < W; i += 128) m = fmaxf(m, s[i]);
  red[t] = m; __syncthreads();
  for (int i = 0; i < 128; ++i) m = fmaxf(m, red[i]);
  __syncthreads();
  float l = 0.f;
  for (int i = t; i < W; i += 128) { float e = __expf(s[i] - m); p[i] = e; l += e; }
  red[t] = l; __syncthreads();
  l = 0.f;
  for (int i = 0; i < 128; ++i) l += red[i];
  __syncthreads();
  for (int i = t; i < W; i += 128) p[i] = rbf(p[i] / l);
  __syncthreads();
  const u16* vp = vc + ((size_t)(h / 2) * SK + (SK - W)) * DH;
  float acc = 0.f;
  for (int i = 0; i < W; ++i) acc += p[i] * bf2f(vp[(size_t)i * DH + t]);
  ws[WS_O + h * DH + t] = rbf(acc);
}

__global__ void k_res1(const u16* x, const u16* wo, float* ws) {
  __shared__ float xs[D];
  int t = threadIdx.x;
  float acc = 0.f;
  for (int i = 0; i < HQ * DH; ++i) acc += ws[WS_O + i] * bf2f(wo[(size_t)i * D + t]);
  float a = rbf(acc);
  float x2 = rbf(bf2f(x[t]) + rbf(ws[WS_MOD + 2 * D + t] * a));
  xs[t] = x2; ws[WS_X2 + t] = x2;
  __syncthreads();
  ln_mod(xs, ws + WS_MOD + 3 * D, ws + WS_MOD + 4 * D, ws + WS_H2, nullptr);
}

__global__ void k_gateup(const u16* wg, const u16* wu, float* ws) {
  int col = blockIdx.x * 64 + threadIdx.x;
  float g = 0.f, u = 0.f;
  for (int i = 0; i < D; ++i) { float h = ws[WS_H2 + i]; g += h * bf2f(wg[(size_t)i * F + col]); u += h * bf2f(wu[(size_t)i * F + col]); }
  g = rbf(g); u = rbf(u);
  float ge = rbf(0.5f * g * (1.f + tanhf(0.7978845608028654f * (g + 0.044715f * g * g * g))));
  ws[WS_ACT + col] = rbf(ge * u);
}

__global__ void k_down(const u16* wd, float* ws, u16* out) {
  int t = threadIdx.x;
  float acc = 0.f;
  for (int i = 0; i < F; ++i) acc += ws[WS_ACT + i] * bf2f(wd[(size_t)i * D + t]);
  float a = rbf(acc);
  out[t] = f2bf(ws[WS_X2 + t] + rbf(ws[WS_MOD + 5 * D + t] * a));
}

extern "C" void kernel_launch(void* const* d_in, const int* in_sizes, int n_in,
                              void* d_out, int out_size, void* d_ws, size_t ws_size,
                              hipStream_t stream) {
  const u16* x = (const u16*)d_in[0];
  const u16* kc = (const u16*)d_in[1];
  const u16* vc = (const u16*)d_in[2];
  const u16* wq = (const u16*)d_in[3];
  const u16* wo = (const u16*)d_in[4];
  const u16* wg = (const u16*)d_in[6];
  const u16* wu = (const u16*)d_in[7];
  const u16* wd = (const u16*)d_in[8];
  const u16* c = (const u16*)d_in[9];
  const u16* wmod = (const u16*)d_in[10];
  float* ws = (float*)d_ws;
  k_mod<<<54, 64, 0, stream>>>(c, wmod, ws);
  k_ln1<<<1, 576, 0, stream>>>(x, ws);
  k_gemv<<<64, 64, 0, stream>>>(ws + WS_H, D, wq, HQ * DH, ws + WS_Q);
  k_scores<<<dim3(W / 64, HQ), 64, 0, stream>>>(kc, ws);
  k_softmax_pv<<<HQ, 128, 0, stream>>>(vc, ws);
  k_res1<<<1, 576, 0, stream>>>(x, wo, ws);
  k_gateup<<<F / 64, 64, 0, stream>>>(wg, wu, ws);
  k_down<<<1, 576, 0, stream>>>(wd, ws, (u16*)d_out);
}
```

```cpp
#include <hip/hip_runtime.h>
#include <hip/hip_bf16.h>
#include <stdint.h>

typedef unsigned short u16;
__device__ __forceinline__ float bf2f(u16 b) { return __uint_as_float(((unsigned)b) << 16); }
__device__ __forceinline__ u16 f2bf(float f) { unsigned u = __float_as_uint(f); u += 0x7FFFu + ((u >> 16) & 1u); return (u16)(u >> 16); }
__device__ __forceinline__ float rbf(float f) { return bf2f(f2bf(f)); }

constexpr int D = 576, HQ = 32, HKV = 16, SK = 65536, DH = 128, W = 4096, F = 4608;

constexpr int WS_MOD = 0;
constexpr int WS_H = 4096;
constexpr int WS_Q = 5120;
constexpr int WS_O = 10240;
constexpr int WS_X2 = 15360;
constexpr int WS_H2 = 16384;
constexpr int WS_ACT = 17408;
constexpr int WS_S = 32768;

__global__ void k_mod(const u16* c, const u16* wmod, float* ws) {
  int col = blockIdx.x * 64 + threadIdx.x;
  if (col >= 6 * D) return;
  float acc = 0.f;
  for (int i = 0; i < D; ++i) {
    float cv = bf2f(c[i]);
    float s = rbf(cv / (1.f + __expf(-cv)));
    acc += s * bf2f(wmod[(size_t)i * (6 * D) + col]);
  }
  ws[WS_MOD + col] = rbf(acc);
}

__device__ void ln_mod(const float* x, const float* shift, const float* scale, float* out, float* red) {
  int t = threadIdx.x;
  float s = 0.f;
  for (int i = 0; i < D; ++i) s += x[i];
  float m = rbf(s / D);
  float mf = s / D;
  float v = 0.f;
  for (int i = 0; i < D; ++i) { float d = x[i] - mf; v += d * d; }
  v = rbf(v / D);
  float den = rbf(sqrtf(rbf(v + 1e-5f)));
  if (t < D) {
    float ln = rbf(rbf(x[t] - m) / den);
    out[t] = rbf(rbf(ln * rbf(1.f + scale[t])) + shift[t]);
  }
}

__global__ void k_ln1(const u16* x, float* ws) {
  __shared__ float xs[D];
  int t = threadIdx.x;
  if (t < D) xs[t] = bf2f(x[t]);
  __syncthreads();
  ln_mod(xs, ws + WS_MOD + 0 * D, ws + WS_MOD + 1 * D, ws + WS_H, nullptr);
}

__global__ void k_gemv(const float* in, int K, const u16* w, int N, float* out) {
  int col = blockIdx.x * 64 + threadIdx.x;
  if (col >= N) return;
  float acc = 0.f;
  for (int i = 0; i < K; ++i) acc += in[i] * bf2f(w[(size_t)i * N + col]);
  out[col] = rbf(acc);
}

__global__ void k_scores(const u16* kc, float* ws) {
  int h = blockIdx.y, key = blockIdx.x * 64 + threadIdx.x;
  const u16* kp = kc + ((size_t)(h / 2) * SK + (SK - W) + key) * DH;
  const float* q = ws + WS_Q + h * DH;
  float acc = 0.f;
  for (int d = 0; d < DH; ++d) acc += q[d] * bf2f(kp[d]);
  ws[WS_S + h * W + key] = rbf(rbf(acc) * 0.08838834764831845f);
}

__global__ void k_softmax_pv(const u16* vc, float* ws) {
  __shared__ float p[W];
  __shared__ float red[128];
  int h = blockIdx.x, t = threadIdx.x;
  const float* s = ws + WS_S + h * W;
  float m = -INFINITY;
  for (int i = t; i < W; i += 128) m = fmaxf(m, s[i]);
  red[t] = m; __syncthreads();
  for (int i = 0; i < 128; ++i) m = fmaxf(m, red[i]);
  __syncthreads();
  float l = 0.f;
  for (int i = t; i < W; i += 128) { float e = __expf(s[i] - m); p[i] = e; l += e; }
  red[t] = l; __syncthreads();
  l = 0.f;
  for (int i = 0; i < 128; ++i) l += red[i];
  __syncthreads();
  for (int i = t; i < W; i += 128) p[i] = rbf(p[i] / l);
  __syncthreads();
  const u16* vp = vc + ((size_t)(h / 2) * SK + (SK - W)) * DH;
  float acc = 0.f;
  for (int i = 0; i < W; ++i) acc += p[i] * bf2f(vp[(size_t)i * DH + t]);
  ws[WS_O + h * DH + t] = rbf(acc);
}

__global__ void k_res1(const u16* x, const u16* wo, float* ws) {
  __shared__ float xs[D];
  int t = threadIdx.x;
  float acc = 0.f;
  for (int i = 0; i < HQ * DH; ++i) acc += ws[WS_O + i] * bf2f(wo[(size_t)i * D + t]);
  float a = rbf(acc);
  float x2 = rbf(bf2f(x[t]) + rbf(ws[WS_MOD + 2 * D + t] * a));
  xs[t] = x2; ws[WS_X2 + t] = x2;
  __syncthreads();
  ln_mod(xs, ws + WS_MOD + 3 * D, ws + WS_MOD + 4 * D, ws + WS_H2, nullptr);
}

__global__ void k_gateup(const u16* wg, const u16* wu, float* ws) {
  int col = blockIdx.x * 64 + threadIdx.x;
  float g = 0.f, u = 0.f;
  for (int i = 0; i < D; ++i) { float h = ws[WS_H2 + i]; g += h * bf2f(wg[(size_t)i * F + col]); u += h * bf2f(wu[(size_t)i * F + col]); }
  g = rbf(g); u = rbf(u);
  float ge = rbf(0.5f * g * (1.f + tanhf(0.7978845608028654f * (g + 0.044715f * g * g * g))));
  ws[WS_ACT + col] = rbf(ge * u);
}

__global__ void k_down(const u16* wd, float* ws, u16* out) {
  int t = threadIdx.x;
  float acc = 0.f;
  for (int i = 0; i < F; ++i) acc += ws[WS_ACT + i] * bf2f(wd[(size_t)i * D + t]);
  float a = rbf(acc);
  out[t] = f2bf(ws[WS_X2 + t] + rbf(ws[WS_MOD + 5 * D + t] * a));
}


#define R4(x) x x x x
#define R16(x) R4(R4(x))
#define R64(x) R4(R16(x))
#define R256(x) R16(R16(x))

__global__ void __launch_bounds__(64) k_extra(float* ws) {
  float a = threadIdx.x;
  _Pragma("unroll 1") for (int i = 0; i < 64; ++i) { asm volatile(R64("v_add_f32_e64 %0, %0, 1.0\n\t") : "+v"(a)); }
  if (a == 1234.5f) ws[1 << 20] = a;
}

extern "C" void kernel_launch(void* const* d_in, const int* in_sizes, int n_in,
                              void* d_out, int out_size, void* d_ws, size_t ws_size,
                              hipStream_t stream) {
  const u16* x = (const u16*)d_in[0];
  const u16* kc = (const u16*)d_in[1];
  const u16* vc = (const u16*)d_in[2];
  const u16* wq = (const u16*)d_in[3];
  const u16* wo = (const u16*)d_in[4];
  const u16* wg = (const u16*)d_in[6];
  const u16* wu = (const u16*)d_in[7];
  const u16* wd = (const u16*)d_in[8];
  const u16* c = (const u16*)d_in[9];
  const u16* wmod = (const u16*)d_in[10];
  float* ws = (float*)d_ws;
  k_mod<<<54, 64, 0, stream>>>(c, wmod, ws);
  k_ln1<<<1, 576, 0, stream>>>(x, ws);
  k_gemv<<<64, 64, 0, stream>>>(ws + WS_H, D, wq, HQ * DH, ws + WS_Q);
  k_scores<<<dim3(W / 64, HQ), 64, 0, stream>>>(kc, ws);
  k_softmax_pv<<<HQ, 128, 0, stream>>>(vc, ws);
  k_res1<<<1, 576, 0, stream>>>(x, wo, ws);
  k_gateup<<<F / 64, 64, 0, stream>>>(wg, wu, ws);
  k_down<<<1, 576, 0, stream>>>(wd, ws, (u16*)d_out);
  k_extra<<<256, 64, 0, stream>>>(ws);
}
```

```cpp
#include <hip/hip_runtime.h>
#include <stdint.h>

typedef unsigned short u16;
typedef unsigned long long u64;
typedef unsigned v4u __attribute__((ext_vector_type(4)));
typedef float f32x4 __attribute__((ext_vector_type(4)));
typedef short bf16x8 __attribute__((ext_vector_type(8)));
#define GAS __attribute__((address_space(1)))
#define LAS __attribute__((address_space(3)))
#define DEV __device__ __forceinline__
typedef GAS u64 gu64;
#define RLX_AGENT __ATOMIC_RELAXED, __HIP_MEMORY_SCOPE_AGENT

constexpr int D = 576, SK = 65536, WIN = 4096, DH = 128, FF = 4608, NMOD = 6 * D;
constexpr int NSPLIT = 11;
constexpr int NTHR = 576;
constexpr int SPIN_CAP = 1 << 16;

constexpr int G_EPOCH = 0;
constexpr int G_MODA = 64;
constexpr int G_MODB = G_MODA + 4 * 1152;
constexpr int G_Q = G_MODB + 2304;
constexpr int G_O = G_Q + 16 * 4 * 256;
constexpr int G_WO = G_O + 16 * 8 * NSPLIT * 64;
constexpr int G_X2 = G_WO + 128 * 576;
constexpr int G_DN = G_X2 + 72 * 16;
constexpr int G_END = G_DN + 72 * 576;
constexpr size_t G_STAMP = 1 << 20;

constexpr int L_K = 0;
constexpr int L_OL = 98304;
constexpr int L_Q = L_OL + 9216;
constexpr int L_MX = L_Q + 512;
constexpr int L_LS = L_MX + 64;
constexpr int L_OB = L_LS + 64;
constexpr int L_PF = L_OB + 128;
constexpr int L_END = L_PF + 256;
constexpr int F_RED = 0;
constexpr int F_H = 16384;
constexpr int F_H2 = 17408;
constexpr int F_ACT = 20480;
constexpr int F_TR = 20992;

struct Params {
  const u16 *x, *kc, *vc, *wq, *wo, *wg, *wu, *wd, *c, *wmod;
  u16* out;
  u64* ws;
};

#define STAMP(i) do {} while (0)
#define STAMP_INIT() do {} while (0)
#define STAMP_DUMP() do {} while (0)
#define L_EXTRA 0

DEV float bf2f(unsigned b) { return __uint_as_float(b << 16); }
DEV float rbf(float f) {
  unsigned u = __float_as_uint(f);
  u += 0x7FFFu + ((u >> 16) & 1u);
  return __uint_as_float(u & 0xFFFF0000u);
}
DEV unsigned pk2(float f) {
  unsigned u = __float_as_uint(f);
  return (u & 0xFFFF0000u) | (u >> 16);
}
struct Tag { unsigned hi, lo, rest; };
DEV u64 enc(float v, Tag t) {
  unsigned u = (__float_as_uint(v) + 0x80u) & 0xFFFFFF00u;
  return ((u64)t.hi << 32) | (u64)(u | t.lo);
}
DEV bool gok(u64 g, Tag t) { return ((unsigned)(g >> 32) == t.hi) & (((unsigned)g & 0xFFu) == t.lo); }
DEV float gval(u64 g) { return __uint_as_float((unsigned)g & 0xFFFFFF00u); }
DEV void pub(u64* ws, int idx, float v, Tag t) { __hip_atomic_store((gu64*)ws + idx, enc(v, t), RLX_AGENT); }
DEV u64 peek(u64* ws, int idx) { return __hip_atomic_load((gu64*)ws + idx, RLX_AGENT); }

DEV f32x4 mfma(v4u a, v4u b, f32x4 c) {
  return __builtin_amdgcn_mfma_f32_16x16x32_bf16(__builtin_bit_cast(bf16x8, a), __builtin_bit_cast(bf16x8, b), c, 0, 0, 0);
}
DEV v4u ldnt(const u16* p) { return __builtin_nontemporal_load((const GAS v4u*)p); }
DEV void bar() {
  asm volatile("s_waitcnt lgkmcnt(0)" ::: "memory");
  __builtin_amdgcn_s_barrier();
  asm volatile("" ::: "memory");
}
template <int CTRL> DEV float dpp(float x) {
  return __builtin_bit_cast(float, __builtin_amdgcn_mov_dpp(__builtin_bit_cast(int, x), CTRL, 0xf, 0xf, true));
}
constexpr int ROR8 = 0x128, ROR4 = 0x124, ROR2 = 0x122, ROR1 = 0x121;
DEV float x16_sum(float x) {
  auto s = __builtin_amdgcn_permlane16_swap(__float_as_uint(x), __float_as_uint(x), false, false);
  x = __uint_as_float(s[0]) + __uint_as_float(s[1]);
  auto t = __builtin_amdgcn_permlane32_swap(__float_as_uint(x), __float_as_uint(x), false, false);
  return __uint_as_float(t[0]) + __uint_as_float(t[1]);
}
DEV float x16_max(float x) {
  auto s = __builtin_amdgcn_permlane16_swap(__float_as_uint(x), __float_as_uint(x), false, false);
  x = fmaxf(__uint_as_float(s[0]), __uint_as_float(s[1]));
  auto t = __builtin_amdgcn_permlane32_swap(__float_as_uint(x), __float_as_uint(x), false, false);
  return fmaxf(__uint_as_float(t[0]), __uint_as_float(t[1]));
}
DEV float wave_sum(float x) {
  x += dpp<ROR8>(x); x += dpp<ROR4>(x); x += dpp<ROR2>(x); x += dpp<ROR1>(x);
  return x16_sum(x);
}
DEV float exp_f(float x) { return __builtin_amdgcn_exp2f(x * 1.4426950408889634f); }
DEV float rcp_f(float x) { return __builtin_amdgcn_rcpf(x); }

struct Mask { unsigned m[4]; };
DEV Mask make_mask(int lane, bool on) {
  Mask k;
  const unsigned h = on ? ((lane & 1) ? 0xFFFF0000u : 0x0000FFFFu) : 0u;
#pragma unroll
  for (int r = 0; r < 4; ++r) k.m[r] = (((lane & 7) >> 1) == r) ? h : 0u;
  return k;
}
DEV v4u afrag(unsigned pp, const Mask& k) { return v4u{pp & k.m[0], pp & k.m[1], pp & k.m[2], pp & k.m[3]}; }

DEV void ln_stats(const float (&xv)[9], float& mean_b, float& rden) {
  float s = 0.f;
#pragma unroll
  for (int k = 0; k < 9; ++k) s += xv[k];
  s = wave_sum(s);
  const float mf = s * (1.f / D);
  float v = 0.f;
#pragma unroll
  for (int k = 0; k < 9; ++k) { const float d = xv[k] - mf; v += d * d; }
  v = wave_sum(v);
  mean_b = rbf(mf);
  rden = rbf(__builtin_amdgcn_sqrtf(rbf(rbf(v * (1.f / D)) + 1e-5f)));
}
DEV float ln_apply(float x, float mean_b, float den, float scale, float shift) {
  const float ln = rbf(rbf(x - mean_b) / den);
  return rbf(rbf(ln * rbf(1.f + scale)) + shift);
}

DEV void red_store(LAS float* area, f32x4 d, int lane) {
  const int u = (lane >> 3) & 1;
  if ((lane >> 5) == u) *(LAS f32x4*)(area + u * 64 + 8 * (lane & 7) + 4 * ((lane >> 4) & 1)) = d;
}

template <int N> struct GV { float v[N]; };

DEV void role_att(const Params& p, LAS unsigned char* L, Tag tag, int x, int i, int wave, int lane) {
  const int g = 2 * x + (i >= NSPLIT ? 1 : 0), j = i >= NSPLIT ? i - NSPLIT : i;
  const int ntile = j < 8 ? 23 : 24, tile0 = j < 8 ? 23 * j : 184 + 24 * (j - 8);
  const bool owner = j < 8;
  int redc = -1;
  if (j >= 8) redc = 9 * x + (g & 1) * 3 + (j - 8);
  else if (j == 0) redc = 9 * x + 6 + (g & 1);
  else if (j == 1 && (g & 1) == 0) redc = 9 * x + 8;
  u64* ws = p.ws;
  const Mask mk = make_mask(lane, true);
  const int kap = lane >> 4, cc = lane & 15;

  if (wave < 8) {
    const size_t hb = ((size_t)g * SK + (SK - WIN)) * DH;
    const int nst = (wave + 16 < ntile) ? 3 : 2;
    const u16* kb = p.kc + hb + (size_t)(tile0 + wave) * 16 * DH;
    const unsigned ldsk = (unsigned)(uintptr_t)(L + L_K) + wave * 3 * 4096;
#pragma unroll
    for (int s = 0; s < 3; ++s) {
      if (s < nst) {
#pragma unroll
        for (int q = 0; q < 4; ++q) {
          const int key = 4 * q + kap;
          const u16* src = kb + (size_t)(s * 8 * 16 + key) * DH + 8 * (cc ^ key);
          unsigned keep;
          asm volatile("s_mov_b32 %0, m0\n\ts_mov_b32 m0, %2\n\ts_nop 0\n\tglobal_load_lds_dwordx4 %1, off nt\n\ts_mov_b32 m0, %0"
                       : "=&s"(keep) : "v"((const GAS u16*)src), "s"(ldsk + s * 4096 + q * 1024) : "memory");
        }
      }
    }
    v4u vr[12];
    const u16* vb = p.vc + hb + (size_t)(tile0 + wave) * 16 * DH + (size_t)(4 * kap) * DH + 8 * cc;
#pragma unroll
    for (int s = 0; s < 3; ++s)
#pragma unroll
      for (int q = 0; q < 4; ++q) {
        if (s < 2 || nst == 3) vr[s * 4 + q] = ldnt(vb + (size_t)(s * 8 * 16 + q) * DH);
        else vr[s * 4 + q] = v4u{0u, 0u, 0u, 0u};
      }
    __builtin_amdgcn_sched_barrier(0);
    STAMP(1);
    bar();
    STAMP(2);
    if (nst == 3) asm volatile("s_waitcnt vmcnt(12)" ::: "memory");
    else asm volatile("s_waitcnt vmcnt(8)" ::: "memory");
    STAMP(3);
    v4u qf[4];
    const int head = (lane >> 3) & 1;
#pragma unroll
    for (int ks = 0; ks < 4; ++ks) qf[ks] = *(const LAS v4u*)(L + L_Q + head * 256 + ks * 64 + kap * 16);
    float sc[12];
    float mx = -INFINITY;
#pragma unroll
    for (int s = 0; s < 3; ++s) {
      f32x4 a = {0.f, 0.f, 0.f, 0.f};
      if (s < 2 || nst == 3) {
#pragma unroll
        for (int ks = 0; ks < 4; ++ks) {
          const v4u kf = *(const LAS v4u*)(L + L_K + (wave * 3 + s) * 4096 + cc * 256 + (((4 * ks + kap) ^ cc) << 4));
          a = mfma(kf, qf[ks], a);
        }
#pragma unroll
        for (int r = 0; r < 4; ++r) { sc[s * 4 + r] = rbf(rbf(a[r]) * 0.08838834764831845f); mx = fmaxf(mx, sc[s * 4 + r]); }
      } else {
#pragma unroll
        for (int r = 0; r < 4; ++r) sc[s * 4 + r] = -INFINITY;
      }
    }
    mx = x16_max(mx);
    if ((lane & 0x37) == 0) ((LAS float*)(L + L_MX))[head * 8 + wave] = mx;
    bar();
    float m;
    {
      const f32x4 a = *(const LAS f32x4*)(L + L_MX + head * 32), b = *(const LAS f32x4*)(L + L_MX + head * 32 + 16);
      m = fmaxf(fmaxf(fmaxf(a[0], a[1]), fmaxf(a[2], a[3])), fmaxf(fmaxf(b[0], b[1]), fmaxf(b[2], b[3])));
    }
    unsigned pp[12];
    float ls = 0.f;
#pragma unroll
    for (int t = 0; t < 12; ++t) { const float e = exp_f(sc[t] - m); ls += e; pp[t] = pk2(rbf(e)); }
    ls = x16_sum(ls);
    if ((lane & 0x37) == 0) ((LAS float*)(L + L_LS))[head * 8 + wave] = ls;
    v4u wr[4];
    if (owner) {
      const u16* wb = p.wo + (size_t)(g * 256 + j * 32 + (lane >> 3)) * D + wave * 64 + 8 * (lane & 7);
#pragma unroll
      for (int t = 0; t < 4; ++t) wr[t] = ldnt(wb + (size_t)t * 8 * D);
    }
    __builtin_amdgcn_sched_barrier(0);
    STAMP(4);
    f32x4 o0 = {0.f, 0.f, 0.f, 0.f}, o1 = {0.f, 0.f, 0.f, 0.f};
#pragma unroll
    for (int t = 0; t < 12; t += 2) { o0 = mfma(afrag(pp[t], mk), vr[t], o0); o1 = mfma(afrag(pp[t + 1], mk), vr[t + 1], o1); }
    *(LAS f32x4*)(L + L_OL + wave * 1024 + lane * 16) = o0 + o1;
    STAMP(5);
    bar();
    if (wave < 4) {
      const int e = wave * 64 + lane, hh = e >> 7, dd = e & 127, mm = hh * 8 + (dd & 7);
      const LAS float* src = (const LAS float*)(L + L_OL) + (16 * (mm >> 2) + (dd >> 3)) * 4 + (mm & 3);
      float s = 0.f;
#pragma unroll
      for (int w = 0; w < 8; ++w) s += src[w * 256];
      pub(ws, G_O + ((g * 8 + (e >> 5)) * NSPLIT + j) * 64 + (e & 31), s, tag);
    } else if (wave == 4 && lane < 16) {
      const int u = lane >> 1, hh = u >> 2;
      float v;
      if (lane & 1) { v = 0.f;
#pragma unroll
        for (int w = 0; w < 8; ++w) v += ((const LAS float*)(L + L_LS))[hh * 8 + w];
      } else { v = -INFINITY;
#pragma unroll
        for (int w = 0; w < 8; ++w) v = fmaxf(v, ((const LAS float*)(L + L_MX))[hh * 8 + w]);
      }
      pub(ws, G_O + ((g * 8 + u) * NSPLIT + j) * 64 + 32 + (lane & 1), v, tag);
    }
    STAMP(6);
    if (owner) {
      bar();
      STAMP(7);
      const v4u ob = *(const LAS v4u*)(L + L_OB + (lane >> 3) * 16);
      f32x4 d = {0.f, 0.f, 0.f, 0.f};
#pragma unroll
      for (int t = 0; t < 4; ++t) d = mfma(afrag(ob[t], mk), wr[t], d);
      LAS float* tr = (LAS float*)(L + L_OL + wave * 512);
      red_store(tr, d, lane);
      asm volatile("s_waitcnt lgkmcnt(0)" ::: "memory");
      pub(ws, G_WO + (g * 8 + j) * 576 + wave * 64 + lane, tr[lane] + tr[64 + lane], tag);
      STAMP(8);
    }
  } else {
    {
      u64 gq[16];
      for (int spin = 0; spin < SPIN_CAP; ++spin) {
        bool ok = true;
#pragma unroll
        for (int r = 0; r < 4; ++r)
#pragma unroll
          for (int k = 0; k < 4; ++k) gq[r * 4 + k] = peek(ws, G_Q + (g * 4 + r) * 256 + k * 64 + lane);
#pragma unroll
        for (int k = 0; k < 16; ++k) ok &= gok(gq[k], tag);
        if (__all(ok)) break;
      }
#pragma unroll
      for (int k = 0; k < 4; ++k) {
        const float q = rbf((gval(gq[k]) + gval(gq[4 + k])) + (gval(gq[8 + k]) + gval(gq[12 + k])));
        ((LAS u16*)(L + L_Q))[k * 64 + lane] = (u16)(__float_as_uint(q) >> 16);
      }
    }
    STAMP(1);
    v4u wr[4];
    float xr = 0.f;
    if (redc >= 0) xr = bf2f(p.x[redc * 8 + (lane & 7)]);
    bar();
    bar();
    if (owner) {
      const u16* wb = p.wo + (size_t)(g * 256 + j * 32 + (lane >> 3)) * D + 8 * 64 + 8 * (lane & 7);
#pragma unroll
      for (int t = 0; t < 4; ++t) wr[t] = ldnt(wb + (size_t)t * 8 * D);
    }
    bar();
    STAMP(2);
    if (owner) {
      u64 go[NSPLIT];
      const int li = lane < 34 ? lane : 33;
      for (int spin = 0; spin < SPIN_CAP; ++spin) {
        bool ok = true;
#pragma unroll
        for (int k = 0; k < NSPLIT; ++k) go[k] = peek(ws, G_O + ((g * 8 + j) * NSPLIT + k) * 64 + li);
#pragma unroll
        for (int k = 0; k < NSPLIT; ++k) ok &= gok(go[k], tag);
        if (__all(ok)) break;
      }
      STAMP(3);
      float mk_[NSPLIT], M = -INFINITY;
#pragma unroll
      for (int k = 0; k < NSPLIT; ++k) { mk_[k] = __uint_as_float(__builtin_amdgcn_readlane((unsigned)go[k] & 0xFFFFFF00u, 32)); M = fmaxf(M, mk_[k]); }
      float Ls = 0.f, o = 0.f;
#pragma unroll
      for (int k = 0; k < NSPLIT; ++k) {
        const float w = exp_f(mk_[k] - M);
        Ls += w * __uint_as_float(__builtin_amdgcn_readlane((unsigned)go[k] & 0xFFFFFF00u, 33));
        o += w * gval(go[k]);
      }
      o = rbf(o / Ls);
      if (lane < 32) ((LAS unsigned*)(L + L_OB))[(lane & 7) * 4 + (lane >> 3)] = pk2(o);
      bar();
      const v4u ob = *(const LAS v4u*)(L + L_OB + (lane >> 3) * 16);
      f32x4 d = {0.f, 0.f, 0.f, 0.f};
#pragma unroll
      for (int t = 0; t < 4; ++t) d = mfma(afrag(ob[t], mk), wr[t], d);
      LAS float* tr = (LAS float*)(L + L_OL + 8 * 512);
      red_store(tr, d, lane);
      asm volatile("s_waitcnt lgkmcnt(0)" ::: "memory");
      pub(ws, G_WO + (g * 8 + j) * 576 + 8 * 64 + lane, tr[lane] + tr[64 + lane], tag);
      STAMP(4);
    }
    if (redc >= 0) {
      u64 gg0, gg1;
      for (int spin = 0; spin < SPIN_CAP; ++spin) {
        gg0 = peek(ws, G_MODB + redc * 8 + (lane & 7));
        gg1 = peek(ws, G_MODB + 3 * D + redc * 8 + (lane & 7));
        if (__all(gok(gg0, tag) & gok(gg1, tag))) break;
      }
      float x2;
      {
        u64 gw[16];
        for (int spin = 0; spin < SPIN_CAP; ++spin) {
          bool ok = true;
#pragma unroll
          for (int s = 0; s < 16; ++s) gw[s] = peek(ws, G_WO + (s * 8 + (lane >> 3)) * 576 + redc * 8 + (lane & 7));
#pragma unroll
          for (int s = 0; s < 16; ++s) ok &= gok(gw[s], tag);
          if (__all(ok)) break;
        }
        STAMP(5);
        float a = 0.f;
#pragma unroll
        for (int s = 0; s < 16; ++s) a += gval(gw[s]);
        a += dpp<ROR8>(a);
        a = x16_sum(a);
        x2 = rbf(xr + rbf(gval(gg0) * rbf(a)));
        if (lane < 8) pub(ws, G_X2 + redc * 16 + lane, x2, tag);
      }
      STAMP(6);
      {
        u64 gd[9];
        for (int spin = 0; spin < SPIN_CAP; ++spin) {
          bool ok = true;
#pragma unroll
          for (int s = 0; s < 9; ++s) gd[s] = peek(ws, G_DN + (s * 8 + (lane >> 3)) * 576 + redc * 8 + (lane & 7));
#pragma unroll
          for (int s = 0; s < 9; ++s) ok &= gok(gd[s], tag);
          if (__all(ok)) break;
        }
        STAMP(7);
        float a = 0.f;
#pragma unroll
        for (int s = 0; s < 9; ++s) a += gval(gd[s]);
        a += dpp<ROR8>(a);
        a = x16_sum(a);
        const float o = rbf(x2 + rbf(gval(gg1) * rbf(a)));
        if (lane < 8) p.out[redc * 8 + lane] = (u16)(__float_as_uint(o) >> 16);
        if (redc == 0 && lane == 0) __hip_atomic_store((gu64*)ws + G_EPOCH, ((u64)tag.hi << 32) | (u64)tag.rest, RLX_AGENT);
      }
      STAMP(8);
    }
  }
}

DEV void mod_item_load(const Params& p, int col0, int wave, int lane, v4u (&wr)[8]) {
  const u16* wb = p.wmod + (size_t)(wave * 64 + (lane >> 3)) * NMOD + col0 + 8 * (lane & 7);
#pragma unroll
  for (int t = 0; t < 8; ++t) wr[t] = ldnt(wb + (size_t)t * 8 * NMOD);
}
DEV unsigned silu_pk(unsigned craw) {
  const float v = bf2f(craw);
  return pk2(rbf(v / (1.f + exp_f(-v))));
}
DEV void mod_item_mma(LAS float* red, const unsigned (&sp)[8], const v4u (&wr)[8], const Mask& mk, int wave, int lane) {
  f32x4 d = {0.f, 0.f, 0.f, 0.f};
#pragma unroll
  for (int t = 0; t < 8; ++t) d = mfma(afrag(sp[t], mk), wr[t], d);
  red_store(red + wave * 128, d, lane);
}
DEV void mod_item_pub(u64* ws, const LAS float* red, int item, int lane, Tag tag) {
  float s = 0.f;
#pragma unroll
  for (int w = 0; w < 18; ++w) s += red[w * 64 + lane];
  pub(ws, G_MODB + item * 64 + lane, rbf(s), tag);
}

DEV void role_ffn(const Params& p, LAS unsigned char* L, Tag tag, int x, int i, int wave, int lane) {
  const int f = 9 * x + i;
  u64* ws = p.ws;
  const Mask mk = make_mask(lane, true);
  LAS float* red = (LAS float*)(L + F_RED);
  const int mcc = f >> 2, mrr = f & 3;
  const int mrow = mrr * 144 + wave * 16 + (lane >> 3);
  const unsigned c0 = p.c[mrow], c1 = p.c[mrow + 8];
  const v4u m0 = ldnt(p.wmod + (size_t)mrow * NMOD + mcc * 64 + 8 * (lane & 7));
  const v4u m1 = ldnt(p.wmod + (size_t)(mrow + 8) * NMOD + mcc * 64 + 8 * (lane & 7));
  v4u wa[8], wb2[8];
  unsigned sp[8];
  const int qg = 2 * x + (i >> 2), qr = i & 3;
  if (i < 8) {
    const u16* wb = p.wq + (size_t)(qr * 144 + wave * 16 + (lane >> 5)) * 4096 + qg * 256 + 8 * (lane & 31);
#pragma unroll
    for (int t = 0; t < 8; ++t) wa[t] = ldnt(wb + (size_t)t * 2 * 4096);
  } else {
#pragma unroll
    for (int t = 0; t < 8; ++t) sp[t] = p.c[wave * 64 + t * 8 + (lane >> 3)];
    mod_item_load(p, 2 * D + x * 64, wave, lane, wa);
    mod_item_load(p, 2 * D + (27 + x) * 64, wave, lane, wb2);
  }
  __builtin_amdgcn_sched_barrier(0);
  STAMP(1);
  {
    f32x4 d = {0.f, 0.f, 0.f, 0.f};
    d = mfma(afrag(silu_pk(c0), mk), m0, d);
    d = mfma(afrag(silu_pk(c1), mk), m1, d);
    red_store(red + wave * 128, d, lane);
  }
  bar();
  if (wave == 0) {
    float s = 0.f;
#pragma unroll
    for (int w = 0; w < 18; ++w) s += red[w * 64 + lane];
    pub(ws, G_MODA + mrr * 1152 + mcc * 64 + lane, s, tag);
  }
  STAMP(2);
  if (i < 8) {
    if (wave == 0) {
      float xv[9];
#pragma unroll
      for (int k = 0; k < 9; ++k) xv[k] = bf2f(p.x[lane + 64 * k]);
      float xo[3];
#pragma unroll
      for (int k = 0; k < 3; ++k) { const int r = lane + 64 * k; xo[k] = bf2f(p.x[qr * 144 + (r < 144 ? r : 143)]); }
      float mean, den;
      ln_stats(xv, mean, den);
      u64 ga[24];
      for (int spin = 0; spin < SPIN_CAP; ++spin) {
        bool ok = true;
#pragma unroll
        for (int k = 0; k < 3; ++k) {
          const int r0 = lane + 64 * k, r = qr * 144 + (r0 < 144 ? r0 : 143);
#pragma unroll
          for (int rr = 0; rr < 4; ++rr) {
            ga[k * 8 + rr] = peek(ws, G_MODA + rr * 1152 + r);
            ga[k * 8 + 4 + rr] = peek(ws, G_MODA + rr * 1152 + D + r);
          }
        }
#pragma unroll
        for (int k = 0; k < 24; ++k) ok &= gok(ga[k], tag);
        if (__all(ok)) break;
      }
      STAMP(3);
#pragma unroll
      for (int k = 0; k < 3; ++k) {
        const int r = lane + 64 * k;
        const float sa = rbf((gval(ga[k * 8]) + gval(ga[k * 8 + 1])) + (gval(ga[k * 8 + 2]) + gval(ga[k * 8 + 3])));
        const float ca = rbf((gval(ga[k * 8 + 4]) + gval(ga[k * 8 + 5])) + (gval(ga[k * 8 + 6]) + gval(ga[k * 8 + 7])));
        const float h = ln_apply(xo[k], mean, den, ca, sa);
        if (r < 144) ((LAS unsigned*)(L + F_H))[(r >> 4) * 16 + (r & 1) * 8 + ((r & 15) >> 1)] = pk2(h);
      }
    }
    bar();
    {
      const Mask mq = make_mask(lane, ((lane >> 4) & 1) == ((lane >> 3) & 1));
      const LAS v4u* hp = (const LAS v4u*)(L + F_H + wave * 64 + (lane >> 5) * 32);
      const v4u h0 = hp[0], h1 = hp[1];
      f32x4 d0 = {0.f, 0.f, 0.f, 0.f}, d1 = {0.f, 0.f, 0.f, 0.f};
#pragma unroll
      for (int t = 0; t < 4; ++t) { d0 = mfma(afrag(h0[t], mq), wa[t], d0); d1 = mfma(afrag(h1[t], mq), wa[4 + t], d1); }
      *(LAS f32x4*)(L + F_RED + 4608 + wave * 1024 + lane * 16) = d0 + d1;
    }
    bar();
    if (wave < 4) {
      const int e = wave * 64 + lane, hh = e >> 7, dd = e & 127, mm = hh * 8 + (dd & 7);
      const LAS float* src = (const LAS float*)(L + F_RED + 4608) + (16 * (mm >> 2) + (dd >> 3)) * 4 + (mm & 3);
      float s = 0.f;
#pragma unroll
      for (int w = 0; w < 9; ++w) s += src[w * 256];
      pub(ws, G_Q + (qg * 4 + qr) * 256 + e, s, tag);
    }
  } else {
    bar();
#pragma unroll
    for (int t = 0; t < 8; ++t) sp[t] = silu_pk(sp[t]);
    mod_item_mma(red, sp, wa, mk, wave, lane);
    mod_item_mma(red + 1152, sp, wb2, mk, wave, lane);
    bar();
    if (wave < 2) mod_item_pub(ws, red + wave * 1152, wave == 0 ? x : 27 + x, lane, tag);
  }
  STAMP(4);
  v4u wd[8];
  const u16* wdb = p.wd + (size_t)(f * 64 + (lane >> 3)) * D + wave * 64 + 8 * (lane & 7);
  if (wave < 8) {
    v4u wg[9], wu[9];
    const size_t o = (size_t)(wave * 72 + (lane >> 3)) * FF + f * 64 + 8 * (lane & 7);
#pragma unroll
    for (int t = 0; t < 9; ++t) { wg[t] = ldnt(p.wg + o + (size_t)t * 8 * FF); wu[t] = ldnt(p.wu + o + (size_t)t * 8 * FF); }
#pragma unroll
    for (int t = 0; t < 8; ++t) wd[t] = ldnt(wdb + (size_t)t * 8 * D);
    __builtin_amdgcn_sched_barrier(0);
    STAMP(5);
    bar();
    STAMP(8);
    const LAS v4u* hp = (const LAS v4u*)(L + F_H2 + (wave * 8 + (lane >> 3)) * 48);
    const v4u h0 = hp[0], h1 = hp[1];
    const unsigned h8 = ((const LAS unsigned*)hp)[8];
    f32x4 dg = {0.f, 0.f, 0.f, 0.f}, du = {0.f, 0.f, 0.f, 0.f};
#pragma unroll
    for (int t = 0; t < 9; ++t) {
      const v4u a = afrag(t < 4 ? h0[t & 3] : t < 8 ? h1[t & 3] : h8, mk);
      dg = mfma(a, wg[t], dg);
      du = mfma(a, wu[t], du);
    }
    red_store(red + wave * 256, dg, lane);
    red_store(red + wave * 256 + 128, du, lane);
    bar();
    if (wave == 0) {
      float g = 0.f, u = 0.f;
#pragma unroll
      for (int w = 0; w < 8; ++w) { g += red[w * 256 + lane] + red[w * 256 + 64 + lane]; u += red[w * 256 + 128 + lane] + red[w * 256 + 192 + lane]; }
      g = rbf(g); u = rbf(u);
      const float y = 0.7978845608028654f * (g + 0.044715f * g * g * g);
      const float th = 1.f - 2.f * rcp_f(exp_f(2.f * y) + 1.f);
      const float act = rbf(rbf(0.5f * g * (1.f + th)) * u);
      ((LAS unsigned*)(L + F_ACT))[(lane & 7) * 8 + (lane >> 3)] = pk2(act);
    }
    bar();
  } else {
#pragma unroll
    for (int t = 0; t < 8; ++t) wd[t] = ldnt(wdb + (size_t)t * 8 * D);
    __builtin_amdgcn_sched_barrier(0);
    STAMP(5);
    float sv[18];
    {
      u64 gs[18];
      for (int spin = 0; spin < SPIN_CAP; ++spin) {
        bool ok = true;
#pragma unroll
        for (int k = 0; k < 18; ++k) gs[k] = peek(ws, G_MODB + D + k * 64 + lane);
#pragma unroll
        for (int k = 0; k < 18; ++k) ok &= gok(gs[k], tag);
        if (__all(ok)) break;
      }
#pragma unroll
      for (int k = 0; k < 18; ++k) sv[k] = gval(gs[k]);
    }
    STAMP(6);
    u64 gx[9];
    for (int spin = 0; spin < SPIN_CAP; ++spin) {
      bool ok = true;
#pragma unroll
      for (int k = 0; k < 9; ++k) gx[k] = peek(ws, G_X2 + (k * 8 + (lane >> 3)) * 16 + (lane & 7));
#pragma unroll
      for (int k = 0; k < 9; ++k) ok &= gok(gx[k], tag);
      if (__all(ok)) break;
    }
    STAMP(7);
    float xv[9];
#pragma unroll
    for (int k = 0; k < 9; ++k) xv[k] = gval(gx[k]);
    float mean, den;
    ln_stats(xv, mean, den);
#pragma unroll
    for (int k = 0; k < 9; ++k) {
      const int e = lane + 64 * k, w = e / 72, rem = e - w * 72;
      const float h = ln_apply(xv[k], mean, den, sv[9 + k], sv[k]);
      ((LAS unsigned*)(L + F_H2))[(w * 8 + (rem & 7)) * 12 + (rem >> 3)] = pk2(h);
    }
    bar();
    STAMP(8);
    bar();
    bar();
  }
  STAMP(9);
  {
    const LAS v4u* ap = (const LAS v4u*)(L + F_ACT + (lane >> 3) * 32);
    const v4u a0 = ap[0], a1 = ap[1];
    f32x4 d0 = {0.f, 0.f, 0.f, 0.f}, d1 = {0.f, 0.f, 0.f, 0.f};
#pragma unroll
    for (int t = 0; t < 4; ++t) { d0 = mfma(afrag(a0[t], mk), wd[t], d0); d1 = mfma(afrag(a1[t], mk), wd[4 + t], d1); }
    LAS float* tr = (LAS float*)(L + F_TR + wave * 512);
    red_store(tr, d0 + d1, lane);
    asm volatile("s_waitcnt lgkmcnt(0)" ::: "memory");
    pub(ws, G_DN + f * 576 + wave * 64 + lane, tr[lane] + tr[64 + lane], tag);
  }
  STAMP(10);
}

DEV void role_modb(const Params& p, LAS unsigned char* L, Tag tag, int x, int wave, int lane) {
  const int n = x < 4 ? 3 : 2, l0 = x < 4 ? 3 * x : 12 + 2 * (x - 4);
  const int it0 = 8 + l0, it1 = l0 + 1 == 19 ? 35 : 9 + l0, it2 = 10 + l0;
  const Mask mk = make_mask(lane, true);
  LAS float* red = (LAS float*)(L + F_RED);
  v4u w0[8], w1[8], w2[8];
  unsigned sp[8];
#pragma unroll
  for (int t = 0; t < 8; ++t) sp[t] = p.c[wave * 64 + t * 8 + (lane >> 3)];
  mod_item_load(p, 2 * D + it0 * 64, wave, lane, w0);
  mod_item_load(p, 2 * D + it1 * 64, wave, lane, w1);
  if (n == 3) mod_item_load(p, 2 * D + it2 * 64, wave, lane, w2);
  __builtin_amdgcn_sched_barrier(0);
  STAMP(1);
#pragma unroll
  for (int t = 0; t < 8; ++t) sp[t] = silu_pk(sp[t]);
  mod_item_mma(red, sp, w0, mk, wave, lane);
  mod_item_mma(red + 1152, sp, w1, mk, wave, lane);
  if (n == 3) mod_item_mma(red + 2304, sp, w2, mk, wave, lane);
  bar();
  if (wave < n) mod_item_pub(p.ws, red + wave * 1152, wave == 0 ? it0 : wave == 1 ? it1 : it2, lane, tag);
  STAMP(2);
}

__global__ void __launch_bounds__(NTHR) adaln_block_kernel(Params p) {
  __shared__ __attribute__((aligned(16))) unsigned char smem[163840];
  LAS unsigned char* L = (LAS unsigned char*)smem;
  const int lane = threadIdx.x & 63, wave = __builtin_amdgcn_readfirstlane(threadIdx.x >> 6);
  STAMP_INIT();
  STAMP(0);
  const u64 st = *(const GAS u64*)(p.ws + G_EPOCH);
  const u64 nx = st * 6364136223846793005ull + 1442695040888963407ull;
  Tag tag;
  tag.hi = __builtin_amdgcn_readfirstlane((unsigned)(nx >> 32));
  tag.rest = __builtin_amdgcn_readfirstlane((unsigned)nx);
  tag.lo = tag.rest >> 24;
  const int x = blockIdx.x & 7, idx = blockIdx.x >> 3;
  if (idx < 22) role_att(p, L, tag, x, idx, wave, lane);
  else if (idx < 31) role_ffn(p, L, tag, x, idx - 22, wave, lane);
  else role_modb(p, L, tag, x, wave, lane);
  STAMP_DUMP();
}

extern "C" void kernel_launch(void* const* d_in, const int* in_sizes, int n_in, void* d_out, int out_size, void* d_ws,
                              size_t ws_size, hipStream_t stream) {
  Params p;
  p.x = (const u16*)d_in[0];
  p.kc = (const u16*)d_in[1];
  p.vc = (const u16*)d_in[2];
  p.wq = (const u16*)d_in[3];
  p.wo = (const u16*)d_in[4];
  p.wg = (const u16*)d_in[6];
  p.wu = (const u16*)d_in[7];
  p.wd = (const u16*)d_in[8];
  p.c = (const u16*)d_in[9];
  p.wmod = (const u16*)d_in[10];
  p.out = (u16*)d_out;
  p.ws = (u64*)d_ws;
  hipLaunchKernelGGL(adaln_block_kernel, dim3(256), dim3(NTHR), 0, stream, p);
}
```

```cpp
#include <hip/hip_runtime.h>
#include <stdint.h>

typedef unsigned short u16;
typedef unsigned long long u64;
typedef unsigned v4u __attribute__((ext_vector_type(4)));
typedef float f32x4 __attribute__((ext_vector_type(4)));
typedef float f32x2 __attribute__((ext_vector_type(2)));
typedef __bf16 bf16x2 __attribute__((ext_vector_type(2)));
typedef short bf16x8 __attribute__((ext_vector_type(8)));
typedef __amdgpu_buffer_rsrc_t rsrc_t;
#define GAS __attribute__((address_space(1)))
#define LAS __attribute__((address_space(3)))
#define DEV __device__ __forceinline__
typedef GAS u64 gu64;
#define RLX_AGENT __ATOMIC_RELAXED, __HIP_MEMORY_SCOPE_AGENT

constexpr int D = 576, SK = 65536, WIN = 4096, DH = 128, FF = 4608, NMOD = 6 * D;
constexpr int NSPLIT = 11;
constexpr int NTHR = 576;
constexpr int SPIN_CAP = 1 << 16;
#define GATE_FFN 600
#define GATE_MODB 0
#define GATE_ATT 120
#define GATE_Q 450
#define F_N1 10
#define F_D1 2
#define V_N1 0
#define V_D1 2
#define V_D2 4
#define ATT_N1 0
#define ATT_D1 1
#define ATT_D2 4

constexpr int G_EPOCH = 0;
constexpr int G_MOD = 64;
constexpr int G_Q = G_MOD + 4 * NMOD;
constexpr int G_O = G_Q + 32 * 2 * 128;
constexpr int G_WO = G_O + 16 * 8 * NSPLIT * 64;
constexpr int G_X2 = G_WO + 128 * 576;
constexpr int G_DN = G_X2 + 72 * 16;
constexpr int G_END = G_DN + 72 * 576;
constexpr size_t G_STAMP = 1 << 20;

constexpr int L_K = 0;
constexpr int L_OL = 98304;
constexpr int L_Q = L_OL + 9216;
constexpr int L_MX = L_Q + 512;
constexpr int L_LS = L_MX + 64;
constexpr int L_OB = L_LS + 64;
constexpr int L_PF = L_OB + 128;
constexpr int L_WO = L_PF + 256;
constexpr int L_MA = L_WO + 36864;
constexpr int L_TAG = L_MA + 1536 + 16;
constexpr int L_END = L_TAG + 16;
constexpr int F_RED = 0;
constexpr int F_H = 16384;
constexpr int F_H2 = 17664;
constexpr int F_ACT = 20736;
constexpr int F_TR = 21248;
constexpr int F_ST = 27136;

struct Params {
  const u16 *x, *kc, *vc, *wq, *wo, *wg, *wu, *wd, *c, *wmod;
  u16* out;
  u64* ws;
};

#define STAMP(i) do {} while (0)
#define STAMP_INIT() do {} while (0)
#define STAMP_DUMP() do {} while (0)
#define L_EXTRA 0

DEV float bf2f(unsigned b) { return __uint_as_float(b << 16); }
DEV unsigned cvt2(float lo, float hi) {
  const f32x2 v = {lo, hi};
  return __builtin_bit_cast(unsigned, __builtin_convertvector(v, bf16x2));
}
DEV unsigned pkr(float f) { return cvt2(f, f); }
DEV float rbf(float f) { return __uint_as_float(cvt2(f, f) & 0xFFFF0000u); }
struct Tag { unsigned hi, lo, rest; };
DEV Tag make_tag(u64 st) {
  const u64 nx = st * 6364136223846793005ull + 1442695040888963407ull;
  Tag t;
  t.hi = __builtin_amdgcn_readfirstlane((unsigned)(nx >> 32));
  t.rest = __builtin_amdgcn_readfirstlane((unsigned)nx);
  t.lo = t.rest >> 24;
  return t;
}
DEV u64 enc(float v, unsigned hi, unsigned lo) {
  const unsigned u = (__float_as_uint(v) + 0x80u) & 0xFFFFFF00u;
  return ((u64)hi << 32) | (u64)(u | lo);
}
DEV Tag tag_put(LAS unsigned char* L, u64 st, int lane) {
  const Tag t = make_tag(st);
  if (lane == 0) *(LAS v4u*)(L + L_TAG) = v4u{t.hi, t.lo, t.rest, 0u};
  return t;
}
DEV Tag tag_get(const LAS unsigned char* L) {
  const v4u v = *(const LAS v4u*)(L + L_TAG);
  Tag t; t.hi = __builtin_amdgcn_readfirstlane(v[0]); t.lo = __builtin_amdgcn_readfirstlane(v[1]); t.rest = __builtin_amdgcn_readfirstlane(v[2]);
  return t;
}
struct Chk {
  unsigned a = 0, b = 0;
  DEV void add(u64 g, const Tag& t) { a |= (unsigned)(g >> 32) ^ t.hi; b |= (unsigned)g ^ t.lo; }
  DEV bool all_ok() const { return __builtin_amdgcn_ballot_w64((a | (b & 0xFFu)) != 0u) == 0ull; }
};
DEV float gval(u64 g) { return __uint_as_float((unsigned)g & 0xFFFFFF00u); }
DEV void pub(u64* ws, int idx, float v, const Tag& t) { __hip_atomic_store((gu64*)ws + idx, enc(v, t.hi, t.lo), RLX_AGENT); }
DEV void pubp(gu64* dst, float v, const Tag& t) { __hip_atomic_store(dst, enc(v, t.hi, t.lo), RLX_AGENT); }
DEV u64 peek(u64* ws, int idx) { return __hip_atomic_load((gu64*)ws + idx, RLX_AGENT); }
template <int N, class F> DEV void gather(u64* ws, const Tag& t, u64 (&g)[N], F idx) {
  for (int spin = 0; spin < SPIN_CAP; ++spin) {
#pragma unroll
    for (int k = 0; k < N; ++k) g[k] = peek(ws, idx(k));
    Chk c;
#pragma unroll
    for (int k = 0; k < N; ++k) c.add(g[k], t);
    if (c.all_ok()) break;
  }
}

struct G2 { v4u v; DEV float lo() const { return __uint_as_float(v[0] & 0xFFFFFF00u); } DEV float hi() const { return __uint_as_float(v[2] & 0xFFFFFF00u); } };
template <int N, class F> DEV void gather2(rsrc_t rw, const Tag& t, G2 (&g)[N], F idx) {
  for (int spin = 0; spin < SPIN_CAP; ++spin) {
    asm volatile("" ::: "memory");
#pragma unroll
    for (int k = 0; k < N; ++k) g[k].v = __builtin_amdgcn_raw_buffer_load_b128(rw, idx(k) * 8, 0, 16);
    unsigned a = 0, b = 0;
#pragma unroll
    for (int k = 0; k < N; ++k) { a |= (g[k].v[1] ^ t.hi) | (g[k].v[3] ^ t.hi); b |= (g[k].v[0] ^ t.lo) | (g[k].v[2] ^ t.lo); }
    if (__builtin_amdgcn_ballot_w64((a | (b & 0xFFu)) != 0u) == 0ull) break;
  }
}
template <int N, class F> DEV void pretouch(LAS unsigned char* L, u64* ws, F idx) {
  const unsigned dump = (unsigned)(uintptr_t)(L + L_PF);
#pragma unroll
  for (int k = 0; k < N; ++k)
    asm volatile("s_mov_b32 m0, %0\n\ts_nop 0\n\tglobal_load_lds_dword %1, off sc1" :: "s"(dump), "v"((gu64*)ws + idx(k)) : "m0", "memory");
}
DEV f32x4 mfma(v4u a, v4u b, f32x4 c) {
  return __builtin_amdgcn_mfma_f32_16x16x32_bf16(__builtin_bit_cast(bf16x8, a), __builtin_bit_cast(bf16x8, b), c, 0, 0, 0);
}
DEV rsrc_t mk_rsrc(const void* p, unsigned bytes) { return __builtin_amdgcn_make_buffer_rsrc((void*)p, 0, (int)bytes, 0x00020000); }
DEV v4u ldb(rsrc_t r, unsigned voff, unsigned soff) { return __builtin_amdgcn_raw_buffer_load_b128(r, (int)voff, (int)soff, 2); }
#define SMALL_AUX 0
DEV unsigned ldb16(rsrc_t r, unsigned voff) { return __builtin_amdgcn_raw_buffer_load_b16(r, (int)voff, 0, SMALL_AUX); }
DEV void bar() {
  asm volatile("s_waitcnt lgkmcnt(0)" ::: "memory");
  __builtin_amdgcn_s_barrier();
  asm volatile("" ::: "memory");
}
#define PIN(x) asm volatile("" : "+v"(x))
#define PINS(x) asm volatile("" : "+s"(x))
DEV void lds_wait() { asm volatile("s_waitcnt lgkmcnt(0)" ::: "memory"); }
template <int CTRL> DEV float dpp(float x) {
  return __builtin_bit_cast(float, __builtin_amdgcn_mov_dpp(__builtin_bit_cast(int, x), CTRL, 0xf, 0xf, true));
}
constexpr int ROR8 = 0x128, ROR4 = 0x124, ROR2 = 0x122, ROR1 = 0x121, XOR1 = 0xB1, XOR2 = 0x4E;
DEV float x16_sum(float x) {
  auto s = __builtin_amdgcn_permlane16_swap(__float_as_uint(x), __float_as_uint(x), false, false);
  x = __uint_as_float(s[0]) + __uint_as_float(s[1]);
  auto t = __builtin_amdgcn_permlane32_swap(__float_as_uint(x), __float_as_uint(x), false, false);
  return __uint_as_float(t[0]) + __uint_as_float(t[1]);
}
DEV float x16_max(float x) {
  auto s = __builtin_amdgcn_permlane16_swap(__float_as_uint(x), __float_as_uint(x), false, false);
  x = fmaxf(__uint_as_float(s[0]), __uint_as_float(s[1]));
  auto t = __builtin_amdgcn_permlane32_swap(__float_as_uint(x), __float_as_uint(x), false, false);
  return fmaxf(__uint_as_float(t[0]), __uint_as_float(t[1]));
}
DEV float row_sum(float x) { x += dpp<ROR8>(x); x += dpp<ROR4>(x); x += dpp<ROR2>(x); x += dpp<ROR1>(x); return x; }
DEV float row_max(float x) { x = fmaxf(x, dpp<ROR8>(x)); x = fmaxf(x, dpp<ROR4>(x)); x = fmaxf(x, dpp<ROR2>(x)); x = fmaxf(x, dpp<ROR1>(x)); return x; }
DEV float wave_sum(float x) { return x16_sum(row_sum(x)); }
constexpr float LOG2E = 1.4426950408889634f;
DEV float fadd(float a, float b) { float r; asm("v_add_f32 %0, %1, %2" : "=v"(r) : "v"(a), "v"(b)); return r; }
DEV float exp_f(float x) { return __builtin_amdgcn_exp2f(x * LOG2E); }
DEV float rcp_f(float x) { return __builtin_amdgcn_rcpf(x); }
DEV float pair_pick(float a, float b, int lane) {
  const float a2 = dpp<0x50>(a), b2 = dpp<0x50>(b);
  const float a3 = dpp<0xFA>(a), b3 = dpp<0xFA>(b);
  const float lo = (lane & 1) ? b2 : a2, hi = (lane & 1) ? b3 : a3;
  return (lane & 4) ? hi : lo;
}
DEV float rdlane(float x, int l) { return __uint_as_float(__builtin_amdgcn_readlane(__float_as_uint(x), l)); }
DEV void gate_wait(u64 t0, unsigned ticks) { while (__builtin_amdgcn_s_memrealtime() - t0 < (u64)ticks) __builtin_amdgcn_s_sleep(2); }

struct Mask { unsigned m[4]; };
DEV Mask make_mask(int lane, bool on) {
  Mask k;
  const unsigned h = on ? ((lane & 1) ? 0xFFFF0000u : 0x0000FFFFu) : 0u;
#pragma unroll
  for (int r = 0; r < 4; ++r) k.m[r] = (((lane & 7) >> 1) == r) ? h : 0u;
  return k;
}
DEV v4u afrag(unsigned pp, const Mask& k) { return v4u{pp & k.m[0], pp & k.m[1], pp & k.m[2], pp & k.m[3]}; }

DEV unsigned ln_apply_pk(float x, float mean_b, float rden, float c1, float shift) {
  const float ln = rbf(rbf(x - mean_b) * rden);
  return pkr(rbf(ln * c1) + shift);
}
DEV float ln_rden(float var) { return rcp_f(rbf(__builtin_amdgcn_sqrtf(rbf(rbf(var) + 1e-5f)))); }

DEV void red_store(LAS float* area, f32x4 d, int lane) {
  const int u = (lane >> 3) & 1;
  if ((lane >> 5) == u) *(LAS f32x4*)(area + u * 64 + 8 * (lane & 7) + 4 * ((lane >> 4) & 1)) = d;
}
DEV unsigned red_off(int lane, int dump) {
  const int u = (lane >> 3) & 1;
  return 4 * ((lane >> 5) == u ? u * 64 + 8 * (lane & 7) + 4 * ((lane >> 4) & 1) : dump);
}
DEV unsigned silu_pk(unsigned craw) { const float v = bf2f(craw); return pkr(v * rcp_f(1.f + exp_f(-v))); }

DEV void wo_phase(LAS unsigned char* L, u64* ws, const Tag& tag, const Mask& mk, int pidx, int wave, int lane) {
  unsigned tro = L_OL + wave * 640 + red_off(lane, 128), tri = L_OL + wave * 640 + lane * 4, obo = L_OB + (lane >> 3) * 16, wro = L_WO + wave * 4096 + lane * 16;
  gu64* dst = (gu64*)ws + G_WO + pidx * 576 + wave * 64 + lane;
  PIN(tro); PIN(tri); PIN(obo); PIN(wro); PIN(dst);
  bar();
  const v4u ob = *(const LAS v4u*)(L + obo);
  const LAS v4u* wr = (const LAS v4u*)(L + wro);
  f32x4 d0 = {0.f, 0.f, 0.f, 0.f}, d1 = {0.f, 0.f, 0.f, 0.f};
  d0 = mfma(afrag(ob[0], mk), wr[0], d0); d1 = mfma(afrag(ob[1], mk), wr[64], d1);
  d0 = mfma(afrag(ob[2], mk), wr[128], d0); d1 = mfma(afrag(ob[3], mk), wr[192], d1);
  *(LAS f32x4*)(L + tro) = d0 + d1;
  lds_wait();
  pubp(dst, fadd(*(const LAS float*)(L + tri), *(const LAS float*)(L + tri + 256)), tag);
}

struct ModA { v4u w[18]; unsigned c[3]; };
DEV void moda_load(const Params& p, int mt, int lane, ModA& m) {
  const rsrc_t rm = mk_rsrc(p.wmod, D * NMOD * 2), rc = mk_rsrc(p.c, D * 2);
  const int r0 = (mt & 3) * 144;
#pragma unroll
  for (int k = 0; k < 3; ++k) m.c[k] = ldb16(rc, (r0 + (k < 2 || lane < 16 ? lane + 64 * k : 143)) * 2);
  const unsigned v = (r0 + (lane >> 3)) * (NMOD * 2) + (lane & 7) * 16;
#pragma unroll
  for (int t = 0; t < 18; ++t) m.w[t] = ldb(rm, v, (mt >> 2) * 128 + t * 8 * (NMOD * 2));
}
DEV void moda_finish(LAS unsigned char* L, u64* ws, int mt, int lane, ModA& m, const Mask& mk, LAS float* tr, const Tag& tag) {
  unsigned sp[18];
  {
    const unsigned s0 = silu_pk(m.c[0]), s1 = silu_pk(m.c[1]), s2 = silu_pk(m.c[2]);
#pragma unroll
    for (int t = 0; t < 18; ++t) sp[t] = __builtin_amdgcn_ds_bpermute(((8 * t + (lane >> 3)) & 63) * 4, t < 8 ? s0 : t < 16 ? s1 : s2);
  }
  gu64* dst = (gu64*)ws + G_MOD + (mt & 3) * NMOD + (mt >> 2) * 64 + lane;
  unsigned tro = red_off(lane, 128);
  PIN(dst); PIN(tro);
#pragma unroll
  for (int t = 0; t < 18; ++t) PIN(sp[t]);
  __builtin_amdgcn_sched_barrier(0);
  STAMP(17);
  f32x4 d0 = {0.f, 0.f, 0.f, 0.f}, d1 = {0.f, 0.f, 0.f, 0.f};
#pragma unroll
  for (int t = 0; t < 18; t += 2) {
    d0 = mfma(afrag(sp[t], mk), m.w[t], d0);
    __builtin_amdgcn_sched_barrier(0);
    d1 = mfma(afrag(sp[t + 1], mk), m.w[t + 1], d1);
    __builtin_amdgcn_sched_barrier(0);
  }
  *(LAS f32x4*)((LAS unsigned char*)tr + tro) = d0 + d1;
  lds_wait();
  pubp(dst, fadd(tr[lane], tr[64 + lane]), tag);
}

DEV void role_att(const Params& p, LAS unsigned char* L, int x, int i, int wave, int lane, u64 t0) {
  const int g = 2 * x + (i >= NSPLIT ? 1 : 0), j = i >= NSPLIT ? i - NSPLIT : i;
  const int ntile = j < 8 ? 23 : 24, tile0 = j < 8 ? 23 * j : 184 + 24 * (j - 8);
  const bool owner = j < 8;
  int redc = -1;
  if (j >= 8) redc = 9 * x + (g & 1) * 3 + (j - 8);
  else if (j == 0) redc = 9 * x + 6 + (g & 1);
  else if (j == 1 && (g & 1) == 0) redc = 9 * x + 8;
  u64* ws = p.ws;
  const Mask mk = make_mask(lane, true);
  const int kap = lane >> 4, cc = lane & 15;
  const bool moda = i < 9;

  if (wave < 8) {
    const size_t hb = ((size_t)g * SK + (SK - WIN)) * DH;
    const rsrc_t rk = mk_rsrc(p.kc + hb, WIN * DH * 2), rv = mk_rsrc(p.vc + hb, WIN * DH * 2);
    const bool three = wave + 16 < ntile;
    const unsigned tso = (tile0 + wave) * 4096;
    const unsigned ldsk = (unsigned)(uintptr_t)(L + L_K) + wave * 3 * 4096;
    unsigned vk[4];
#pragma unroll
    for (int q = 0; q < 4; ++q) vk[q] = (4 * q + kap) * 256 + ((cc ^ (4 * q + kap)) << 4);
#define KDMA(s, q) asm volatile("s_mov_b32 m0, %0\n\ts_nop 0\n\tbuffer_load_dwordx4 %1, %2, %3 offen nt lds" \
                     :: "s"(ldsk + (s) * 4096 + (q) * 1024), "v"(vk[q]), "s"(rk), "s"(tso + (s) * 32768) : "m0", "memory")
    STAMP(11);
#define THR(n) do { if ((n) < ATT_N1) { if ((n) >= ATT_D1 && moda) asm volatile("s_waitcnt vmcnt(%0)" :: "n"(ATT_D1 - 1) : "memory"); } \
                     else if ((n) >= ATT_D2) asm volatile("s_waitcnt vmcnt(%0)" :: "n"(ATT_D2 - 1) : "memory"); } while (0)
#define THR_DEPTH(n) ((n) < ATT_N1 ? ATT_D1 : ATT_D2)
#define THRV(t) asm volatile("s_waitcnt vmcnt(%0)" :: "n"(((t) < V_N1 ? V_D1 : V_D2) - 1) : "memory")
    if (moda) __builtin_amdgcn_s_barrier();
    __builtin_amdgcn_s_setprio(2);
    KDMA(0, 0);
    if (GATE_ATT) gate_wait(t0, GATE_ATT);
    THR(1); KDMA(0, 1); THR(2); KDMA(0, 2); THR(3); KDMA(0, 3);
    STAMP(12);
    THR(4); KDMA(1, 0); THR(5); KDMA(1, 1); THR(6); KDMA(1, 2); THR(7); KDMA(1, 3);
    if (three) { THR(8); KDMA(2, 0); THR(9); KDMA(2, 1); THR(10); KDMA(2, 2); THR(11); KDMA(2, 3); }
#undef KDMA
    __builtin_amdgcn_s_setprio(0);
    STAMP(13);
    v4u vr[12];
    const unsigned vv = kap * 1024 + cc * 16;
#pragma unroll
    for (int t = 0; t < 8; ++t) { THRV(t); vr[t] = ldb(rv, vv + (t & 3) * 256, tso + (t >> 2) * 32768); }
    if (three) {
#pragma unroll
      for (int t = 8; t < 12; ++t) { THRV(t); vr[t] = ldb(rv, vv + (t & 3) * 256, tso + 65536); }
    } else {
#pragma unroll
      for (int t = 8; t < 12; ++t) vr[t] = v4u{0u, 0u, 0u, 0u};
    }
#undef THR
#undef THRV
#undef THR_DEPTH
    __builtin_amdgcn_sched_barrier(0);
    STAMP(1);
    if (three) asm volatile("s_waitcnt vmcnt(12)" ::: "memory");
    else asm volatile("s_waitcnt vmcnt(8)" ::: "memory");
    v4u kf[12];
#pragma unroll
    for (int s = 0; s < 3; ++s)
#pragma unroll
      for (int ks = 0; ks < 4; ++ks)
        kf[s * 4 + ks] = *(const LAS v4u*)(L + L_K + (wave * 3 + (s < 2 || three ? s : 0)) * 4096 + cc * 256 + (((4 * ks + kap) ^ cc) << 4));
#pragma unroll
    for (int t = 0; t < 12; ++t) PIN(kf[t]);
    STAMP(3);
    bar();
    const Tag tag = tag_get(L);
    STAMP(2);
    v4u qf[4];
    const int head = (lane >> 3) & 1;
#pragma unroll
    for (int ks = 0; ks < 4; ++ks) qf[ks] = *(const LAS v4u*)(L + L_Q + head * 256 + ks * 64 + kap * 16);
    float sc[12];
#pragma unroll
    for (int s = 0; s < 3; ++s) {
      if (s < 2 || three) {
        f32x4 a = {0.f, 0.f, 0.f, 0.f};
#pragma unroll
        for (int ks = 0; ks < 4; ++ks) a = mfma(kf[s * 4 + ks], qf[ks], a);
#pragma unroll
        for (int r = 0; r < 4; r += 2) {
          const unsigned u = cvt2(a[r], a[r + 1]);
          const unsigned w = cvt2(__uint_as_float(u << 16) * 0.08838834764831845f, __uint_as_float(u & 0xFFFF0000u) * 0.08838834764831845f);
          sc[s * 4 + r] = __uint_as_float(w << 16);
          sc[s * 4 + r + 1] = __uint_as_float(w & 0xFFFF0000u);
        }
      } else {
#pragma unroll
        for (int r = 0; r < 4; ++r) sc[s * 4 + r] = -INFINITY;
      }
    }
    float mx = sc[0];
#pragma unroll
    for (int t = 1; t < 12; ++t) mx = fmaxf(mx, sc[t]);
    mx = x16_max(mx);
    if ((lane & 0x37) == 0) ((LAS float*)(L + L_MX))[head * 8 + wave] = mx;
    STAMP(15);
    bar();
    STAMP(16);
    float m;
    {
      const f32x4 a = *(const LAS f32x4*)(L + L_MX + head * 32), b = *(const LAS f32x4*)(L + L_MX + head * 32 + 16);
      m = fmaxf(fmaxf(fmaxf(a[0], a[1]), fmaxf(a[2], a[3])), fmaxf(fmaxf(b[0], b[1]), fmaxf(b[2], b[3])));
    }
    v4u af[12];
    float ls = 0.f;
    const float mneg = -m * LOG2E;
#pragma unroll
    for (int t = 0; t < 12; ++t) { const float e = __builtin_amdgcn_exp2f(fmaf(sc[t], LOG2E, mneg)); ls += e; af[t] = afrag(pkr(e), mk); }
    ls = x16_sum(ls);
    if ((lane & 0x37) == 0) ((LAS float*)(L + L_LS))[head * 8 + wave] = ls;
    const int me = (wave & 3) * 64 + lane;
    unsigned msrc = L_OL + ((16 * (((me >> 7) * 8 + (me & 7)) >> 2) + ((me & 127) >> 3)) * 4 + (me & 3)) * 4;
    gu64* mdst = (gu64*)ws + G_O + ((g * 8 + (me >> 5)) * NSPLIT + j) * 64 + (me & 31);
    PIN(msrc); PIN(mdst);
    __builtin_amdgcn_sched_barrier(0);
    STAMP(4);
    f32x4 o0 = {0.f, 0.f, 0.f, 0.f}, o1 = {0.f, 0.f, 0.f, 0.f};
#pragma unroll
    for (int t = 0; t < 12; t += 2) { o0 = mfma(af[t], vr[t], o0); o1 = mfma(af[t + 1], vr[t + 1], o1); }
    *(LAS f32x4*)(L + L_OL + wave * 1024 + lane * 16) = o0 + o1;
    STAMP(5);
    bar();
    if (wave < 4) {
      const LAS float* src = (const LAS float*)(L + msrc);
      float s[8];
#pragma unroll
      for (int w = 0; w < 8; ++w) s[w] = src[w * 256];
      pubp(mdst, fadd(fadd(fadd(s[0], s[1]), fadd(s[2], s[3])), fadd(fadd(s[4], s[5]), fadd(s[6], s[7]))), tag);
    } else if (wave == 4 && lane < 16) {
      const int u = lane >> 1, hh = u >> 2;
      const f32x4 a = *(const LAS f32x4*)(L + ((lane & 1) ? L_LS : L_MX) + hh * 32), b = *(const LAS f32x4*)(L + ((lane & 1) ? L_LS : L_MX) + hh * 32 + 16);
      const float vs = ((a[0] + a[1]) + (a[2] + a[3])) + ((b[0] + b[1]) + (b[2] + b[3]));
      const float vm = fmaxf(fmaxf(fmaxf(a[0], a[1]), fmaxf(a[2], a[3])), fmaxf(fmaxf(b[0], b[1]), fmaxf(b[2], b[3])));
      pub(ws, G_O + ((g * 8 + u) * NSPLIT + j) * 64 + 32 + (lane & 1), (lane & 1) ? vs : vm, tag);
    }
    STAMP(6);
    if (owner) {
      wo_phase(L, ws, tag, mk, g * 8 + j, wave, lane);
      STAMP(8);
    }
  } else {
    __builtin_amdgcn_s_setprio(3);
    int zoff = G_EPOCH;
    asm volatile("" : "+v"(zoff));
    Tag tag;
    {
      const int mt = moda ? 9 * x + i : 72 + 13 * x + (i - 9);
      u64 st = __hip_atomic_load((gu64*)ws + zoff, RLX_AGENT);
      if (!moda && GATE_MODB) gate_wait(t0, GATE_MODB);
      ModA ma;
      moda_load(p, mt, lane, ma);
      __builtin_amdgcn_sched_barrier(0);
      STAMP(14);
      if (moda) __builtin_amdgcn_s_barrier();
      asm volatile("" : "+v"(st));
      tag = tag_put(L, st, lane);
      moda_finish(L, ws, mt, lane, ma, mk, (LAS float*)(L + L_MA), tag);
      STAMP(9);
    }
    float xr = 0.f;
    if (redc >= 0) xr = bf2f(p.x[redc * 8 + (lane & 7)]);
    if (GATE_Q) gate_wait(t0, GATE_Q);
    const rsrc_t rw = mk_rsrc(ws, G_END * 8);
    {
      G2 gq[4];
      gather2<4>(rw, tag, gq, [&](int k) { return G_Q + ((2 * g + (k >> 1)) * 2 + (k & 1)) * 128 + 2 * lane; });
      ((LAS unsigned*)(L + L_Q))[lane] = cvt2(fadd(gq[0].lo(), gq[1].lo()), fadd(gq[0].hi(), gq[1].hi()));
      ((LAS unsigned*)(L + L_Q))[64 + lane] = cvt2(fadd(gq[2].lo(), gq[3].lo()), fadd(gq[2].hi(), gq[3].hi()));
    }
    STAMP(1);
    if (owner) {
      const int ob = G_O + (g * 8 + j) * NSPLIT * 64;
      pretouch<1>(L, ws, [&](int) { const int q_ = lane < 33 ? lane : 32; return ob + (q_ / 3) * 64 + (q_ % 3) * 16; });
    }
    if (redc >= 0) {
      pretouch<2>(L, ws, [&](int s) { return G_WO + (s * 64 + lane) * 576 + redc * 8; });
      pretouch<2>(L, ws, [&](int s) { const int f_ = s * 64 + lane; return G_DN + (f_ < 72 ? f_ : 71) * 576 + redc * 8; });
    }
    bar();
    if (owner) {
      const rsrc_t rwo = mk_rsrc(p.wo, 4096 * D * 2);
      const unsigned wo_v = (lane >> 3) * (D * 2) + (lane & 7) * 16;
      const unsigned wo_s = (g * 256 + j * 32) * (D * 2), ldsw = (unsigned)(uintptr_t)(L + L_WO);
#pragma unroll
      for (int w = 0; w < 9; ++w)
#pragma unroll
        for (int t = 0; t < 4; ++t)
          asm volatile("s_mov_b32 m0, %0\n\ts_nop 0\n\tbuffer_load_dwordx4 %1, %2, %3 offen nt lds"
                       :: "s"(ldsw + (w * 4 + t) * 1024), "v"(wo_v), "s"(rwo), "s"(wo_s + w * 128 + t * 8 * (D * 2)) : "m0", "memory");
    }
    bar();
    bar();
    STAMP(2);
    if (owner) {
      const int ob = G_O + (g * 8 + j) * NSPLIT * 64;
      const int k2 = kap < 3 ? 8 + kap : 10;
      unsigned obw = L_OB + (((2 * cc) & 7) * 4 + (cc >> 2)) * 4;
      float live = kap < 3 ? 1.f : 0.f;
      PIN(obw); PIN(live);
      G2 go[6];
      gather2<6>(rw, tag, go, [&](int k) { const int sp = k % 3 == 0 ? kap : k % 3 == 1 ? 4 + kap : k2; return ob + sp * 64 + (k < 3 ? 2 * cc : 32); });
      STAMP(3);
      const float M = x16_max(fmaxf(fmaxf(go[3].lo(), go[4].lo()), go[5].lo()));
      const float Mn = -M * LOG2E;
      const float w0 = __builtin_amdgcn_exp2f(fmaf(go[3].lo(), LOG2E, Mn)), w1 = __builtin_amdgcn_exp2f(fmaf(go[4].lo(), LOG2E, Mn));
      const float w2 = __builtin_amdgcn_exp2f(fmaf(go[5].lo(), LOG2E, Mn)) * live;
      const float Ls = x16_sum(fmaf(w2, go[5].hi(), fmaf(w1, go[4].hi(), w0 * go[3].hi())));
      const float o0 = x16_sum(fmaf(w2, go[2].lo(), fmaf(w1, go[1].lo(), w0 * go[0].lo())));
      const float o1 = x16_sum(fmaf(w2, go[2].hi(), fmaf(w1, go[1].hi(), w0 * go[0].hi())));
      const float rl = rcp_f(Ls);
      *(LAS unsigned*)(L + obw) = pkr(o0 * rl);
      *(LAS unsigned*)(L + obw + 16) = pkr(o1 * rl);
      asm volatile("s_waitcnt vmcnt(0)" ::: "memory");
      wo_phase(L, ws, tag, mk, g * 8 + j, 8, lane);
      STAMP(4);
    }
    if (redc >= 0) {
      u64 gg[8];
      gather<8>(ws, tag, gg, [&](int k) { return G_MOD + (k & 3) * NMOD + (k < 4 ? 2 : 5) * D + redc * 8 + (lane & 7); });
      float ga = rbf(fadd(fadd(gval(gg[0]), gval(gg[1])), fadd(gval(gg[2]), gval(gg[3]))));
      float gb = rbf(fadd(fadd(gval(gg[4]), gval(gg[5])), fadd(gval(gg[6]), gval(gg[7]))));
      gu64* xdst = (gu64*)ws + G_X2 + redc * 16 + (lane & 7);
      GAS u16* odst = (GAS u16*)p.out + redc * 8 + (lane & 7);
      PIN(ga); PIN(gb); PIN(xr); PIN(xdst); PIN(odst);
      float x2;
      {
        G2 gw[8];
        gather2<8>(rw, tag, gw, [&](int s) { return G_WO + (s * 16 + (lane >> 2)) * 576 + redc * 8 + 2 * (lane & 3); });
        STAMP(5);
        float t0_ = fadd(fadd(fadd(gw[0].lo(), gw[1].lo()), fadd(gw[2].lo(), gw[3].lo())), fadd(fadd(gw[4].lo(), gw[5].lo()), fadd(gw[6].lo(), gw[7].lo())));
        float t1_ = fadd(fadd(fadd(gw[0].hi(), gw[1].hi()), fadd(gw[2].hi(), gw[3].hi())), fadd(fadd(gw[4].hi(), gw[5].hi()), fadd(gw[6].hi(), gw[7].hi())));
        t0_ += dpp<ROR8>(t0_); t1_ += dpp<ROR8>(t1_); t0_ += dpp<ROR4>(t0_); t1_ += dpp<ROR4>(t1_);
        t0_ = x16_sum(t0_); t1_ = x16_sum(t1_);
        const float ts = pair_pick(t0_, t1_, lane);
        x2 = rbf(xr + rbf(ga * rbf(ts)));
        if (lane < 8) pubp(xdst, x2, tag);
      }
      STAMP(6);
      {
        G2 gd[5];
        float w4 = lane < 32 ? 1.f : 0.f;
        PIN(w4);
        gather2<5>(rw, tag, gd, [&](int s) { const int f_ = s * 16 + (lane >> 2); return G_DN + (f_ < 72 ? f_ : 71) * 576 + redc * 8 + 2 * (lane & 3); });
        STAMP(7);
        float t0_ = fmaf(w4, gd[4].lo(), fadd(fadd(gd[0].lo(), gd[1].lo()), fadd(gd[2].lo(), gd[3].lo())));
        float t1_ = fmaf(w4, gd[4].hi(), fadd(fadd(gd[0].hi(), gd[1].hi()), fadd(gd[2].hi(), gd[3].hi())));
        t0_ += dpp<ROR8>(t0_); t1_ += dpp<ROR8>(t1_); t0_ += dpp<ROR4>(t0_); t1_ += dpp<ROR4>(t1_);
        t0_ = x16_sum(t0_); t1_ = x16_sum(t1_);
        const float ts = pair_pick(t0_, t1_, lane);
        const unsigned o = pkr(x2 + rbf(gb * rbf(ts)));
        if (lane < 8) *odst = (u16)o;
        if (redc == 0 && lane == 0) __hip_atomic_store((gu64*)ws + G_EPOCH, ((u64)tag.hi << 32) | (u64)tag.rest, RLX_AGENT);
      }
      STAMP(8);
    }
  }
}

DEV void role_ffn(const Params& p, LAS unsigned char* L, int x, int i, int wave, int lane, u64 t0) {
  const int f = 9 * x + i;
  u64* ws = p.ws;
  const Mask mk = make_mask(lane, true);
  LAS float* red = (LAS float*)(L + F_RED);
  const rsrc_t rm = mk_rsrc(p.wmod, D * NMOD * 2), rc = mk_rsrc(p.c, D * 2), rx = mk_rsrc(p.x, D * 2);
  STAMP(11);
  const int hq = 4 * x + (i >> 1), qr = i & 1;
  const rsrc_t rw = mk_rsrc(ws, G_END * 8);
  Tag tag;
  if (i < 8) {
    v4u wa[12];
    if (wave >= 3) {
      const rsrc_t rq = mk_rsrc(p.wq, D * 4096 * 2);
      const unsigned v = (lane >> 4) * 8192 + (lane & 15) * 16, s = (qr * 288 + (wave - 3) * 48) * 8192 + hq * 256;
#pragma unroll
      for (int t = 0; t < 12; ++t) wa[t] = ldb(rq, v, s + t * 4 * 8192);
      __builtin_amdgcn_sched_barrier(0);
      STAMP(1);
    } else {
      __builtin_amdgcn_s_setprio(3);
      int zoff = G_EPOCH;
      asm volatile("" : "+v"(zoff));
      const u64 st = __hip_atomic_load((gu64*)ws + zoff, RLX_AGENT);
      float xv[9];
#pragma unroll
      for (int k = 0; k < 9; ++k) xv[k] = bf2f(ldb16(rx, (lane + 64 * k) * 2));
      const int p0 = wave * 64 + lane, pr = p0 < 144 ? p0 : 143;
      const unsigned xo = __builtin_amdgcn_raw_buffer_load_b32(rx, (qr * 288 + 2 * pr) * 2, 0, 0);
      float s = 0.f;
#pragma unroll
      for (int k = 0; k < 9; ++k) s += xv[k];
      const float mf = wave_sum(s) * (1.f / D);
      float v = 0.f;
#pragma unroll
      for (int k = 0; k < 9; ++k) { const float d = xv[k] - mf; v = fmaf(d, d, v); }
      const float rden = ln_rden(wave_sum(v) * (1.f / D)), mean = rbf(mf);
      tag = wave == 0 ? tag_put(L, st, lane) : make_tag(st);
      const int hw = pr / 24, hr = 2 * pr - hw * 48;
      unsigned hdst = F_H + ((hw * 4 + (hr & 3)) * 12 + (hr >> 2)) * 4;
      float ln0 = rbf(rbf(__uint_as_float(xo << 16) - mean) * rden), ln1 = rbf(rbf(__uint_as_float(xo & 0xFFFF0000u) - mean) * rden);
      PIN(hdst); PIN(ln0); PIN(ln1);
      STAMP(2);
      G2 ga[8];
      gather2<8>(rw, tag, ga, [&](int k) { return G_MOD + (k & 3) * NMOD + (k >> 2) * D + qr * 288 + 2 * pr; });
      STAMP(3);
      const float sa0 = rbf(fadd(fadd(ga[0].lo(), ga[1].lo()), fadd(ga[2].lo(), ga[3].lo()))), sa1 = rbf(fadd(fadd(ga[0].hi(), ga[1].hi()), fadd(ga[2].hi(), ga[3].hi())));
      const float ca0 = rbf(fadd(fadd(ga[4].lo(), ga[5].lo()), fadd(ga[6].lo(), ga[7].lo()))), ca1 = rbf(fadd(fadd(ga[4].hi(), ga[5].hi()), fadd(ga[6].hi(), ga[7].hi())));
      *(LAS unsigned*)(L + hdst) = pkr(rbf(ln0 * rbf(1.f + ca0)) + sa0);
      *(LAS unsigned*)(L + hdst + 48) = pkr(rbf(ln1 * rbf(1.f + ca1)) + sa1);
      __builtin_amdgcn_s_setprio(0);
    }
    Mask mq = make_mask(lane, (lane & 8) == 0);
    const int qe = (wave & 1) * 64 + lane;
    unsigned hsrc = F_H + ((wave - 3) * 4 + (lane >> 4)) * 48, qst = F_RED + 4608 + (wave - 3) * 1024 + lane * 16;
    unsigned qsrc = F_RED + 4608 + ((16 * ((qe & 7) >> 2) + (qe >> 3)) * 4 + (qe & 3)) * 4;
    gu64* qdst = (gu64*)ws + G_Q + (hq * 2 + qr) * 128 + qe;
    PIN(mq.m[0]); PIN(mq.m[1]); PIN(mq.m[2]); PIN(mq.m[3]); PIN(hsrc); PIN(qst); PIN(qsrc); PIN(qdst);
    bar();
    if (wave >= 3) {
      const LAS v4u* hp = (const LAS v4u*)(L + hsrc);
      const v4u h0 = hp[0], h1 = hp[1], h2 = hp[2];
      f32x4 d0 = {0.f, 0.f, 0.f, 0.f}, d1 = {0.f, 0.f, 0.f, 0.f};
#pragma unroll
      for (int t = 0; t < 4; ++t) { d0 = mfma(afrag(h0[t], mq), wa[t], d0); d1 = mfma(afrag(h1[t], mq), wa[4 + t], d1); }
#pragma unroll
      for (int t = 0; t < 4; t += 2) { d0 = mfma(afrag(h2[t], mq), wa[8 + t], d0); d1 = mfma(afrag(h2[t + 1], mq), wa[9 + t], d1); }
      *(LAS f32x4*)(L + qst) = d0 + d1;
    }
    bar();
    if (wave < 2) {
      const LAS float* src = (const LAS float*)(L + qsrc);
      pubp(qdst, fadd(fadd(fadd(src[0], src[256]), fadd(src[512], src[768])), fadd(src[1024], src[1280])), tag);
    }
  } else {
    if (wave == 0) {
      int zoff = G_EPOCH;
      asm volatile("" : "+v"(zoff));
      tag = tag_put(L, __hip_atomic_load((gu64*)ws + zoff, RLX_AGENT), lane);
    }
    bar();
  }
  tag = tag_get(L);
  STAMP(4);
  pretouch<1>(L, ws, [&](int) { const int e = wave * 64 + lane; return G_X2 + (e >> 3) * 16 + (e & 7); });
  gate_wait(t0, GATE_FFN);
#define THRF(n) do { if ((n) < F_N1) asm volatile("s_waitcnt vmcnt(%0)" :: "n"(F_D1 - 1) : "memory"); } while (0)
  v4u wg[9], wu[9], wd[8];
  if (wave < 8) {
    const rsrc_t rg = mk_rsrc(p.wg, D * FF * 2), ru = mk_rsrc(p.wu, D * FF * 2);
    const unsigned v = (lane >> 3) * (FF * 2) + (lane & 7) * 16, s = wave * 72 * (FF * 2) + f * 128;
#pragma unroll
    for (int t = 0; t < 9; ++t) { THRF(2 * t); wg[t] = ldb(rg, v, s + t * 8 * (FF * 2)); THRF(2 * t + 1); wu[t] = ldb(ru, v, s + t * 8 * (FF * 2)); }
  }
  {
    const rsrc_t rd = mk_rsrc(p.wd, FF * D * 2);
    const unsigned v = (lane >> 3) * (D * 2) + (lane & 7) * 16, s = f * 64 * (D * 2) + wave * 128;
#pragma unroll
    for (int t = 0; t < 8; ++t) { THRF(18 + t); wd[t] = ldb(rd, v, s + t * 8 * (D * 2)); }
  }
  __builtin_amdgcn_sched_barrier(0);
  STAMP(5);
  unsigned h2w, h2r, stw, str_, gur, guw0, actw, actr, trw, trr;
  gu64* ddst;
  {
    const int e = wave * 64 + lane, hw = e / 72, rem = e - hw * 72, c = (wave & 3) * 16 + (lane >> 2);
    h2w = F_H2 + ((hw * 8 + (rem & 7)) * 12 + (rem >> 3)) * 4;
    h2r = F_H2 + ((wave & 7) * 8 + (lane >> 3)) * 48;
    stw = F_ST + wave * 8;
    str_ = F_ST + ((lane & 15) < 9 ? (lane & 15) : 9) * 8;
    guw0 = F_RED + (wave & 7) * 1040 + red_off(lane, 256);
    gur = F_RED + ((lane & 3) * 520 + c) * 4;
    actw = F_ACT + ((lane & 3) == 0 ? (c & 7) * 8 + (c >> 3) : 64) * 4;
    actr = F_ACT + (lane >> 3) * 32;
    trw = F_TR + wave * 640 + red_off(lane, 128);
    trr = F_TR + wave * 640 + lane * 4;
    ddst = (gu64*)ws + G_DN + f * 576 + wave * 64 + lane;
    if (wave == 0 && lane == 0) *(LAS f32x2*)(L + F_ST + 72) = f32x2{0.f, 0.f};
  }
  PIN(h2w); PIN(h2r); PIN(stw); PIN(str_); PIN(guw0); PIN(gur); PIN(actw); PIN(actr); PIN(trw); PIN(trr); PIN(ddst);
  {
    const int e = wave * 64 + lane;
    u64 gs[8];
    gather<8>(ws, tag, gs, [&](int k) { return G_MOD + (k & 3) * NMOD + (k < 4 ? 3 : 4) * D + e; });
    float sb = rbf(fadd(fadd(gval(gs[0]), gval(gs[1])), fadd(gval(gs[2]), gval(gs[3]))));
    float c1b = rbf(1.f + rbf(fadd(fadd(gval(gs[4]), gval(gs[5])), fadd(gval(gs[6]), gval(gs[7])))));
    float on = (lane & 15) < 9 ? 64.f : 0.f;
    PIN(sb); PIN(c1b); PIN(on);
    STAMP(6);
    u64 gx[1];
    gather<1>(ws, tag, gx, [&](int) { return G_X2 + (e >> 3) * 16 + (e & 7); });
    STAMP(7);
    const float xv = gval(gx[0]);
    const float sh = __uint_as_float(__builtin_amdgcn_readfirstlane(__float_as_uint(xv)));
    const float d = xv - sh;
    float s1 = d, s2 = d * d;
    s1 += dpp<ROR8>(s1); s2 += dpp<ROR8>(s2); s1 += dpp<ROR4>(s1); s2 += dpp<ROR4>(s2);
    s1 += dpp<ROR2>(s1); s2 += dpp<ROR2>(s2); s1 += dpp<ROR1>(s1); s2 += dpp<ROR1>(s2);
    s1 = x16_sum(s1); s2 = x16_sum(s2);
    const float dm = s1 * (1.f / 64.f);
    if (lane == 0) *(LAS f32x2*)(L + stw) = f32x2{sh + dm, fmaf(-dm, s1, s2)};
    bar();
    const f32x2 st = *(const LAS f32x2*)(L + str_);
    const float mf = row_sum(st[0]) * (1.f / 9.f);
    const float dv = st[0] - mf;
    const float m2 = row_sum(fmaf(on * dv, dv, st[1]));
    *(LAS unsigned*)(L + h2w) = ln_apply_pk(xv, rbf(mf), ln_rden(m2 * (1.f / D)), c1b, sb);
  }
  bar();
  STAMP(8);
  if (wave < 8) {
    const LAS v4u* hp = (const LAS v4u*)(L + h2r);
    const v4u h0 = hp[0], h1 = hp[1];
    const unsigned h8 = ((const LAS unsigned*)hp)[8];
    f32x4 dg = {0.f, 0.f, 0.f, 0.f}, du = {0.f, 0.f, 0.f, 0.f};
#pragma unroll
    for (int t = 0; t < 9; ++t) {
      const v4u a = afrag(t < 4 ? h0[t & 3] : t < 8 ? h1[t & 3] : h8, mk);
      dg = mfma(a, wg[t], dg);
      du = mfma(a, wu[t], du);
    }
    *(LAS f32x4*)(L + guw0) = dg;
    *(LAS f32x4*)(L + guw0 + ((lane >> 5) == ((lane >> 3) & 1) ? 512 : 0)) = du;
  }
  bar();
  if (wave < 4) {
    const LAS float* r0 = (const LAS float*)(L + gur);
    float g = fadd(fadd(r0[0], r0[64]), fadd(r0[260], r0[324])), u = fadd(fadd(r0[128], r0[192]), fadd(r0[388], r0[452]));
    g += dpp<XOR1>(g); u += dpp<XOR1>(u);
    g += dpp<XOR2>(g); u += dpp<XOR2>(u);
    g = rbf(g); u = rbf(u);
    const float y = g * fmaf(g * g, 0.044715f * 0.7978845608028654f * 2.f * LOG2E, 0.7978845608028654f * 2.f * LOG2E);
    const float th = fmaf(-2.f, rcp_f(__builtin_amdgcn_exp2f(y) + 1.f), 1.f);
    const float act = rbf(0.5f * g * (1.f + th)) * u;
    *(LAS unsigned*)(L + actw) = pkr(act);
  }
  bar();
  STAMP(9);
  {
    const LAS v4u* ap = (const LAS v4u*)(L + actr);
    const v4u a0 = ap[0], a1 = ap[1];
    f32x4 d0 = {0.f, 0.f, 0.f, 0.f}, d1 = {0.f, 0.f, 0.f, 0.f};
#pragma unroll
    for (int t = 0; t < 4; ++t) { d0 = mfma(afrag(a0[t], mk), wd[t], d0); d1 = mfma(afrag(a1[t], mk), wd[4 + t], d1); }
    *(LAS f32x4*)(L + trw) = d0 + d1;
    lds_wait();
    pubp(ddst, fadd(*(const LAS float*)(L + trr), *(const LAS float*)(L + trr + 256)), tag);
  }
  STAMP(10);
}

DEV void role_modb(const Params& p, LAS unsigned char* L, int x, int wave, int lane, u64 t0) {
  if (wave >= 5) return;
  const Mask mk = make_mask(lane, true);
  u64 st;
  asm volatile("" : "=v"(st));
  if (wave == 0) {
    int zoff = G_EPOCH;
    asm volatile("" : "+v"(zoff));
    st = __hip_atomic_load((gu64*)p.ws + zoff, RLX_AGENT);
    tag_put(L, st, lane);
  }
  bar();
  const Tag tag = tag_get(L);
  if (GATE_MODB) gate_wait(t0, GATE_MODB);
  ModA ma;
  const int mt = 176 + 8 * wave + x;
  moda_load(p, mt, lane, ma);
  __builtin_amdgcn_sched_barrier(0);
  moda_finish(L, p.ws, mt, lane, ma, mk, (LAS float*)(L + wave * 1024), tag);
  STAMP(2);
}

__global__ void __launch_bounds__(NTHR) adaln_block_kernel(Params p) {
  __shared__ __attribute__((aligned(16))) unsigned char smem[L_END + L_EXTRA];
  LAS unsigned char* L = (LAS unsigned char*)smem;
  const int lane = threadIdx.x & 63, wave = __builtin_amdgcn_readfirstlane(threadIdx.x >> 6);
  const u64 t0 = __builtin_amdgcn_s_memrealtime();
  STAMP_INIT();
  STAMP(0);
  const int x = blockIdx.x & 7, idx = blockIdx.x >> 3;
  if (idx < 22) role_att(p, L, x, idx, wave, lane, t0);
  else if (idx < 31) role_ffn(p, L, x, idx - 22, wave, lane, t0);
  else role_modb(p, L, x, wave, lane, t0);
  STAMP_DUMP();
}

extern "C" void kernel_launch(void* const* d_in, const int* in_sizes, int n_in, void* d_out, int out_size, void* d_ws,
                              size_t ws_size, hipStream_t stream) {
  Params p;
  p.x = (const u16*)d_in[0];
  p.kc = (const u16*)d_in[1];
  p.vc = (const u16*)d_in[2];
  p.wq = (const u16*)d_in[3];
  p.wo = (const u16*)d_in[4];
  p.wg = (const u16*)d_in[6];
  p.wu = (const u16*)d_in[7];
  p.wd = (const u16*)d_in[8];
  p.c = (const u16*)d_in[9];
  p.wmod = (const u16*)d_in[10];
  p.out = (u16*)d_out;
  p.ws = (u64*)d_ws;
  hipLaunchKernelGGL(adaln_block_kernel, dim3(256), dim3(NTHR), 0, stream, p);
}
```

```cpp
#include <hip/hip_runtime.h>
#include <stdint.h>

typedef unsigned short u16;
typedef unsigned long long u64;
typedef unsigned v4u __attribute__((ext_vector_type(4)));
typedef float f32x4 __attribute__((ext_vector_type(4)));
typedef float f32x2 __attribute__((ext_vector_type(2)));
typedef __bf16 bf16x2 __attribute__((ext_vector_type(2)));
typedef short bf16x8 __attribute__((ext_vector_type(8)));
typedef __amdgpu_buffer_rsrc_t rsrc_t;
#define GAS __attribute__((address_space(1)))
#define LAS __attribute__((address_space(3)))
#define DEV __device__ __forceinline__
typedef GAS u64 gu64;
#define RLX_AGENT __ATOMIC_RELAXED, __HIP_MEMORY_SCOPE_AGENT

constexpr int D = 576, SK = 65536, WIN = 4096, DH = 128, FF = 4608, NMOD = 6 * D;
constexpr int NSPLIT = 11;
constexpr int NTHR = 576;
constexpr int SPIN_CAP = 1 << 16;
#define GATE_FFN 790
#define GATE_MODB 200
#define GATE_ATT 140
#define GATE_Q 470
#define GATE_FFN2 0
#define F_N1 0
#define F_D1 2
#define GATE_X2 0
#define POLL_SLEEP 0
#define Q_STAGGER 16
#define GATE_H1 260
#define DN_FIRST 4
#define MODA_HOLD 0
#define GATE_MODB2 120
#define V_N1 0
#define V_D1 2
#define V_D2 24
#define ATT_N1 5
#define ATT_D1 2
#define ATT_D2 24

constexpr int G_EPOCH = 0;
constexpr int G_MOD = 64;
constexpr int G_Q = G_MOD + 4 * NMOD;
constexpr int G_O = G_Q + 32 * 2 * 128;
constexpr int G_WO = G_O + 16 * 8 * NSPLIT * 64;
constexpr int G_X2 = G_WO + 128 * 576;
constexpr int G_DN = G_X2 + 72 * 16;
constexpr int G_END = G_DN + 72 * 576;
constexpr size_t G_STAMP = 1 << 20;

constexpr int L_K = 0;
constexpr int L_OL = 98304;
constexpr int L_Q = L_OL + 9216;
constexpr int L_MX = L_Q + 512;
constexpr int L_LS = L_MX + 64;
constexpr int L_OB = L_LS + 64;
constexpr int L_PF = L_OB + 128;
constexpr int L_WO = L_PF + 256;
constexpr int L_MA = L_WO + 36864;
constexpr int L_TAG = L_MA + 1536 + 16;
constexpr int L_END = L_TAG + 16;
constexpr int F_RED = 0;
constexpr int F_H = 16384;
constexpr int F_H2 = 17664;
constexpr int F_ACT = 20736;
constexpr int F_TR = 21248;
constexpr int F_ST = 27136;

struct Params {
  const u16 *x, *kc, *vc, *wq, *wo, *wg, *wu, *wd, *c, *wmod;
  u16* out;
  u64* ws;
};

#define STAMP(i) do {} while (0)
#define STAMP_INIT() do {} while (0)
#define STAMP_DUMP() do {} while (0)
#define L_EXTRA 0

DEV float bf2f(unsigned b) { return __uint_as_float(b << 16); }
DEV unsigned cvt2(float lo, float hi) {
  const f32x2 v = {lo, hi};
  return __builtin_bit_cast(unsigned, __builtin_convertvector(v, bf16x2));
}
DEV unsigned pkr(float f) { return cvt2(f, f); }
DEV float rbf(float f) { return __uint_as_float(cvt2(f, f) & 0xFFFF0000u); }
struct Tag { unsigned hi, lo, rest; };
DEV Tag make_tag(u64 st) {
  const u64 nx = st * 6364136223846793005ull + 1442695040888963407ull;
  Tag t;
  t.hi = __builtin_amdgcn_readfirstlane((unsigned)(nx >> 32));
  t.rest = __builtin_amdgcn_readfirstlane((unsigned)nx);
  t.lo = t.rest >> 24;
  return t;
}
DEV u64 enc(float v, unsigned hi, unsigned lo) {
  const unsigned u = (__float_as_uint(v) + 0x80u) & 0xFFFFFF00u;
  return ((u64)hi << 32) | (u64)(u | lo);
}
DEV Tag tag_put(LAS unsigned char* L, u64 st, int lane) {
  const Tag t = make_tag(st);
  if (lane == 0) *(LAS v4u*)(L + L_TAG) = v4u{t.hi, t.lo, t.rest, 0u};
  return t;
}
DEV Tag tag_get(const LAS unsigned char* L) {
  const v4u v = *(const LAS v4u*)(L + L_TAG);
  Tag t; t.hi = __builtin_amdgcn_readfirstlane(v[0]); t.lo = __builtin_amdgcn_readfirstlane(v[1]); t.rest = __builtin_amdgcn_readfirstlane(v[2]);
  return t;
}
DEV unsigned or_xor(unsigned a, unsigned b, unsigned c) { return __builtin_amdgcn_bitop3_b32(a, b, c, 0xF6); }
struct Chk {
  unsigned a = 0, b = 0;
  DEV void add(u64 g, const Tag& t) { a = or_xor(a, (unsigned)(g >> 32), t.hi); b = or_xor(b, (unsigned)g, t.lo); }
  DEV bool all_ok() const { return __builtin_amdgcn_ballot_w64((a | (b & 0xFFu)) != 0u) == 0ull; }
};
DEV float gval(u64 g) { return __uint_as_float((unsigned)g & 0xFFFFFF00u); }
DEV void pub(u64* ws, int idx, float v, const Tag& t) { __hip_atomic_store((gu64*)ws + idx, enc(v, t.hi, t.lo), RLX_AGENT); }
DEV void pubp(gu64* dst, float v, const Tag& t) { __hip_atomic_store(dst, enc(v, t.hi, t.lo), RLX_AGENT); }
DEV u64 peek(u64* ws, int idx) { return __hip_atomic_load((gu64*)ws + idx, RLX_AGENT); }
template <int N, class F> DEV void gather(u64* ws, const Tag& t, u64 (&g)[N], F idx) {
  for (int spin = 0; spin < SPIN_CAP; ++spin) {
#pragma unroll
    for (int k = 0; k < N; ++k) g[k] = peek(ws, idx(k));
    Chk c;
#pragma unroll
    for (int k = 0; k < N; ++k) c.add(g[k], t);
    if (c.all_ok()) break;
    if (POLL_SLEEP) __builtin_amdgcn_s_sleep(POLL_SLEEP);
  }
}

struct G2 { v4u v; DEV float lo() const { return __uint_as_float(v[0] & 0xFFFFFF00u); } DEV float hi() const { return __uint_as_float(v[2] & 0xFFFFFF00u); } };
template <int N, class F> DEV void gather2(rsrc_t rw, const Tag& t, G2 (&g)[N], F idx) {
  for (int spin = 0; spin < SPIN_CAP; ++spin) {
    asm volatile("" ::: "memory");
#pragma unroll
    for (int k = 0; k < N; ++k) g[k].v = __builtin_amdgcn_raw_buffer_load_b128(rw, idx(k) * 8, 0, 16);
    unsigned a = 0, b = 0;
#pragma unroll
    for (int k = 0; k < N; ++k) { a = or_xor(or_xor(a, g[k].v[1], t.hi), g[k].v[3], t.hi); b = or_xor(or_xor(b, g[k].v[0], t.lo), g[k].v[2], t.lo); }
    if (__builtin_amdgcn_ballot_w64((a | (b & 0xFFu)) != 0u) == 0ull) break;
    if (POLL_SLEEP) __builtin_amdgcn_s_sleep(POLL_SLEEP);
  }
}
template <int N, class F> DEV void pretouch(LAS unsigned char* L, u64* ws, F idx) {
  const unsigned dump = (unsigned)(uintptr_t)(L + L_PF);
#pragma unroll
  for (int k = 0; k < N; ++k)
    asm volatile("s_mov_b32 m0, %0\n\ts_nop 0\n\tglobal_load_lds_dword %1, off sc1" :: "s"(dump), "v"((gu64*)ws + idx(k)) : "m0", "memory");
}
DEV f32x4 mfma(v4u a, v4u b, f32x4 c) {
  return __builtin_amdgcn_mfma_f32_16x16x32_bf16(__builtin_bit_cast(bf16x8, a), __builtin_bit_cast(bf16x8, b), c, 0, 0, 0);
}
DEV rsrc_t mk_rsrc(const void* p, unsigned bytes) { return __builtin_amdgcn_make_buffer_rsrc((void*)p, 0, (int)bytes, 0x00020000); }
DEV v4u ldb(rsrc_t r, unsigned voff, unsigned soff) { return __builtin_amdgcn_raw_buffer_load_b128(r, (int)voff, (int)soff, 2); }
#define SMALL_AUX 0
DEV unsigned ldb16(rsrc_t r, unsigned voff) { return __builtin_amdgcn_raw_buffer_load_b16(r, (int)voff, 0, SMALL_AUX); }
#define E_AUX 16
DEV u64 load_state(u64* ws) {
  int zoff = G_EPOCH * 8;
  asm volatile("" : "+v"(zoff));
  typedef unsigned v2u_ __attribute__((ext_vector_type(2)));
  const v2u_ v = __builtin_amdgcn_raw_buffer_load_b64(mk_rsrc(ws, 64), zoff, 0, E_AUX);
  return ((u64)v[1] << 32) | v[0];
}
DEV void bar() {
  asm volatile("s_waitcnt lgkmcnt(0)" ::: "memory");
  __builtin_amdgcn_s_barrier();
  asm volatile("" ::: "memory");
}
#define PIN(x) asm volatile("" : "+v"(x))
#define PINS(x) asm volatile("" : "+s"(x))
DEV void lds_wait() { asm volatile("s_waitcnt lgkmcnt(0)" ::: "memory"); }
template <int CTRL> DEV float dpp(float x) {
  return __builtin_bit_cast(float, __builtin_amdgcn_mov_dpp(__builtin_bit_cast(int, x), CTRL, 0xf, 0xf, true));
}
constexpr int ROR8 = 0x128, ROR4 = 0x124, ROR2 = 0x122, ROR1 = 0x121, XOR1 = 0xB1, XOR2 = 0x4E;
DEV float fmx(float a, float b) { float r; asm("v_max_f32 %0, %1, %2\n\ts_nop 1" : "=v"(r) : "v"(a), "v"(b)); return r; }
DEV float fmx3(float a, float b, float c) { float r; asm("v_max3_f32 %0, %1, %2, %3" : "=v"(r) : "v"(a), "v"(b), "v"(c)); return r; }
DEV float fmx3n(float a, float b, float c) { float r; asm("v_max3_f32 %0, %1, %2, %3\n\ts_nop 1" : "=v"(r) : "v"(a), "v"(b), "v"(c)); return r; }
DEV float x16_sum(float x) {
  auto s = __builtin_amdgcn_permlane16_swap(__float_as_uint(x), __float_as_uint(x), false, false);
  x = __uint_as_float(s[0]) + __uint_as_float(s[1]);
  auto t = __builtin_amdgcn_permlane32_swap(__float_as_uint(x), __float_as_uint(x), false, false);
  return __uint_as_float(t[0]) + __uint_as_float(t[1]);
}
DEV float x16_max(float x) {
  auto s = __builtin_amdgcn_permlane16_swap(__float_as_uint(x), __float_as_uint(x), false, false);
  x = fmaxf(__uint_as_float(s[0]), __uint_as_float(s[1]));
  auto t = __builtin_amdgcn_permlane32_swap(__float_as_uint(x), __float_as_uint(x), false, false);
  return fmaxf(__uint_as_float(t[0]), __uint_as_float(t[1]));
}
DEV float row_sum(float x) { x += dpp<ROR8>(x); x += dpp<ROR4>(x); x += dpp<ROR2>(x); x += dpp<ROR1>(x); return x; }
DEV float row_max(float x) { x = fmaxf(x, dpp<ROR8>(x)); x = fmaxf(x, dpp<ROR4>(x)); x = fmaxf(x, dpp<ROR2>(x)); x = fmaxf(x, dpp<ROR1>(x)); return x; }
DEV float wave_sum(float x) { return x16_sum(row_sum(x)); }
constexpr float LOG2E = 1.4426950408889634f;
DEV float fadd(float a, float b) { float r; asm("v_add_f32 %0, %1, %2" : "=v"(r) : "v"(a), "v"(b)); return r; }
DEV float faddn(float a, float b) { float r; asm("v_add_f32 %0, %1, %2\n\ts_nop 1" : "=v"(r) : "v"(a), "v"(b)); return r; }
DEV float exp_f(float x) { return __builtin_amdgcn_exp2f(x * LOG2E); }
DEV float rcp_f(float x) { return __builtin_amdgcn_rcpf(x); }
DEV float pair_pick(float a, float b, int lane) {
  const float a2 = dpp<0x50>(a), b2 = dpp<0x50>(b);
  const float a3 = dpp<0xFA>(a), b3 = dpp<0xFA>(b);
  const float lo = (lane & 1) ? b2 : a2, hi = (lane & 1) ? b3 : a3;
  return (lane & 4) ? hi : lo;
}
DEV float rdlane(float x, int l) { return __uint_as_float(__builtin_amdgcn_readlane(__float_as_uint(x), l)); }
DEV void gate_wait(u64 t0, unsigned ticks) { while (__builtin_amdgcn_s_memrealtime() - t0 < (u64)ticks) __builtin_amdgcn_s_sleep(2); }

struct Mask { unsigned m[4]; };
DEV Mask make_mask(int lane, bool on) {
  Mask k;
  const unsigned h = on ? ((lane & 1) ? 0xFFFF0000u : 0x0000FFFFu) : 0u;
#pragma unroll
  for (int r = 0; r < 4; ++r) k.m[r] = (((lane & 7) >> 1) == r) ? h : 0u;
  return k;
}
DEV v4u afrag(unsigned pp, const Mask& k) { return v4u{pp & k.m[0], pp & k.m[1], pp & k.m[2], pp & k.m[3]}; }

DEV unsigned ln_apply_pk(float x, float mean_b, float rden, float c1, float shift) {
  const float ln = rbf(rbf(x - mean_b) * rden);
  return pkr(rbf(ln * c1) + shift);
}
DEV float ln_rden(float var) { return rcp_f(rbf(__builtin_amdgcn_sqrtf(rbf(rbf(var) + 1e-5f)))); }

DEV void red_store(LAS float* area, f32x4 d, int lane) {
  const int u = (lane >> 3) & 1;
  if ((lane >> 5) == u) *(LAS f32x4*)(area + u * 64 + 8 * (lane & 7) + 4 * ((lane >> 4) & 1)) = d;
}
DEV unsigned red_off(int lane, int dump) {
  const int u = (lane >> 3) & 1;
  return 4 * ((lane >> 5) == u ? u * 64 + 8 * (lane & 7) + 4 * ((lane >> 4) & 1) : dump);
}
DEV unsigned silu_pk(unsigned craw) { const float v = bf2f(craw); return pkr(v * rcp_f(1.f + exp_f(-v))); }

struct WoAddr { unsigned tro, tri, obo, wro; gu64* dst; };
DEV WoAddr wo_prep(u64* ws, int pidx, int wave, int lane) {
  WoAddr a;
  a.tro = L_OL + wave * 640 + red_off(lane, 128); a.tri = L_OL + wave * 640 + lane * 4; a.obo = L_OB + (lane >> 3) * 16; a.wro = L_WO + wave * 4096 + lane * 16;
  a.dst = (gu64*)ws + G_WO + pidx * 576 + wave * 64 + lane;
  PIN(a.tro); PIN(a.tri); PIN(a.obo); PIN(a.wro); PIN(a.dst);
  return a;
}
DEV void wo_phase(LAS unsigned char* L, const WoAddr& a, const Tag& tag, const Mask& mk) {
  bar();
  v4u ob = *(const LAS v4u*)(L + a.obo);
  const LAS v4u* wr = (const LAS v4u*)(L + a.wro);
  v4u w0 = wr[0], w1 = wr[64], w2 = wr[128], w3 = wr[192];
  PIN(ob); PIN(w0); PIN(w1); PIN(w2); PIN(w3);
  __builtin_amdgcn_sched_barrier(0);
  f32x4 d0 = {0.f, 0.f, 0.f, 0.f}, d1 = {0.f, 0.f, 0.f, 0.f};
  d0 = mfma(afrag(ob[0], mk), w0, d0); d1 = mfma(afrag(ob[1], mk), w1, d1);
  d0 = mfma(afrag(ob[2], mk), w2, d0); d1 = mfma(afrag(ob[3], mk), w3, d1);
  *(LAS f32x4*)(L + a.tro) = d0 + d1;
  lds_wait();
  pubp(a.dst, fadd(*(const LAS float*)(L + a.tri), *(const LAS float*)(L + a.tri + 256)), tag);
}

struct ModA { v4u w[18]; unsigned c[3]; };
DEV void moda_load(const Params& p, int mt, int lane, ModA& m) {
  const rsrc_t rm = mk_rsrc(p.wmod, D * NMOD * 2), rc = mk_rsrc(p.c, D * 2);
  const int r0 = (mt & 3) * 144;
#pragma unroll
  for (int k = 0; k < 3; ++k) m.c[k] = ldb16(rc, (r0 + (k < 2 || lane < 16 ? lane + 64 * k : 143)) * 2);
  const unsigned v = (r0 + (lane >> 3)) * (NMOD * 2) + (lane & 7) * 16;
#pragma unroll
  for (int t = 0; t < 18; ++t) m.w[t] = ldb(rm, v, (mt >> 2) * 128 + t * 8 * (NMOD * 2));
}
DEV void moda_finish(LAS unsigned char* L, u64* ws, int mt, int lane, ModA& m, const Mask& mk, LAS float* tr, const Tag& tag) {
  unsigned sp[18];
  {
    const unsigned s0 = silu_pk(m.c[0]), s1 = silu_pk(m.c[1]), s2 = silu_pk(m.c[2]);
#pragma unroll
    for (int t = 0; t < 18; ++t) sp[t] = __builtin_amdgcn_ds_bpermute(((8 * t + (lane >> 3)) & 63) * 4, t < 8 ? s0 : t < 16 ? s1 : s2);
  }
  gu64* dst = (gu64*)ws + G_MOD + (mt & 3) * NMOD + (mt >> 2) * 64 + lane;
  unsigned tro = red_off(lane, 128);
  PIN(dst); PIN(tro);
#pragma unroll
  for (int t = 0; t < 18; ++t) PIN(sp[t]);
  __builtin_amdgcn_sched_barrier(0);
  f32x4 d0 = {0.f, 0.f, 0.f, 0.f}, d1 = {0.f, 0.f, 0.f, 0.f};
#pragma unroll
  for (int t = 0; t < 18; t += 2) {
    d0 = mfma(afrag(sp[t], mk), m.w[t], d0);
    __builtin_amdgcn_sched_barrier(0);
    d1 = mfma(afrag(sp[t + 1], mk), m.w[t + 1], d1);
    __builtin_amdgcn_sched_barrier(0);
  }
  *(LAS f32x4*)((LAS unsigned char*)tr + tro) = d0 + d1;
  lds_wait();
  pubp(dst, fadd(tr[lane], tr[64 + lane]), tag);
}

DEV void role_att(const Params& p, LAS unsigned char* L, int x, int i, int wave, int lane, u64 t0) {
  const int g = 2 * x + (i >= NSPLIT ? 1 : 0), j = i >= NSPLIT ? i - NSPLIT : i;
  const int ntile = j < 8 ? 23 : 24, tile0 = j < 8 ? 23 * j : 184 + 24 * (j - 8);
  const bool owner = j < 8;
  int redc = -1;
  if (j >= 8) redc = 9 * x + (g & 1) * 3 + (j - 8);
  else if (j == 0) redc = 9 * x + 6 + (g & 1);
  else if (j == 1 && (g & 1) == 0) redc = 9 * x + 8;
  u64* ws = p.ws;
  const Mask mk = make_mask(lane, true);
  const int kap = lane >> 4, cc = lane & 15;
  const bool moda = i < 9;

  if (wave < 8) {
    const size_t hb = ((size_t)g * SK + (SK - WIN)) * DH;
    const rsrc_t rk = mk_rsrc(p.kc + hb, WIN * DH * 2), rv = mk_rsrc(p.vc + hb, WIN * DH * 2);
    const bool three = wave + 16 < ntile;
    const unsigned tso = (tile0 + wave) * 4096;
    const unsigned ldsk = (unsigned)(uintptr_t)(L + L_K) + wave * 3 * 4096;
    unsigned vk[4];
#pragma unroll
    for (int q = 0; q < 4; ++q) vk[q] = (4 * q + kap) * 256 + ((cc ^ (4 * q + kap)) << 4);
#define KDMA(s, q) asm volatile("s_mov_b32 m0, %0\n\ts_nop 0\n\tbuffer_load_dwordx4 %1, %2, %3 offen nt lds" \
                     :: "s"(ldsk + (s) * 4096 + (q) * 1024), "v"(vk[q]), "s"(rk), "s"(tso + (s) * 32768) : "m0", "memory")
#define THR(n) do { if ((n) < ATT_N1) { if ((n) >= ATT_D1 && moda) asm volatile("s_waitcnt vmcnt(%0)" :: "n"(ATT_D1 - 1) : "memory"); } \
                     else if ((n) >= ATT_D2) asm volatile("s_waitcnt vmcnt(%0)" :: "n"(ATT_D2 - 1) : "memory"); } while (0)
#define THR_DEPTH(n) ((n) < ATT_N1 ? ATT_D1 : ATT_D2)
#define THRV(t) asm volatile("s_waitcnt vmcnt(%0)" :: "n"(((t) < V_N1 ? V_D1 : V_D2) - 1) : "memory")
    if (moda) __builtin_amdgcn_s_barrier();
    __builtin_amdgcn_s_setprio(2);
    KDMA(0, 0);
    if (GATE_ATT) gate_wait(t0, GATE_ATT);
    if (MODA_HOLD && moda) __builtin_amdgcn_s_barrier();
    THR(1); KDMA(0, 1); THR(2); KDMA(0, 2); THR(3); KDMA(0, 3);
    THR(4); KDMA(1, 0); THR(5); KDMA(1, 1); THR(6); KDMA(1, 2); THR(7); KDMA(1, 3);
    if (three) { THR(8); KDMA(2, 0); THR(9); KDMA(2, 1); THR(10); KDMA(2, 2); THR(11); KDMA(2, 3); }
#undef KDMA
    __builtin_amdgcn_s_setprio(0);
    v4u vr[12];
    const unsigned vv = kap * 1024 + cc * 16;
#pragma unroll
    for (int t = 0; t < 8; ++t) { THRV(t); vr[t] = ldb(rv, vv + (t & 3) * 256, tso + (t >> 2) * 32768); }
    if (three) {
#pragma unroll
      for (int t = 8; t < 12; ++t) { THRV(t); vr[t] = ldb(rv, vv + (t & 3) * 256, tso + 65536); }
    } else {
#pragma unroll
      for (int t = 8; t < 12; ++t) vr[t] = v4u{0u, 0u, 0u, 0u};
    }
#undef THR
#undef THRV
#undef THR_DEPTH
    __builtin_amdgcn_sched_barrier(0);
    STAMP(1);
    if (three) asm volatile("s_waitcnt vmcnt(12)" ::: "memory");
    else asm volatile("s_waitcnt vmcnt(8)" ::: "memory");
    v4u kf[12];
#pragma unroll
    for (int s = 0; s < 3; ++s)
#pragma unroll
      for (int ks = 0; ks < 4; ++ks)
        kf[s * 4 + ks] = *(const LAS v4u*)(L + L_K + (wave * 3 + (s < 2 || three ? s : 0)) * 4096 + cc * 256 + (((4 * ks + kap) ^ cc) << 4));
#pragma unroll
    for (int t = 0; t < 12; ++t) PIN(kf[t]);
    const int head = (lane >> 3) & 1;
    const int me = (wave & 3) * 64 + lane;
    unsigned qsrc = L_Q + head * 256 + kap * 16, mxw = L_MX + (head * 8 + wave) * 4, mxr = L_MX + head * 32, olw = L_OL + wave * 1024 + lane * 16;
    unsigned msrc = L_OL + ((16 * (((me >> 7) * 8 + (me & 7)) >> 2) + ((me & 127) >> 3)) * 4 + (me & 3)) * 4;
    gu64* mdst = (gu64*)ws + G_O + ((g * 8 + (me >> 5)) * NSPLIT + j) * 64 + (me & 31);
    bool lead = (lane & 0x37) == 0;
    PIN(qsrc); PIN(mxw); PIN(mxr); PIN(olw); PIN(msrc); PIN(mdst);
    STAMP(3);
    bar();
    STAMP(2);
    v4u qf[4];
#pragma unroll
    for (int ks = 0; ks < 4; ++ks) qf[ks] = *(const LAS v4u*)(L + qsrc + ks * 64);
#pragma unroll
    for (int ks = 0; ks < 4; ++ks) PIN(qf[ks]);
    __builtin_amdgcn_sched_barrier(0);
    f32x4 acc[3] = {{0.f, 0.f, 0.f, 0.f}, {0.f, 0.f, 0.f, 0.f}, {0.f, 0.f, 0.f, 0.f}};
#pragma unroll
    for (int ks = 0; ks < 4; ++ks)
#pragma unroll
      for (int s = 0; s < 3; ++s) acc[s] = mfma(kf[s * 4 + ks], qf[ks], acc[s]);
    __builtin_amdgcn_sched_barrier(0);
    float sc[12];
#pragma unroll
    for (int t = 0; t < 12; t += 2) {
      const unsigned u = cvt2(acc[t >> 2][t & 3], acc[t >> 2][(t & 3) + 1]);
      const unsigned w = cvt2(__uint_as_float(u << 16) * 0.08838834764831845f, __uint_as_float(u & 0xFFFF0000u) * 0.08838834764831845f);
      sc[t] = __uint_as_float(w << 16);
      sc[t + 1] = __uint_as_float(w & 0xFFFF0000u);
    }
    if (!three) {
#pragma unroll
      for (int t = 8; t < 12; ++t) sc[t] = -INFINITY;
    }
    float mx = fmx3n(fmx3(fmx3(sc[0], sc[1], sc[2]), fmx3(sc[3], sc[4], sc[5]), fmx3(sc[6], sc[7], sc[8])), fmx3(sc[9], sc[10], sc[11]), sc[0]);
    mx = x16_max(mx);
    if (lead) *(LAS float*)(L + mxw) = mx;
    bar();
    float m;
    {
      const f32x4 a = *(const LAS f32x4*)(L + mxr), b = *(const LAS f32x4*)(L + mxr + 16);
      m = fmx3(fmx3(a[0], a[1], a[2]), fmx3(a[3], b[0], b[1]), fmx(b[2], b[3]));
    }
    v4u af[12];
    float ls0 = 0.f, ls1 = 0.f;
    const float mneg = -m * LOG2E;
#pragma unroll
    for (int t = 0; t < 12; t += 2) {
      const float e0 = __builtin_amdgcn_exp2f(fmaf(sc[t], LOG2E, mneg)), e1 = __builtin_amdgcn_exp2f(fmaf(sc[t + 1], LOG2E, mneg));
      ls0 += e0; ls1 += e1;
      af[t] = afrag(pkr(e0), mk); af[t + 1] = afrag(pkr(e1), mk);
    }
    const float ls = x16_sum(ls0 + ls1);
    if (lead) *(LAS float*)(L + mxw + 64) = ls;
    const Tag tag = tag_get(L);
    __builtin_amdgcn_sched_barrier(0);
    STAMP(4);
    f32x4 o0 = {0.f, 0.f, 0.f, 0.f}, o1 = {0.f, 0.f, 0.f, 0.f};
#pragma unroll
    for (int t = 0; t < 12; t += 2) { o0 = mfma(af[t], vr[t], o0); o1 = mfma(af[t + 1], vr[t + 1], o1); }
    *(LAS f32x4*)(L + olw) = o0 + o1;
    STAMP(5);
    bar();
    if (wave < 4) {
      const LAS float* src = (const LAS float*)(L + msrc);
      float s[8];
#pragma unroll
      for (int w = 0; w < 8; ++w) s[w] = src[w * 256];
      pubp(mdst, fadd(fadd(fadd(s[0], s[1]), fadd(s[2], s[3])), fadd(fadd(s[4], s[5]), fadd(s[6], s[7]))), tag);
    } else if (wave == 4 && lane < 16) {
      const int u = lane >> 1, hh = u >> 2;
      const f32x4 a = *(const LAS f32x4*)(L + ((lane & 1) ? L_LS : L_MX) + hh * 32), b = *(const LAS f32x4*)(L + ((lane & 1) ? L_LS : L_MX) + hh * 32 + 16);
      const float vs = ((a[0] + a[1]) + (a[2] + a[3])) + ((b[0] + b[1]) + (b[2] + b[3]));
      const float vm = fmaxf(fmaxf(fmaxf(a[0], a[1]), fmaxf(a[2], a[3])), fmaxf(fmaxf(b[0], b[1]), fmaxf(b[2], b[3])));
      pub(ws, G_O + ((g * 8 + u) * NSPLIT + j) * 64 + 32 + (lane & 1), (lane & 1) ? vs : vm, tag);
    }
    STAMP(6);
    if (owner) {
      wo_phase(L, wo_prep(ws, g * 8 + j, wave, lane), tag, mk);
      STAMP(8);
    }
  } else {
    __builtin_amdgcn_s_setprio(3);
    Tag tag;
    {
      if (moda) {
        u64 st = load_state(ws);
        ModA ma;
        moda_load(p, 9 * x + i, lane, ma);
        __builtin_amdgcn_sched_barrier(0);
        __builtin_amdgcn_s_barrier();
        asm volatile("" : "+v"(st));
        tag = tag_put(L, st, lane);
        moda_finish(L, ws, 9 * x + i, lane, ma, mk, (LAS float*)(L + L_MA), tag);
        STAMP(9);
        if (MODA_HOLD) __builtin_amdgcn_s_barrier();
      } else {
        tag = tag_put(L, load_state(ws), lane);
      }
    }
    float xr = 0.f;
    if (redc >= 0) xr = bf2f(p.x[redc * 8 + (lane & 7)]);
    if (GATE_Q) gate_wait(t0, GATE_Q);
    const rsrc_t rw = mk_rsrc(ws, G_END * 8);
    {
      G2 gq[4];
      gather2<4>(rw, tag, gq, [&](int k) { return G_Q + ((2 * g + (k >> 1)) * 2 + (k & 1)) * 128 + 2 * lane; });
      ((LAS unsigned*)(L + L_Q))[lane] = cvt2(fadd(gq[0].lo(), gq[1].lo()), fadd(gq[0].hi(), gq[1].hi()));
      ((LAS unsigned*)(L + L_Q))[64 + lane] = cvt2(fadd(gq[2].lo(), gq[3].lo()), fadd(gq[2].hi(), gq[3].hi()));
    }
    STAMP(1);
    if (owner) {
      const int ob = G_O + (g * 8 + j) * NSPLIT * 64;
      pretouch<1>(L, ws, [&](int) { const int q_ = lane < 33 ? lane : 32; return ob + (q_ / 3) * 64 + (q_ % 3) * 16; });
    }
    if (redc >= 0) {
      pretouch<2>(L, ws, [&](int s) { return G_WO + (s * 64 + lane) * 576 + redc * 8; });
      pretouch<2>(L, ws, [&](int s) { const int f_ = s * 64 + lane; return G_DN + (f_ < 72 ? f_ : 71) * 576 + redc * 8; });
    }
    bar();
    if (owner) {
      const rsrc_t rwo = mk_rsrc(p.wo, 4096 * D * 2);
      const unsigned wo_v = (lane >> 3) * (D * 2) + (lane & 7) * 16;
      const unsigned wo_s = (g * 256 + j * 32) * (D * 2), ldsw = (unsigned)(uintptr_t)(L + L_WO);
#pragma unroll
      for (int w = 0; w < 9; ++w)
#pragma unroll
        for (int t = 0; t < 4; ++t)
          asm volatile("s_mov_b32 m0, %0\n\ts_nop 0\n\tbuffer_load_dwordx4 %1, %2, %3 offen nt lds"
                       :: "s"(ldsw + (w * 4 + t) * 1024), "v"(wo_v), "s"(rwo), "s"(wo_s + w * 128 + t * 8 * (D * 2)) : "m0", "memory");
    }
    bar();
    bar();
    STAMP(2);
    if (owner) {
      const int ob = G_O + (g * 8 + j) * NSPLIT * 64;
      const int k2 = kap < 3 ? 8 + kap : 10;
      unsigned obw = L_OB + (((2 * cc) & 7) * 4 + (cc >> 2)) * 4;
      float live = kap < 3 ? 1.f : 0.f;
      PIN(obw); PIN(live);
      const WoAddr wa = wo_prep(ws, g * 8 + j, 8, lane);
      G2 go[6];
      gather2<6>(rw, tag, go, [&](int k) { const int sp = k % 3 == 0 ? kap : k % 3 == 1 ? 4 + kap : k2; return ob + sp * 64 + (k < 3 ? 2 * cc : 32); });
      STAMP(3);
      const float M = x16_max(fmaxf(fmaxf(go[3].lo(), go[4].lo()), go[5].lo()));
      const float Mn = -M * LOG2E;
      const float w0 = __builtin_amdgcn_exp2f(fmaf(go[3].lo(), LOG2E, Mn)), w1 = __builtin_amdgcn_exp2f(fmaf(go[4].lo(), LOG2E, Mn));
      const float w2 = __builtin_amdgcn_exp2f(fmaf(go[5].lo(), LOG2E, Mn)) * live;
      const float Ls = x16_sum(fmaf(w2, go[5].hi(), fmaf(w1, go[4].hi(), w0 * go[3].hi())));
      const float o0 = x16_sum(fmaf(w2, go[2].lo(), fmaf(w1, go[1].lo(), w0 * go[0].lo())));
      const float o1 = x16_sum(fmaf(w2, go[2].hi(), fmaf(w1, go[1].hi(), w0 * go[0].hi())));
      const float rl = rcp_f(Ls);
      *(LAS unsigned*)(L + obw) = pkr(o0 * rl);
      *(LAS unsigned*)(L + obw + 16) = pkr(o1 * rl);
      asm volatile("s_waitcnt vmcnt(0)" ::: "memory");
      wo_phase(L, wa, tag, mk);
      STAMP(4);
    }
    if (redc >= 0) {
      u64 gg[8];
      gather<8>(ws, tag, gg, [&](int k) { return G_MOD + (k & 3) * NMOD + (k < 4 ? 2 : 5) * D + redc * 8 + (lane & 7); });
      float ga = rbf(fadd(fadd(gval(gg[0]), gval(gg[1])), fadd(gval(gg[2]), gval(gg[3]))));
      float gb = rbf(fadd(fadd(gval(gg[4]), gval(gg[5])), fadd(gval(gg[6]), gval(gg[7]))));
      gu64* xdst = (gu64*)ws + G_X2 + redc * 16 + (lane & 7);
      GAS u16* odst = (GAS u16*)p.out + redc * 8 + (lane & 7);
      PIN(ga); PIN(gb); PIN(xr); PIN(xdst); PIN(odst);
      float x2;
      {
        G2 gw[8];
        gather2<8>(rw, tag, gw, [&](int s) { return G_WO + (s * 16 + (lane >> 2)) * 576 + redc * 8 + 2 * (lane & 3); });
        STAMP(5);
        float t0_ = faddn(fadd(fadd(gw[0].lo(), gw[1].lo()), fadd(gw[2].lo(), gw[3].lo())), fadd(fadd(gw[4].lo(), gw[5].lo()), fadd(gw[6].lo(), gw[7].lo())));
        float t1_ = faddn(fadd(fadd(gw[0].hi(), gw[1].hi()), fadd(gw[2].hi(), gw[3].hi())), fadd(fadd(gw[4].hi(), gw[5].hi()), fadd(gw[6].hi(), gw[7].hi())));
        t0_ += dpp<ROR8>(t0_); t1_ += dpp<ROR8>(t1_); t0_ += dpp<ROR4>(t0_); t1_ += dpp<ROR4>(t1_);
        t0_ = x16_sum(t0_); t1_ = x16_sum(t1_);
        const float ts = pair_pick(t0_, t1_, lane);
        x2 = rbf(xr + rbf(ga * rbf(ts)));
        if (lane < 8) pubp(xdst, x2, tag);
      }
      STAMP(6);
      {
        G2 gd[5];
        float w4 = lane < 32 ? 1.f : 0.f;
        PIN(w4);
        gather2<5>(rw, tag, gd, [&](int s) { const int f_ = s * 16 + (lane >> 2); return G_DN + (f_ < 72 ? f_ : 71) * 576 + redc * 8 + 2 * (lane & 3); });
        STAMP(7);
        float t0_ = fmaf(w4, gd[4].lo(), fadd(fadd(gd[0].lo(), gd[1].lo()), fadd(gd[2].lo(), gd[3].lo())));
        float t1_ = fmaf(w4, gd[4].hi(), fadd(fadd(gd[0].hi(), gd[1].hi()), fadd(gd[2].hi(), gd[3].hi())));
        t0_ += dpp<ROR8>(t0_); t1_ += dpp<ROR8>(t1_); t0_ += dpp<ROR4>(t0_); t1_ += dpp<ROR4>(t1_);
        t0_ = x16_sum(t0_); t1_ = x16_sum(t1_);
        const float ts = pair_pick(t0_, t1_, lane);
        const unsigned o = pkr(x2 + rbf(gb * rbf(ts)));
        if (lane < 8) *odst = (u16)o;
        if (redc == 0 && lane == 0) __hip_atomic_store((gu64*)ws + G_EPOCH, ((u64)tag.hi << 32) | (u64)tag.rest, RLX_AGENT);
      }
      STAMP(8);
    }
  }
}

DEV void role_ffn(const Params& p, LAS unsigned char* L, int x, int i, int wave, int lane, u64 t0) {
  const int f = 9 * x + i;
  u64* ws = p.ws;
  const Mask mk = make_mask(lane, true);
  LAS float* red = (LAS float*)(L + F_RED);
  const rsrc_t rm = mk_rsrc(p.wmod, D * NMOD * 2), rc = mk_rsrc(p.c, D * 2), rx = mk_rsrc(p.x, D * 2);
  const int hq = 4 * x + (i >> 1), qr = i & 1;
  const rsrc_t rw = mk_rsrc(ws, G_END * 8);
  Tag tag;
  if (i < 8) {
    v4u wa[12];
    if (wave >= 3) {
      const rsrc_t rq = mk_rsrc(p.wq, D * 4096 * 2);
      const unsigned v = (lane >> 4) * 8192 + (lane & 15) * 16, s = (qr * 288 + (wave - 3) * 48) * 8192 + hq * 256;
#pragma unroll
      for (int t = 0; t < 12; ++t) wa[t] = ldb(rq, v, s + t * 4 * 8192);
      __builtin_amdgcn_sched_barrier(0);
      STAMP(1);
    } else {
      __builtin_amdgcn_s_setprio(3);
      const u64 st = load_state(ws);
      float xv[9];
#pragma unroll
      for (int k = 0; k < 9; ++k) xv[k] = bf2f(ldb16(rx, (lane + 64 * k) * 2));
      const int p0 = wave * 64 + lane, pr = p0 < 144 ? p0 : 143;
      const unsigned xo = __builtin_amdgcn_raw_buffer_load_b32(rx, (qr * 288 + 2 * pr) * 2, 0, 0);
      float s = 0.f;
#pragma unroll
      for (int k = 0; k < 9; ++k) s += xv[k];
      const float mf = wave_sum(s) * (1.f / D);
      float v = 0.f;
#pragma unroll
      for (int k = 0; k < 9; ++k) { const float d = xv[k] - mf; v = fmaf(d, d, v); }
      const float rden = ln_rden(wave_sum(v) * (1.f / D)), mean = rbf(mf);
      tag = wave == 0 ? tag_put(L, st, lane) : make_tag(st);
      const int hw = pr / 24, hr = 2 * pr - hw * 48;
      unsigned hdst = F_H + ((hw * 4 + (hr & 3)) * 12 + (hr >> 2)) * 4;
      float ln0 = rbf(rbf(__uint_as_float(xo << 16) - mean) * rden), ln1 = rbf(rbf(__uint_as_float(xo & 0xFFFF0000u) - mean) * rden);
      PIN(hdst); PIN(ln0); PIN(ln1);
      STAMP(2);
      if (GATE_H1) gate_wait(t0, GATE_H1);
      G2 ga[8];
      gather2<8>(rw, tag, ga, [&](int k) { return G_MOD + (k & 3) * NMOD + (k >> 2) * D + qr * 288 + 2 * pr; });
      STAMP(3);
      const float sa0 = rbf(fadd(fadd(ga[0].lo(), ga[1].lo()), fadd(ga[2].lo(), ga[3].lo()))), sa1 = rbf(fadd(fadd(ga[0].hi(), ga[1].hi()), fadd(ga[2].hi(), ga[3].hi())));
      const float ca0 = rbf(fadd(fadd(ga[4].lo(), ga[5].lo()), fadd(ga[6].lo(), ga[7].lo()))), ca1 = rbf(fadd(fadd(ga[4].hi(), ga[5].hi()), fadd(ga[6].hi(), ga[7].hi())));
      *(LAS unsigned*)(L + hdst) = pkr(rbf(ln0 * rbf(1.f + ca0)) + sa0);
      *(LAS unsigned*)(L + hdst + 48) = pkr(rbf(ln1 * rbf(1.f + ca1)) + sa1);
      __builtin_amdgcn_s_setprio(0);
    }
    Mask mq = make_mask(lane, (lane & 8) == 0);
    const int qe = (wave & 1) * 64 + lane;
    unsigned hsrc = F_H + ((wave - 3) * 4 + (lane >> 4)) * 48, qst = F_RED + 4608 + (wave - 3) * 1024 + lane * 16;
    unsigned qsrc = F_RED + 4608 + ((16 * ((qe & 7) >> 2) + (qe >> 3)) * 4 + (qe & 3)) * 4;
    gu64* qdst = (gu64*)ws + G_Q + (hq * 2 + qr) * 128 + qe;
    PIN(mq.m[0]); PIN(mq.m[1]); PIN(mq.m[2]); PIN(mq.m[3]); PIN(hsrc); PIN(qst); PIN(qsrc); PIN(qdst);
    bar();
    if (wave >= 3) {
      const LAS v4u* hp = (const LAS v4u*)(L + hsrc);
      const v4u h0 = hp[0], h1 = hp[1], h2 = hp[2];
      f32x4 d0 = {0.f, 0.f, 0.f, 0.f}, d1 = {0.f, 0.f, 0.f, 0.f};
#pragma unroll
      for (int t = 0; t < 4; ++t) { d0 = mfma(afrag(h0[t], mq), wa[t], d0); d1 = mfma(afrag(h1[t], mq), wa[4 + t], d1); }
#pragma unroll
      for (int t = 0; t < 4; t += 2) { d0 = mfma(afrag(h2[t], mq), wa[8 + t], d0); d1 = mfma(afrag(h2[t + 1], mq), wa[9 + t], d1); }
      *(LAS f32x4*)(L + qst) = d0 + d1;
    }
    bar();
    if (wave < 2) {
      const LAS float* src = (const LAS float*)(L + qsrc);
      pubp(qdst, fadd(fadd(fadd(src[0], src[256]), fadd(src[512], src[768])), fadd(src[1024], src[1280])), tag);
    }
  } else {
    if (wave == 0) tag_put(L, load_state(ws), lane);
    if (GATE_MODB2) gate_wait(t0, GATE_MODB2);
    ModA ma;
    if (wave < 6) moda_load(p, 72 + 6 * x + wave, lane, ma);
    __builtin_amdgcn_sched_barrier(0);
    bar();
    if (wave < 6) {
      tag = tag_get(L);
      moda_finish(L, ws, 72 + 6 * x + wave, lane, ma, mk, (LAS float*)(L + F_RED + wave * 1024), tag);
    }
  }
  tag = tag_get(L);
  STAMP(4);
  pretouch<1>(L, ws, [&](int) { const int e = wave * 64 + lane; return G_X2 + (e >> 3) * 16 + (e & 7); });
  const bool slow_tile = ((((unsigned)(uintptr_t)p.wg >> 7) + f) & 7) == 3;
  if (!slow_tile) gate_wait(t0, GATE_FFN);
#define THRF(n) do { if ((n) < F_N1 && !slow_tile && __builtin_amdgcn_s_memrealtime() - t0 < (u64)GATE_FFN2) asm volatile("s_waitcnt vmcnt(%0)" :: "n"(F_D1 - 1) : "memory"); } while (0)
  v4u wg[9], wu[9], wd[8];
  if (wave < 8) {
    const rsrc_t rg = mk_rsrc(p.wg, D * FF * 2), ru = mk_rsrc(p.wu, D * FF * 2);
    const unsigned v = (lane >> 3) * (FF * 2) + (lane & 7) * 16, s = wave * 72 * (FF * 2) + f * 128;
#pragma unroll
    for (int t = 0; t < 9; ++t) { THRF(2 * t); wg[t] = ldb(rg, v, s + t * 8 * (FF * 2)); THRF(2 * t + 1); wu[t] = ldb(ru, v, s + t * 8 * (FF * 2)); }
  }
  {
    const rsrc_t rd = mk_rsrc(p.wd, FF * D * 2);
    const unsigned v = (lane >> 3) * (D * 2) + (lane & 7) * 16, s = f * 64 * (D * 2) + wave * 128;
#pragma unroll
    for (int t = 0; t < DN_FIRST; ++t) { THRF(18 + t); wd[t] = ldb(rd, v, s + t * 8 * (D * 2)); }
  }
  __builtin_amdgcn_sched_barrier(0);
  STAMP(5);
  unsigned h2w, h2r, stw, str_, gur, guw0, actw, actr, trw, trr;
  gu64* ddst;
  {
    const int e = wave * 64 + lane, hw = e / 72, rem = e - hw * 72, c = (wave & 3) * 16 + (lane >> 2);
    h2w = F_H2 + ((hw * 8 + (rem & 7)) * 12 + (rem >> 3)) * 4;
    h2r = F_H2 + ((wave & 7) * 8 + (lane >> 3)) * 48;
    stw = F_ST + wave * 8;
    str_ = F_ST + ((lane & 15) < 9 ? (lane & 15) : 9) * 8;
    guw0 = F_RED + (wave & 7) * 1040 + red_off(lane, 256);
    gur = F_RED + ((lane & 3) * 520 + c) * 4;
    actw = F_ACT + ((lane & 3) == 0 ? (c & 7) * 8 + (c >> 3) : 64) * 4;
    actr = F_ACT + (lane >> 3) * 32;
    trw = F_TR + wave * 640 + red_off(lane, 128);
    trr = F_TR + wave * 640 + lane * 4;
    ddst = (gu64*)ws + G_DN + f * 576 + wave * 64 + lane;
    if (wave == 0 && lane == 0) *(LAS f32x2*)(L + F_ST + 72) = f32x2{0.f, 0.f};
  }
  PIN(h2w); PIN(h2r); PIN(stw); PIN(str_); PIN(guw0); PIN(gur); PIN(actw); PIN(actr); PIN(trw); PIN(trr); PIN(ddst);
  if (GATE_X2) gate_wait(t0, GATE_X2);
  {
    const int e = wave * 64 + lane;
    u64 gs[9];
    bool have_x2 = false;
    for (int spin = 0; spin < SPIN_CAP; ++spin) {
#pragma unroll
      for (int k = 0; k < 8; ++k) gs[k] = peek(ws, G_MOD + (k & 3) * NMOD + (k < 4 ? 3 : 4) * D + e);
      gs[8] = peek(ws, G_X2 + (e >> 3) * 16 + (e & 7));
      Chk c, cx;
#pragma unroll
      for (int k = 0; k < 8; ++k) c.add(gs[k], tag);
      cx.add(gs[8], tag);
      have_x2 = cx.all_ok();
      if (c.all_ok()) break;
    }
    float sb = rbf(fadd(fadd(gval(gs[0]), gval(gs[1])), fadd(gval(gs[2]), gval(gs[3]))));
    float c1b = rbf(1.f + rbf(fadd(fadd(gval(gs[4]), gval(gs[5])), fadd(gval(gs[6]), gval(gs[7])))));
    float on = (lane & 15) < 9 ? 64.f : 0.f;
    PIN(sb); PIN(c1b); PIN(on);
    STAMP(6);
    u64 gx[1] = {gs[8]};
    if (!have_x2) gather<1>(ws, tag, gx, [&](int) { return G_X2 + (e >> 3) * 16 + (e & 7); });
    STAMP(7);
    const float xv = gval(gx[0]);
    const float sh = __uint_as_float(__builtin_amdgcn_readfirstlane(__float_as_uint(xv)));
    const float d = xv - sh;
    float s1 = d, s2 = d * d;
    s1 += dpp<ROR8>(s1); s2 += dpp<ROR8>(s2); s1 += dpp<ROR4>(s1); s2 += dpp<ROR4>(s2);
    s1 += dpp<ROR2>(s1); s2 += dpp<ROR2>(s2); s1 += dpp<ROR1>(s1); s2 += dpp<ROR1>(s2);
    s1 = x16_sum(s1); s2 = x16_sum(s2);
    const float dm = s1 * (1.f / 64.f);
    if (lane == 0) *(LAS f32x2*)(L + stw) = f32x2{sh + dm, fmaf(-dm, s1, s2)};
    bar();
    const f32x2 st = *(const LAS f32x2*)(L + str_);
    {
      const rsrc_t rd = mk_rsrc(p.wd, FF * D * 2);
      const unsigned v = (lane >> 3) * (D * 2) + (lane & 7) * 16, s = f * 64 * (D * 2) + wave * 128;
#pragma unroll
      for (int t = DN_FIRST; t < 8; ++t) wd[t] = ldb(rd, v, s + t * 8 * (D * 2));
      __builtin_amdgcn_sched_barrier(0);
    }
    const float mf = row_sum(st[0]) * (1.f / 9.f);
    const float dv = st[0] - mf;
    const float m2 = row_sum(fmaf(on * dv, dv, st[1]));
    *(LAS unsigned*)(L + h2w) = ln_apply_pk(xv, rbf(mf), ln_rden(m2 * (1.f / D)), c1b, sb);
  }
  bar();
  STAMP(8);
  if (wave < 8) {
    const LAS v4u* hp = (const LAS v4u*)(L + h2r);
    const v4u h0 = hp[0], h1 = hp[1];
    const unsigned h8 = ((const LAS unsigned*)hp)[8];
    f32x4 dg = {0.f, 0.f, 0.f, 0.f}, du = {0.f, 0.f, 0.f, 0.f};
#pragma unroll
    for (int t = 0; t < 9; ++t) {
      const v4u a = afrag(t < 4 ? h0[t & 3] : t < 8 ? h1[t & 3] : h8, mk);
      dg = mfma(a, wg[t], dg);
      du = mfma(a, wu[t], du);
    }
    *(LAS f32x4*)(L + guw0) = dg;
    *(LAS f32x4*)(L + guw0 + ((lane >> 5) == ((lane >> 3) & 1) ? 512 : 0)) = du;
  }
  bar();
  if (wave < 4) {
    const LAS float* r0 = (const LAS float*)(L + gur);
    float g = faddn(fadd(r0[0], r0[64]), fadd(r0[260], r0[324])), u = faddn(fadd(r0[128], r0[192]), fadd(r0[388], r0[452]));
    g += dpp<XOR1>(g); u += dpp<XOR1>(u);
    g += dpp<XOR2>(g); u += dpp<XOR2>(u);
    g = rbf(g); u = rbf(u);
    const float y = g * fmaf(g * g, 0.044715f * 0.7978845608028654f * 2.f * LOG2E, 0.7978845608028654f * 2.f * LOG2E);
    const float th = fmaf(-2.f, rcp_f(__builtin_amdgcn_exp2f(y) + 1.f), 1.f);
    const float act = rbf(0.5f * g * (1.f + th)) * u;
    *(LAS unsigned*)(L + actw) = pkr(act);
  }
  bar();
  STAMP(9);
  {
    const LAS v4u* ap = (const LAS v4u*)(L + actr);
    const v4u a0 = ap[0], a1 = ap[1];
    f32x4 d0 = {0.f, 0.f, 0.f, 0.f}, d1 = {0.f, 0.f, 0.f, 0.f};
#pragma unroll
    for (int t = 0; t < 4; ++t) { d0 = mfma(afrag(a0[t], mk), wd[t], d0); d1 = mfma(afrag(a1[t], mk), wd[4 + t], d1); }
    *(LAS f32x4*)(L + trw) = d0 + d1;
    lds_wait();
    pubp(ddst, fadd(*(const LAS float*)(L + trr), *(const LAS float*)(L + trr + 256)), tag);
  }
  STAMP(10);
}

DEV void role_modb(const Params& p, LAS unsigned char* L, int x, int wave, int lane, u64 t0) {
  const Mask mk = make_mask(lane, true);
  if (wave == 0) tag_put(L, load_state(p.ws), lane);
  bar();
  const Tag tag = tag_get(L);
  gate_wait(t0, GATE_MODB);
  for (int k = wave; k < 12; k += 9) {
    ModA ma;
    moda_load(p, 120 + x + 8 * k, lane, ma);
    __builtin_amdgcn_sched_barrier(0);
    moda_finish(L, p.ws, 120 + x + 8 * k, lane, ma, mk, (LAS float*)(L + wave * 1024), tag);
  }
  STAMP(2);
}

__global__ void __launch_bounds__(NTHR) adaln_block_kernel(Params p) {
  __shared__ __attribute__((aligned(16))) unsigned char smem[L_END + L_EXTRA];
  LAS unsigned char* L = (LAS unsigned char*)smem;
  const int lane = threadIdx.x & 63, wave = __builtin_amdgcn_readfirstlane(threadIdx.x >> 6);
  const u64 t0 = __builtin_amdgcn_s_memrealtime();
  STAMP_INIT();
  STAMP(0);
  const int x = blockIdx.x & 7, idx = blockIdx.x >> 3;
  if (idx < 22) role_att(p, L, x, idx, wave, lane, t0);
  else if (idx < 31) role_ffn(p, L, x, idx - 22, wave, lane, t0);
  else role_modb(p, L, x, wave, lane, t0);
  STAMP_DUMP();
}

extern "C" void kernel_launch(void* const* d_in, const int* in_sizes, int n_in, void* d_out, int out_size, void* d_ws,
                              size_t ws_size, hipStream_t stream) {
  Params p;
  p.x = (const u16*)d_in[0];
  p.kc = (const u16*)d_in[1];
  p.vc = (const u16*)d_in[2];
  p.wq = (const u16*)d_in[3];
  p.wo = (const u16*)d_in[4];
  p.wg = (const u16*)d_in[6];
  p.wu = (const u16*)d_in[7];
  p.wd = (const u16*)d_in[8];
  p.c = (const u16*)d_in[9];
  p.wmod = (const u16*)d_in[10];
  p.out = (u16*)d_out;
  p.ws = (u64*)d_ws;
  hipLaunchKernelGGL(adaln_block_kernel, dim3(256), dim3(NTHR), 0, stream, p);
}
```

```cpp
#include <hip/hip_runtime.h>
#include <stdint.h>

typedef unsigned short u16;
typedef unsigned long long u64;
typedef unsigned v4u __attribute__((ext_vector_type(4)));
typedef float f32x4 __attribute__((ext_vector_type(4)));
typedef float f32x2 __attribute__((ext_vector_type(2)));
typedef __bf16 bf16x2 __attribute__((ext_vector_type(2)));
typedef short bf16x8 __attribute__((ext_vector_type(8)));
typedef __amdgpu_buffer_rsrc_t rsrc_t;
#define GAS __attribute__((address_space(1)))
#define LAS __attribute__((address_space(3)))
#define DEV __device__ __forceinline__
typedef GAS u64 gu64;
#define RLX_AGENT __ATOMIC_RELAXED, __HIP_MEMORY_SCOPE_AGENT

constexpr int D = 576, SK = 65536, WIN = 4096, DH = 128, FF = 4608, NMOD = 6 * D;
constexpr int NSPLIT = 11;
constexpr int NTHR = 576;
constexpr int SPIN_CAP = 1 << 16;
#define GATE_FFN 790
#define GATE_MODB 200
#define GATE_ATT 140
#define GATE_Q 470
#define GATE_FFN2 0
#define F_N1 0
#define F_D1 2
#define GATE_X2 0
#define POLL_SLEEP 0
#define Q_STAGGER 16
#define GATE_H1 260
#define DN_FIRST 4
#define MODA_HOLD 0
#define GATE_MODB2 120
#define CODE_PF 0
#define V_N1 0
#define V_D1 2
#define V_D2 24
#define ATT_N1 5
#define ATT_D1 2
#define ATT_D2 24

constexpr int G_EPOCH = 0;
constexpr int G_MOD = 64;
constexpr int G_Q = G_MOD + 4 * NMOD;
constexpr int G_O = G_Q + 32 * 2 * 128;
constexpr int G_WO = G_O + 16 * 8 * NSPLIT * 64;
constexpr int G_X2 = G_WO + 128 * 576;
constexpr int G_DN = G_X2 + 72 * 16;
constexpr int G_END = G_DN + 72 * 576;
constexpr size_t G_STAMP = 1 << 20;

constexpr int L_K = 0;
constexpr int L_OL = 98304;
constexpr int L_Q = L_OL + 9216;
constexpr int L_MX = L_Q + 512;
constexpr int L_LS = L_MX + 64;
constexpr int L_OB = L_LS + 64;
constexpr int L_PF = L_OB + 128;
constexpr int L_WO = L_PF + 256;
constexpr int L_MA = L_WO + 36864;
constexpr int L_TAG = L_MA + 1536 + 16;
constexpr int L_END = L_TAG + 16;
constexpr int F_RED = 0;
constexpr int F_H = 16384;
constexpr int F_H2 = 17664;
constexpr int F_ACT = 20736;
constexpr int F_TR = 21248;
constexpr int F_ST = 27136;

struct Params {
  const u16 *x, *kc, *vc, *wq, *wo, *wg, *wu, *wd, *c, *wmod;
  u16* out;
  u64* ws;
};

#define STAMP(i) do {} while (0)
#define STAMP_INIT() do {} while (0)
#define STAMP_DUMP() do {} while (0)
#define L_EXTRA 0

DEV float bf2f(unsigned b) { return __uint_as_float(b << 16); }
DEV unsigned cvt2(float lo, float hi) {
  const f32x2 v = {lo, hi};
  return __builtin_bit_cast(unsigned, __builtin_convertvector(v, bf16x2));
}
DEV unsigned pkr(float f) { return cvt2(f, f); }
DEV float rbf(float f) { return __uint_as_float(cvt2(f, f) & 0xFFFF0000u); }
#define TAG_LO_BITS 4
constexpr unsigned TAGM = (1u << TAG_LO_BITS) - 1u;
struct Tag { unsigned hi, lo, rest; };
DEV Tag make_tag(u64 st) {
  const u64 nx = st * 6364136223846793005ull + 1442695040888963407ull;
  Tag t;
  t.hi = __builtin_amdgcn_readfirstlane((unsigned)(nx >> 32));
  t.rest = __builtin_amdgcn_readfirstlane((unsigned)nx);
  t.lo = t.rest >> (32 - TAG_LO_BITS);
  return t;
}
DEV u64 enc(float v, unsigned hi, unsigned lo) {
  const unsigned u = (__float_as_uint(v) + (TAGM + 1) / 2) & ~TAGM;
  return ((u64)hi << 32) | (u64)(u | lo);
}
DEV Tag tag_put(LAS unsigned char* L, u64 st, int lane) {
  const Tag t = make_tag(st);
  if (lane == 0) *(LAS v4u*)(L + L_TAG) = v4u{t.hi, t.lo, t.rest, 0u};
  return t;
}
DEV Tag tag_get(const LAS unsigned char* L) {
  const v4u v = *(const LAS v4u*)(L + L_TAG);
  Tag t; t.hi = __builtin_amdgcn_readfirstlane(v[0]); t.lo = __builtin_amdgcn_readfirstlane(v[1]); t.rest = __builtin_amdgcn_readfirstlane(v[2]);
  return t;
}
DEV unsigned or_xor(unsigned a, unsigned b, unsigned c) { return __builtin_amdgcn_bitop3_b32(a, b, c, 0xF6); }
struct Chk {
  unsigned a = 0, b = 0;
  DEV void add(u64 g, const Tag& t) { a = or_xor(a, (unsigned)(g >> 32), t.hi); b = or_xor(b, (unsigned)g, t.lo); }
  DEV bool all_ok() const { return __builtin_amdgcn_ballot_w64((a | (b & TAGM)) != 0u) == 0ull; }
};
DEV float gval(u64 g) { return __uint_as_float((unsigned)g & ~TAGM); }
DEV void pub(u64* ws, int idx, float v, const Tag& t) { __hip_atomic_store((gu64*)ws + idx, enc(v, t.hi, t.lo), RLX_AGENT); }
DEV void pubp(gu64* dst, float v, const Tag& t) { __hip_atomic_store(dst, enc(v, t.hi, t.lo), RLX_AGENT); }
DEV u64 peek(u64* ws, int idx) { return __hip_atomic_load((gu64*)ws + idx, RLX_AGENT); }
template <int N, class F> DEV void gather(u64* ws, const Tag& t, u64 (&g)[N], F idx) {
  for (int spin = 0; spin < SPIN_CAP; ++spin) {
#pragma unroll
    for (int k = 0; k < N; ++k) g[k] = peek(ws, idx(k));
    Chk c;
#pragma unroll
    for (int k = 0; k < N; ++k) c.add(g[k], t);
    if (c.all_ok()) break;
    if (POLL_SLEEP) __builtin_amdgcn_s_sleep(POLL_SLEEP);
  }
}

struct G2 { v4u v; DEV float lo() const { return __uint_as_float(v[0] & ~TAGM); } DEV float hi() const { return __uint_as_float(v[2] & ~TAGM); } };
template <int N, class F> DEV void gather2(rsrc_t rw, const Tag& t, G2 (&g)[N], F idx) {
  for (int spin = 0; spin < SPIN_CAP; ++spin) {
    asm volatile("" ::: "memory");
#pragma unroll
    for (int k = 0; k < N; ++k) g[k].v = __builtin_amdgcn_raw_buffer_load_b128(rw, idx(k) * 8, 0, 16);
    unsigned a = 0, b = 0;
#pragma unroll
    for (int k = 0; k < N; ++k) { a = or_xor(or_xor(a, g[k].v[1], t.hi), g[k].v[3], t.hi); b = or_xor(or_xor(b, g[k].v[0], t.lo), g[k].v[2], t.lo); }
    if (__builtin_amdgcn_ballot_w64((a | (b & TAGM)) != 0u) == 0ull) break;
    if (POLL_SLEEP) __builtin_amdgcn_s_sleep(POLL_SLEEP);
  }
}
template <int N, class F> DEV void pretouch(LAS unsigned char* L, u64* ws, F idx) {
  const unsigned dump = (unsigned)(uintptr_t)(L + L_PF);
#pragma unroll
  for (int k = 0; k < N; ++k)
    asm volatile("s_mov_b32 m0, %0\n\ts_nop 0\n\tglobal_load_lds_dword %1, off sc1" :: "s"(dump), "v"((gu64*)ws + idx(k)) : "m0", "memory");
}
DEV f32x4 mfma(v4u a, v4u b, f32x4 c) {
  return __builtin_amdgcn_mfma_f32_16x16x32_bf16(__builtin_bit_cast(bf16x8, a), __builtin_bit_cast(bf16x8, b), c, 0, 0, 0);
}
DEV rsrc_t mk_rsrc(const void* p, unsigned bytes) { return __builtin_amdgcn_make_buffer_rsrc((void*)p, 0, (int)bytes, 0x00020000); }
DEV v4u ldb(rsrc_t r, unsigned voff, unsigned soff) { return __builtin_amdgcn_raw_buffer_load_b128(r, (int)voff, (int)soff, 2); }
#define SMALL_AUX 0
DEV unsigned ldb16(rsrc_t r, unsigned voff) { return __builtin_amdgcn_raw_buffer_load_b16(r, (int)voff, 0, SMALL_AUX); }
#define E_AUX 16
DEV u64 load_state(u64* ws) {
  int zoff = G_EPOCH * 8;
  asm volatile("" : "+v"(zoff));
  typedef unsigned v2u_ __attribute__((ext_vector_type(2)));
  const v2u_ v = __builtin_amdgcn_raw_buffer_load_b64(mk_rsrc(ws, 64), zoff, 0, E_AUX);
  return ((u64)v[1] << 32) | v[0];
}
DEV void bar() {
  asm volatile("s_waitcnt lgkmcnt(0)" ::: "memory");
  __builtin_amdgcn_s_barrier();
  asm volatile("" ::: "memory");
}
#define PIN(x) asm volatile("" : "+v"(x))
#define PINS(x) asm volatile("" : "+s"(x))
DEV void lds_wait() { asm volatile("s_waitcnt lgkmcnt(0)" ::: "memory"); }
template <int CTRL> DEV float dpp(float x) {
  return __builtin_bit_cast(float, __builtin_amdgcn_mov_dpp(__builtin_bit_cast(int, x), CTRL, 0xf, 0xf, true));
}
constexpr int ROR8 = 0x128, ROR4 = 0x124, ROR2 = 0x122, ROR1 = 0x121, XOR1 = 0xB1, XOR2 = 0x4E;
DEV float fmx(float a, float b) { float r; asm("v_max_f32 %0, %1, %2\n\ts_nop 1" : "=v"(r) : "v"(a), "v"(b)); return r; }
DEV float fmx3(float a, float b, float c) { float r; asm("v_max3_f32 %0, %1, %2, %3" : "=v"(r) : "v"(a), "v"(b), "v"(c)); return r; }
DEV float fmx3n(float a, float b, float c) { float r; asm("v_max3_f32 %0, %1, %2, %3\n\ts_nop 1" : "=v"(r) : "v"(a), "v"(b), "v"(c)); return r; }
DEV float x16_sum(float x) {
  auto s = __builtin_amdgcn_permlane16_swap(__float_as_uint(x), __float_as_uint(x), false, false);
  x = __uint_as_float(s[0]) + __uint_as_float(s[1]);
  auto t = __builtin_amdgcn_permlane32_swap(__float_as_uint(x), __float_as_uint(x), false, false);
  return __uint_as_float(t[0]) + __uint_as_float(t[1]);
}
DEV float x16_max(float x) {
  auto s = __builtin_amdgcn_permlane16_swap(__float_as_uint(x), __float_as_uint(x), false, false);
  x = fmaxf(__uint_as_float(s[0]), __uint_as_float(s[1]));
  auto t = __builtin_amdgcn_permlane32_swap(__float_as_uint(x), __float_as_uint(x), false, false);
  return fmaxf(__uint_as_float(t[0]), __uint_as_float(t[1]));
}
DEV float row_sum(float x) { x += dpp<ROR8>(x); x += dpp<ROR4>(x); x += dpp<ROR2>(x); x += dpp<ROR1>(x); return x; }
DEV float row_max(float x) { x = fmaxf(x, dpp<ROR8>(x)); x = fmaxf(x, dpp<ROR4>(x)); x = fmaxf(x, dpp<ROR2>(x)); x = fmaxf(x, dpp<ROR1>(x)); return x; }
DEV float wave_sum(float x) { return x16_sum(row_sum(x)); }
constexpr float LOG2E = 1.4426950408889634f;
DEV float fadd(float a, float b) { float r; asm("v_add_f32 %0, %1, %2" : "=v"(r) : "v"(a), "v"(b)); return r; }
DEV float faddn(float a, float b) { float r; asm("v_add_f32 %0, %1, %2\n\ts_nop 1" : "=v"(r) : "v"(a), "v"(b)); return r; }
DEV float exp_f(float x) { return __builtin_amdgcn_exp2f(x * LOG2E); }
DEV float rcp_f(float x) { return __builtin_amdgcn_rcpf(x); }
DEV float pair_pick(float a, float b, int lane) {
  const float a2 = dpp<0x50>(a), b2 = dpp<0x50>(b);
  const float a3 = dpp<0xFA>(a), b3 = dpp<0xFA>(b);
  const float lo = (lane & 1) ? b2 : a2, hi = (lane & 1) ? b3 : a3;
  return (lane & 4) ? hi : lo;
}
DEV float rdlane(float x, int l) { return __uint_as_float(__builtin_amdgcn_readlane(__float_as_uint(x), l)); }
DEV void gate_wait(u64 t0, unsigned ticks) { while (__builtin_amdgcn_s_memrealtime() - t0 < (u64)ticks) __builtin_amdgcn_s_sleep(2); }

struct Mask { unsigned m[4]; };
DEV Mask make_mask(int lane, bool on) {
  Mask k;
  const unsigned h = on ? ((lane & 1) ? 0xFFFF0000u : 0x0000FFFFu) : 0u;
#pragma unroll
  for (int r = 0; r < 4; ++r) k.m[r] = (((lane & 7) >> 1) == r) ? h : 0u;
  return k;
}
DEV v4u afrag(unsigned pp, const Mask& k) { return v4u{pp & k.m[0], pp & k.m[1], pp & k.m[2], pp & k.m[3]}; }

DEV unsigned ln_apply_pk(float x, float mean_b, float rden, float c1, float shift) {
  const float ln = rbf(rbf(x - mean_b) * rden);
  return pkr(rbf(ln * c1) + shift);
}
DEV float ln_rden(float var) { return rcp_f(rbf(__builtin_amdgcn_sqrtf(rbf(rbf(var) + 1e-5f)))); }

DEV void red_store(LAS float* area, f32x4 d, int lane) {
  const int u = (lane >> 3) & 1;
  if ((lane >> 5) == u) *(LAS f32x4*)(area + u * 64 + 8 * (lane & 7) + 4 * ((lane >> 4) & 1)) = d;
}
DEV unsigned red_off(int lane, int dump) {
  const int u = (lane >> 3) & 1;
  return 4 * ((lane >> 5) == u ? u * 64 + 8 * (lane & 7) + 4 * ((lane >> 4) & 1) : dump);
}
DEV unsigned silu_pk(unsigned craw) { const float v = bf2f(craw); return pkr(v * rcp_f(1.f + exp_f(-v))); }

struct WoAddr { unsigned tro, tri, obo, wro; gu64* dst; };
DEV WoAddr wo_prep(u64* ws, int pidx, int wave, int lane) {
  WoAddr a;
  a.tro = L_OL + wave * 640 + red_off(lane, 128); a.tri = L_OL + wave * 640 + lane * 4; a.obo = L_OB + (lane >> 3) * 16; a.wro = L_WO + wave * 4096 + lane * 16;
  a.dst = (gu64*)ws + G_WO + pidx * 576 + wave * 64 + lane;
  PIN(a.tro); PIN(a.tri); PIN(a.obo); PIN(a.wro); PIN(a.dst);
  return a;
}
DEV void wo_phase(LAS unsigned char* L, const WoAddr& a, const Tag& tag, const Mask& mk) {
  bar();
  v4u ob = *(const LAS v4u*)(L + a.obo);
  const LAS v4u* wr = (const LAS v4u*)(L + a.wro);
  v4u w0 = wr[0], w1 = wr[64], w2 = wr[128], w3 = wr[192];
  PIN(ob); PIN(w0); PIN(w1); PIN(w2); PIN(w3);
  __builtin_amdgcn_sched_barrier(0);
  f32x4 d0 = {0.f, 0.f, 0.f, 0.f}, d1 = {0.f, 0.f, 0.f, 0.f};
  d0 = mfma(afrag(ob[0], mk), w0, d0); d1 = mfma(afrag(ob[1], mk), w1, d1);
  d0 = mfma(afrag(ob[2], mk), w2, d0); d1 = mfma(afrag(ob[3], mk), w3, d1);
  *(LAS f32x4*)(L + a.tro) = d0 + d1;
  lds_wait();
  pubp(a.dst, fadd(*(const LAS float*)(L + a.tri), *(const LAS float*)(L + a.tri + 256)), tag);
}

struct ModA { v4u w[18]; unsigned c[3]; };
DEV void moda_load(const Params& p, int mt, int lane, ModA& m) {
  const rsrc_t rm = mk_rsrc(p.wmod, D * NMOD * 2), rc = mk_rsrc(p.c, D * 2);
  const int r0 = (mt & 3) * 144;
#pragma unroll
  for (int k = 0; k < 3; ++k) m.c[k] = ldb16(rc, (r0 + (k < 2 || lane < 16 ? lane + 64 * k : 143)) * 2);
  const unsigned v = (r0 + (lane >> 3)) * (NMOD * 2) + (lane & 7) * 16;
#pragma unroll
  for (int t = 0; t < 18; ++t) m.w[t] = ldb(rm, v, (mt >> 2) * 128 + t * 8 * (NMOD * 2));
}
DEV void moda_finish(LAS unsigned char* L, u64* ws, int mt, int lane, ModA& m, const Mask& mk, LAS float* tr, const Tag& tag) {
  unsigned sp[18];
  {
    const unsigned s0 = silu_pk(m.c[0]), s1 = silu_pk(m.c[1]), s2 = silu_pk(m.c[2]);
#pragma unroll
    for (int t = 0; t < 18; ++t) sp[t] = __builtin_amdgcn_ds_bpermute(((8 * t + (lane >> 3)) & 63) * 4, t < 8 ? s0 : t < 16 ? s1 : s2);
  }
  gu64* dst = (gu64*)ws + G_MOD + (mt & 3) * NMOD + (mt >> 2) * 64 + lane;
  unsigned tro = red_off(lane, 128);
  PIN(dst); PIN(tro);
#pragma unroll
  for (int t = 0; t < 18; ++t) PIN(sp[t]);
  __builtin_amdgcn_sched_barrier(0);
  f32x4 d0 = {0.f, 0.f, 0.f, 0.f}, d1 = {0.f, 0.f, 0.f, 0.f};
#pragma unroll
  for (int t = 0; t < 18; t += 2) {
    d0 = mfma(afrag(sp[t], mk), m.w[t], d0);
    __builtin_amdgcn_sched_barrier(0);
    d1 = mfma(afrag(sp[t + 1], mk), m.w[t + 1], d1);
    __builtin_amdgcn_sched_barrier(0);
  }
  *(LAS f32x4*)((LAS unsigned char*)tr + tro) = d0 + d1;
  lds_wait();
  pubp(dst, fadd(tr[lane], tr[64 + lane]), tag);
}

DEV void role_att(const Params& p, LAS unsigned char* L, int x, int i, int wave, int lane, u64 t0) {
  const int g = 2 * x + (i >= NSPLIT ? 1 : 0), j = i >= NSPLIT ? i - NSPLIT : i;
  const int ntile = j < 8 ? 23 : 24, tile0 = j < 8 ? 23 * j : 184 + 24 * (j - 8);
  const bool owner = j < 8;
  int redc = -1;
  if (j >= 8) redc = 9 * x + (g & 1) * 3 + (j - 8);
  else if (j == 0) redc = 9 * x + 6 + (g & 1);
  else if (j == 1 && (g & 1) == 0) redc = 9 * x + 8;
  u64* ws = p.ws;
  const Mask mk = make_mask(lane, true);
  const int kap = lane >> 4, cc = lane & 15;
  const bool moda = i < 9;

  if (wave < 8) {
    const size_t hb = ((size_t)g * SK + (SK - WIN)) * DH;
    const rsrc_t rk = mk_rsrc(p.kc + hb, WIN * DH * 2), rv = mk_rsrc(p.vc + hb, WIN * DH * 2);
    const bool three = wave + 16 < ntile;
    const unsigned tso = (tile0 + wave) * 4096;
    const unsigned ldsk = (unsigned)(uintptr_t)(L + L_K) + wave * 3 * 4096;
    unsigned vk[4];
#pragma unroll
    for (int q = 0; q < 4; ++q) vk[q] = (4 * q + kap) * 256 + ((cc ^ (4 * q + kap)) << 4);
#define KDMA(s, q) asm volatile("s_mov_b32 m0, %0\n\ts_nop 0\n\tbuffer_load_dwordx4 %1, %2, %3 offen nt lds" \
                     :: "s"(ldsk + (s) * 4096 + (q) * 1024), "v"(vk[q]), "s"(rk), "s"(tso + (s) * 32768) : "m0", "memory")
#define THR(n) do { if ((n) < ATT_N1) { if ((n) >= ATT_D1 && moda) asm volatile("s_waitcnt vmcnt(%0)" :: "n"(ATT_D1 - 1) : "memory"); } \
                     else if ((n) >= ATT_D2) asm volatile("s_waitcnt vmcnt(%0)" :: "n"(ATT_D2 - 1) : "memory"); } while (0)
#define THR_DEPTH(n) ((n) < ATT_N1 ? ATT_D1 : ATT_D2)
#define THRV(t) asm volatile("s_waitcnt vmcnt(%0)" :: "n"(((t) < V_N1 ? V_D1 : V_D2) - 1) : "memory")
    if (moda) __builtin_amdgcn_s_barrier();
    __builtin_amdgcn_s_setprio(2);
    KDMA(0, 0);
    if (GATE_ATT) gate_wait(t0, GATE_ATT);
    if (MODA_HOLD && moda) __builtin_amdgcn_s_barrier();
    THR(1); KDMA(0, 1); THR(2); KDMA(0, 2); THR(3); KDMA(0, 3);
    THR(4); KDMA(1, 0); THR(5); KDMA(1, 1); THR(6); KDMA(1, 2); THR(7); KDMA(1, 3);
    if (three) { THR(8); KDMA(2, 0); THR(9); KDMA(2, 1); THR(10); KDMA(2, 2); THR(11); KDMA(2, 3); }
#undef KDMA
    __builtin_amdgcn_s_setprio(0);
    v4u vr[12];
    const unsigned vv = kap * 1024 + cc * 16;
#pragma unroll
    for (int t = 0; t < 8; ++t) { THRV(t); vr[t] = ldb(rv, vv + (t & 3) * 256, tso + (t >> 2) * 32768); }
    if (three) {
#pragma unroll
      for (int t = 8; t < 12; ++t) { THRV(t); vr[t] = ldb(rv, vv + (t & 3) * 256, tso + 65536); }
    } else {
#pragma unroll
      for (int t = 8; t < 12; ++t) vr[t] = v4u{0u, 0u, 0u, 0u};
    }
#undef THR
#undef THRV
#undef THR_DEPTH
    __builtin_amdgcn_sched_barrier(0);
    STAMP(1);
    if (three) asm volatile("s_waitcnt vmcnt(12)" ::: "memory");
    else asm volatile("s_waitcnt vmcnt(8)" ::: "memory");
    v4u kf[12];
#pragma unroll
    for (int s = 0; s < 3; ++s)
#pragma unroll
      for (int ks = 0; ks < 4; ++ks)
        kf[s * 4 + ks] = *(const LAS v4u*)(L + L_K + (wave * 3 + (s < 2 || three ? s : 0)) * 4096 + cc * 256 + (((4 * ks + kap) ^ cc) << 4));
#pragma unroll
    for (int t = 0; t < 12; ++t) PIN(kf[t]);
    const int head = (lane >> 3) & 1;
    const int me = (wave & 3) * 64 + lane;
    unsigned qsrc = L_Q + head * 256 + kap * 16, mxw = L_MX + (head * 8 + wave) * 4, mxr = L_MX + head * 32, olw = L_OL + wave * 1024 + lane * 16;
    unsigned msrc = L_OL + ((16 * (((me >> 7) * 8 + (me & 7)) >> 2) + ((me & 127) >> 3)) * 4 + (me & 3)) * 4;
    gu64* mdst = (gu64*)ws + G_O + ((g * 8 + (me >> 5)) * NSPLIT + j) * 64 + (me & 31);
    bool lead = (lane & 0x37) == 0;
    PIN(qsrc); PIN(mxw); PIN(mxr); PIN(olw); PIN(msrc); PIN(mdst);
    STAMP(3);
    bar();
    STAMP(2);
    v4u qf[4];
#pragma unroll
    for (int ks = 0; ks < 4; ++ks) qf[ks] = *(const LAS v4u*)(L + qsrc + ks * 64);
#pragma unroll
    for (int ks = 0; ks < 4; ++ks) PIN(qf[ks]);
    __builtin_amdgcn_sched_barrier(0);
    f32x4 acc[3] = {{0.f, 0.f, 0.f, 0.f}, {0.f, 0.f, 0.f, 0.f}, {0.f, 0.f, 0.f, 0.f}};
#pragma unroll
    for (int ks = 0; ks < 4; ++ks)
#pragma unroll
      for (int s = 0; s < 3; ++s) acc[s] = mfma(kf[s * 4 + ks], qf[ks], acc[s]);
    __builtin_amdgcn_sched_barrier(0);
    float sc[12];
#pragma unroll
    for (int t = 0; t < 12; t += 2) {
      const unsigned u = cvt2(acc[t >> 2][t & 3], acc[t >> 2][(t & 3) + 1]);
      const unsigned w = cvt2(__uint_as_float(u << 16) * 0.08838834764831845f, __uint_as_float(u & 0xFFFF0000u) * 0.08838834764831845f);
      sc[t] = __uint_as_float(w << 16);
      sc[t + 1] = __uint_as_float(w & 0xFFFF0000u);
    }
    if (!three) {
#pragma unroll
      for (int t = 8; t < 12; ++t) sc[t] = -INFINITY;
    }
    float mx = fmx3n(fmx3(fmx3(sc[0], sc[1], sc[2]), fmx3(sc[3], sc[4], sc[5]), fmx3(sc[6], sc[7], sc[8])), fmx3(sc[9], sc[10], sc[11]), sc[0]);
    mx = x16_max(mx);
    if (lead) *(LAS float*)(L + mxw) = mx;
    bar();
    float m;
    {
      const f32x4 a = *(const LAS f32x4*)(L + mxr), b = *(const LAS f32x4*)(L + mxr + 16);
      m = fmx3(fmx3(a[0], a[1], a[2]), fmx3(a[3], b[0], b[1]), fmx(b[2], b[3]));
    }
    v4u af[12];
    float ls0 = 0.f, ls1 = 0.f;
    const float mneg = -m * LOG2E;
#pragma unroll
    for (int t = 0; t < 12; t += 2) {
      const float e0 = __builtin_amdgcn_exp2f(fmaf(sc[t], LOG2E, mneg)), e1 = __builtin_amdgcn_exp2f(fmaf(sc[t + 1], LOG2E, mneg));
      ls0 += e0; ls1 += e1;
      af[t] = afrag(pkr(e0), mk); af[t + 1] = afrag(pkr(e1), mk);
    }
    const float ls = x16_sum(ls0 + ls1);
    if (lead) *(LAS float*)(L + mxw + 64) = ls;
    const Tag tag = tag_get(L);
    __builtin_amdgcn_sched_barrier(0);
    STAMP(4);
    f32x4 o0 = {0.f, 0.f, 0.f, 0.f}, o1 = {0.f, 0.f, 0.f, 0.f};
#pragma unroll
    for (int t = 0; t < 12; t += 2) { o0 = mfma(af[t], vr[t], o0); o1 = mfma(af[t + 1], vr[t + 1], o1); }
    *(LAS f32x4*)(L + olw) = o0 + o1;
    STAMP(5);
    bar();
    if (wave < 4) {
      const LAS float* src = (const LAS float*)(L + msrc);
      float s[8];
#pragma unroll
      for (int w = 0; w < 8; ++w) s[w] = src[w * 256];
      pubp(mdst, fadd(fadd(fadd(s[0], s[1]), fadd(s[2], s[3])), fadd(fadd(s[4], s[5]), fadd(s[6], s[7]))), tag);
    } else if (wave == 4 && lane < 16) {
      const int u = lane >> 1, hh = u >> 2;
      const f32x4 a = *(const LAS f32x4*)(L + ((lane & 1) ? L_LS : L_MX) + hh * 32), b = *(const LAS f32x4*)(L + ((lane & 1) ? L_LS : L_MX) + hh * 32 + 16);
      const float vs = ((a[0] + a[1]) + (a[2] + a[3])) + ((b[0] + b[1]) + (b[2] + b[3]));
      const float vm = fmaxf(fmaxf(fmaxf(a[0], a[1]), fmaxf(a[2], a[3])), fmaxf(fmaxf(b[0], b[1]), fmaxf(b[2], b[3])));
      pub(ws, G_O + ((g * 8 + u) * NSPLIT + j) * 64 + 32 + (lane & 1), (lane & 1) ? vs : vm, tag);
    }
    STAMP(6);
    if (owner) {
      wo_phase(L, wo_prep(ws, g * 8 + j, wave, lane), tag, mk);
      STAMP(8);
    }
  } else {
    __builtin_amdgcn_s_setprio(3);
    Tag tag;
    {
      if (moda) {
        u64 st = load_state(ws);
        ModA ma;
        moda_load(p, 9 * x + i, lane, ma);
        __builtin_amdgcn_sched_barrier(0);
        __builtin_amdgcn_s_barrier();
        asm volatile("" : "+v"(st));
        tag = tag_put(L, st, lane);
        moda_finish(L, ws, 9 * x + i, lane, ma, mk, (LAS float*)(L + L_MA), tag);
        STAMP(9);
        if (MODA_HOLD) __builtin_amdgcn_s_barrier();
      } else {
        tag = tag_put(L, load_state(ws), lane);
      }
    }
    float xr = 0.f;
    if (redc >= 0) xr = bf2f(p.x[redc * 8 + (lane & 7)]);
    if (GATE_Q) gate_wait(t0, GATE_Q);
    const rsrc_t rw = mk_rsrc(ws, G_END * 8);
    {
      G2 gq[4];
      gather2<4>(rw, tag, gq, [&](int k) { return G_Q + ((2 * g + (k >> 1)) * 2 + (k & 1)) * 128 + 2 * lane; });
      ((LAS unsigned*)(L + L_Q))[lane] = cvt2(fadd(gq[0].lo(), gq[1].lo()), fadd(gq[0].hi(), gq[1].hi()));
      ((LAS unsigned*)(L + L_Q))[64 + lane] = cvt2(fadd(gq[2].lo(), gq[3].lo()), fadd(gq[2].hi(), gq[3].hi()));
    }
    STAMP(1);
    if (owner) {
      const int ob = G_O + (g * 8 + j) * NSPLIT * 64;
      pretouch<1>(L, ws, [&](int) { const int q_ = lane < 33 ? lane : 32; return ob + (q_ / 3) * 64 + (q_ % 3) * 16; });
    }
    if (redc >= 0) {
      pretouch<2>(L, ws, [&](int s) { return G_WO + (s * 64 + lane) * 576 + redc * 8; });
      pretouch<2>(L, ws, [&](int s) { const int f_ = s * 64 + lane; return G_DN + (f_ < 72 ? f_ : 71) * 576 + redc * 8; });
    }
    bar();
    if (owner) {
      const rsrc_t rwo = mk_rsrc(p.wo, 4096 * D * 2);
      const unsigned wo_v = (lane >> 3) * (D * 2) + (lane & 7) * 16;
      const unsigned wo_s = (g * 256 + j * 32) * (D * 2), ldsw = (unsigned)(uintptr_t)(L + L_WO);
#pragma unroll
      for (int w = 0; w < 9; ++w)
#pragma unroll
        for (int t = 0; t < 4; ++t)
          asm volatile("s_mov_b32 m0, %0\n\ts_nop 0\n\tbuffer_load_dwordx4 %1, %2, %3 offen nt lds"
                       :: "s"(ldsw + (w * 4 + t) * 1024), "v"(wo_v), "s"(rwo), "s"(wo_s + w * 128 + t * 8 * (D * 2)) : "m0", "memory");
    }
    bar();
    bar();
    STAMP(2);
    if (owner) {
      const int ob = G_O + (g * 8 + j) * NSPLIT * 64;
      const int k2 = kap < 3 ? 8 + kap : 10;
      unsigned obw = L_OB + (((2 * cc) & 7) * 4 + (cc >> 2)) * 4;
      float live = kap < 3 ? 1.f : 0.f;
      PIN(obw); PIN(live);
      const WoAddr wa = wo_prep(ws, g * 8 + j, 8, lane);
      G2 go[6];
      gather2<6>(rw, tag, go, [&](int k) { const int sp = k % 3 == 0 ? kap : k % 3 == 1 ? 4 + kap : k2; return ob + sp * 64 + (k < 3 ? 2 * cc : 32); });
      STAMP(3);
      const float M = x16_max(fmaxf(fmaxf(go[3].lo(), go[4].lo()), go[5].lo()));
      const float Mn = -M * LOG2E;
      const float w0 = __builtin_amdgcn_exp2f(fmaf(go[3].lo(), LOG2E, Mn)), w1 = __builtin_amdgcn_exp2f(fmaf(go[4].lo(), LOG2E, Mn));
      const float w2 = __builtin_amdgcn_exp2f(fmaf(go[5].lo(), LOG2E, Mn)) * live;
      const float Ls = x16_sum(fmaf(w2, go[5].hi(), fmaf(w1, go[4].hi(), w0 * go[3].hi())));
      const float o0 = x16_sum(fmaf(w2, go[2].lo(), fmaf(w1, go[1].lo(), w0 * go[0].lo())));
      const float o1 = x16_sum(fmaf(w2, go[2].hi(), fmaf(w1, go[1].hi(), w0 * go[0].hi())));
      const float rl = rcp_f(Ls);
      *(LAS unsigned*)(L + obw) = pkr(o0 * rl);
      *(LAS unsigned*)(L + obw + 16) = pkr(o1 * rl);
      asm volatile("s_waitcnt vmcnt(0)" ::: "memory");
      wo_phase(L, wa, tag, mk);
      STAMP(4);
    }
    if (redc >= 0) {
      u64 gg[8];
      gather<8>(ws, tag, gg, [&](int k) { return G_MOD + (k & 3) * NMOD + (k < 4 ? 2 : 5) * D + redc * 8 + (lane & 7); });
      float ga = rbf(fadd(fadd(gval(gg[0]), gval(gg[1])), fadd(gval(gg[2]), gval(gg[3]))));
      float gb = rbf(fadd(fadd(gval(gg[4]), gval(gg[5])), fadd(gval(gg[6]), gval(gg[7]))));
      gu64* xdst = (gu64*)ws + G_X2 + redc * 16 + (lane & 7);
      GAS u16* odst = (GAS u16*)p.out + redc * 8 + (lane & 7);
      PIN(ga); PIN(gb); PIN(xr); PIN(xdst); PIN(odst);
      float x2;
      {
        G2 gw[8];
        gather2<8>(rw, tag, gw, [&](int s) { return G_WO + (s * 16 + (lane >> 2)) * 576 + redc * 8 + 2 * (lane & 3); });
        STAMP(5);
        float t0_ = faddn(fadd(fadd(gw[0].lo(), gw[1].lo()), fadd(gw[2].lo(), gw[3].lo())), fadd(fadd(gw[4].lo(), gw[5].lo()), fadd(gw[6].lo(), gw[7].lo())));
        float t1_ = faddn(fadd(fadd(gw[0].hi(), gw[1].hi()), fadd(gw[2].hi(), gw[3].hi())), fadd(fadd(gw[4].hi(), gw[5].hi()), fadd(gw[6].hi(), gw[7].hi())));
        t0_ += dpp<ROR8>(t0_); t1_ += dpp<ROR8>(t1_); t0_ += dpp<ROR4>(t0_); t1_ += dpp<ROR4>(t1_);
        t0_ = x16_sum(t0_); t1_ = x16_sum(t1_);
        const float ts = pair_pick(t0_, t1_, lane);
        x2 = rbf(xr + rbf(ga * rbf(ts)));
        if (lane < 8) pubp(xdst, x2, tag);
      }
      STAMP(6);
      {
        G2 gd[5];
        float w4 = lane < 32 ? 1.f : 0.f;
        PIN(w4);
        gather2<5>(rw, tag, gd, [&](int s) { const int f_ = s * 16 + (lane >> 2); return G_DN + (f_ < 72 ? f_ : 71) * 576 + redc * 8 + 2 * (lane & 3); });
        STAMP(7);
        float t0_ = fmaf(w4, gd[4].lo(), fadd(fadd(gd[0].lo(), gd[1].lo()), fadd(gd[2].lo(), gd[3].lo())));
        float t1_ = fmaf(w4, gd[4].hi(), fadd(fadd(gd[0].hi(), gd[1].hi()), fadd(gd[2].hi(), gd[3].hi())));
        t0_ += dpp<ROR8>(t0_); t1_ += dpp<ROR8>(t1_); t0_ += dpp<ROR4>(t0_); t1_ += dpp<ROR4>(t1_);
        t0_ = x16_sum(t0_); t1_ = x16_sum(t1_);
        const float ts = pair_pick(t0_, t1_, lane);
        const unsigned o = pkr(x2 + rbf(gb * rbf(ts)));
        if (lane < 8) *odst = (u16)o;
        if (redc == 0 && lane == 0) __hip_atomic_store((gu64*)ws + G_EPOCH, ((u64)tag.hi << 32) | (u64)tag.rest, RLX_AGENT);
      }
      STAMP(8);
    }
  }
}

DEV void role_ffn(const Params& p, LAS unsigned char* L, int x, int i, int wave, int lane, u64 t0) {
  const int f = 9 * x + i;
  u64* ws = p.ws;
  const Mask mk = make_mask(lane, true);
  LAS float* red = (LAS float*)(L + F_RED);
  const rsrc_t rm = mk_rsrc(p.wmod, D * NMOD * 2), rc = mk_rsrc(p.c, D * 2), rx = mk_rsrc(p.x, D * 2);
  const int hq = 4 * x + (i >> 1), qr = i & 1;
  const rsrc_t rw = mk_rsrc(ws, G_END * 8);
  Tag tag;
  if (i < 8) {
    v4u wa[12];
    if (wave >= 3) {
      const rsrc_t rq = mk_rsrc(p.wq, D * 4096 * 2);
      const unsigned v = (lane >> 4) * 8192 + (lane & 15) * 16, s = (qr * 288 + (wave - 3) * 48) * 8192 + hq * 256;
#pragma unroll
      for (int t = 0; t < 12; ++t) wa[t] = ldb(rq, v, s + t * 4 * 8192);
      __builtin_amdgcn_sched_barrier(0);
      STAMP(1);
    } else {
      __builtin_amdgcn_s_setprio(3);
      const u64 st = load_state(ws);
      float xv[9];
#pragma unroll
      for (int k = 0; k < 9; ++k) xv[k] = bf2f(ldb16(rx, (lane + 64 * k) * 2));
      const int p0 = wave * 64 + lane, pr = p0 < 144 ? p0 : 143;
      const unsigned xo = __builtin_amdgcn_raw_buffer_load_b32(rx, (qr * 288 + 2 * pr) * 2, 0, 0);
      float s = 0.f;
#pragma unroll
      for (int k = 0; k < 9; ++k) s += xv[k];
      const float mf = wave_sum(s) * (1.f / D);
      float v = 0.f;
#pragma unroll
      for (int k = 0; k < 9; ++k) { const float d = xv[k] - mf; v = fmaf(d, d, v); }
      const float rden = ln_rden(wave_sum(v) * (1.f / D)), mean = rbf(mf);
      tag = wave == 0 ? tag_put(L, st, lane) : make_tag(st);
      const int hw = pr / 24, hr = 2 * pr - hw * 48;
      unsigned hdst = F_H + ((hw * 4 + (hr & 3)) * 12 + (hr >> 2)) * 4;
      float ln0 = rbf(rbf(__uint_as_float(xo << 16) - mean) * rden), ln1 = rbf(rbf(__uint_as_float(xo & 0xFFFF0000u) - mean) * rden);
      PIN(hdst); PIN(ln0); PIN(ln1);
      STAMP(2);
      if (GATE_H1) gate_wait(t0, GATE_H1);
      G2 ga[8];
      gather2<8>(rw, tag, ga, [&](int k) { return G_MOD + (k & 3) * NMOD + (k >> 2) * D + qr * 288 + 2 * pr; });
      STAMP(3);
      const float sa0 = rbf(fadd(fadd(ga[0].lo(), ga[1].lo()), fadd(ga[2].lo(), ga[3].lo()))), sa1 = rbf(fadd(fadd(ga[0].hi(), ga[1].hi()), fadd(ga[2].hi(), ga[3].hi())));
      const float ca0 = rbf(fadd(fadd(ga[4].lo(), ga[5].lo()), fadd(ga[6].lo(), ga[7].lo()))), ca1 = rbf(fadd(fadd(ga[4].hi(), ga[5].hi()), fadd(ga[6].hi(), ga[7].hi())));
      *(LAS unsigned*)(L + hdst) = pkr(rbf(ln0 * rbf(1.f + ca0)) + sa0);
      *(LAS unsigned*)(L + hdst + 48) = pkr(rbf(ln1 * rbf(1.f + ca1)) + sa1);
      __builtin_amdgcn_s_setprio(0);
    }
    Mask mq = make_mask(lane, (lane & 8) == 0);
    const int qe = (wave & 1) * 64 + lane;
    unsigned hsrc = F_H + ((wave - 3) * 4 + (lane >> 4)) * 48, qst = F_RED + 4608 + (wave - 3) * 1024 + lane * 16;
    unsigned qsrc = F_RED + 4608 + ((16 * ((qe & 7) >> 2) + (qe >> 3)) * 4 + (qe & 3)) * 4;
    gu64* qdst = (gu64*)ws + G_Q + (hq * 2 + qr) * 128 + qe;
    PIN(mq.m[0]); PIN(mq.m[1]); PIN(mq.m[2]); PIN(mq.m[3]); PIN(hsrc); PIN(qst); PIN(qsrc); PIN(qdst);
    bar();
    if (wave >= 3) {
      const LAS v4u* hp = (const LAS v4u*)(L + hsrc);
      const v4u h0 = hp[0], h1 = hp[1], h2 = hp[2];
      f32x4 d0 = {0.f, 0.f, 0.f, 0.f}, d1 = {0.f, 0.f, 0.f, 0.f};
#pragma unroll
      for (int t = 0; t < 4; ++t) { d0 = mfma(afrag(h0[t], mq), wa[t], d0); d1 = mfma(afrag(h1[t], mq), wa[4 + t], d1); }
#pragma unroll
      for (int t = 0; t < 4; t += 2) { d0 = mfma(afrag(h2[t], mq), wa[8 + t], d0); d1 = mfma(afrag(h2[t + 1], mq), wa[9 + t], d1); }
      *(LAS f32x4*)(L + qst) = d0 + d1;
    }
    bar();
    if (wave < 2) {
      const LAS float* src = (const LAS float*)(L + qsrc);
      pubp(qdst, fadd(fadd(fadd(src[0], src[256]), fadd(src[512], src[768])), fadd(src[1024], src[1280])), tag);
    }
  } else {
    if (wave == 0) tag_put(L, load_state(ws), lane);
    if (GATE_MODB2) gate_wait(t0, GATE_MODB2);
    ModA ma;
    if (wave < 6) moda_load(p, 72 + 6 * x + wave, lane, ma);
    __builtin_amdgcn_sched_barrier(0);
    bar();
    if (wave < 6) {
      tag = tag_get(L);
      moda_finish(L, ws, 72 + 6 * x + wave, lane, ma, mk, (LAS float*)(L + F_RED + wave * 1024), tag);
    }
  }
  tag = tag_get(L);
  STAMP(4);
  pretouch<1>(L, ws, [&](int) { const int e = wave * 64 + lane; return G_X2 + (e >> 3) * 16 + (e & 7); });
  const bool slow_tile = ((((unsigned)(uintptr_t)p.wg >> 7) + f) & 7) == 3;
  if (!slow_tile) gate_wait(t0, GATE_FFN);
#define THRF(n) do { if ((n) < F_N1 && !slow_tile && __builtin_amdgcn_s_memrealtime() - t0 < (u64)GATE_FFN2) asm volatile("s_waitcnt vmcnt(%0)" :: "n"(F_D1 - 1) : "memory"); } while (0)
  v4u wg[9], wu[9], wd[8];
  if (wave < 8) {
    const rsrc_t rg = mk_rsrc(p.wg, D * FF * 2), ru = mk_rsrc(p.wu, D * FF * 2);
    const unsigned v = (lane >> 3) * (FF * 2) + (lane & 7) * 16, s = wave * 72 * (FF * 2) + f * 128;
#pragma unroll
    for (int t = 0; t < 9; ++t) { THRF(2 * t); wg[t] = ldb(rg, v, s + t * 8 * (FF * 2)); THRF(2 * t + 1); wu[t] = ldb(ru, v, s + t * 8 * (FF * 2)); }
  }
  {
    const rsrc_t rd = mk_rsrc(p.wd, FF * D * 2);
    const unsigned v = (lane >> 3) * (D * 2) + (lane & 7) * 16, s = f * 64 * (D * 2) + wave * 128;
#pragma unroll
    for (int t = 0; t < DN_FIRST; ++t) { THRF(18 + t); wd[t] = ldb(rd, v, s + t * 8 * (D * 2)); }
  }
  __builtin_amdgcn_sched_barrier(0);
  STAMP(5);
  unsigned h2w, h2r, stw, str_, gur, guw0, actw, actr, trw, trr;
  gu64* ddst;
  {
    const int e = wave * 64 + lane, hw = e / 72, rem = e - hw * 72, c = (wave & 3) * 16 + (lane >> 2);
    h2w = F_H2 + ((hw * 8 + (rem & 7)) * 12 + (rem >> 3)) * 4;
    h2r = F_H2 + ((wave & 7) * 8 + (lane >> 3)) * 48;
    stw = F_ST + wave * 8;
    str_ = F_ST + ((lane & 15) < 9 ? (lane & 15) : 9) * 8;
    guw0 = F_RED + (wave & 7) * 1040 + red_off(lane, 256);
    gur = F_RED + ((lane & 3) * 520 + c) * 4;
    actw = F_ACT + ((lane & 3) == 0 ? (c & 7) * 8 + (c >> 3) : 64) * 4;
    actr = F_ACT + (lane >> 3) * 32;
    trw = F_TR + wave * 640 + red_off(lane, 128);
    trr = F_TR + wave * 640 + lane * 4;
    ddst = (gu64*)ws + G_DN + f * 576 + wave * 64 + lane;
    if (wave == 0 && lane == 0) *(LAS f32x2*)(L + F_ST + 72) = f32x2{0.f, 0.f};
  }
  PIN(h2w); PIN(h2r); PIN(stw); PIN(str_); PIN(guw0); PIN(gur); PIN(actw); PIN(actr); PIN(trw); PIN(trr); PIN(ddst);
  if (GATE_X2) gate_wait(t0, GATE_X2);
  {
    const int e = wave * 64 + lane;
    u64 gs[9];
    bool have_x2 = false;
    for (int spin = 0; spin < SPIN_CAP; ++spin) {
#pragma unroll
      for (int k = 0; k < 8; ++k) gs[k] = peek(ws, G_MOD + (k & 3) * NMOD + (k < 4 ? 3 : 4) * D + e);
      gs[8] = peek(ws, G_X2 + (e >> 3) * 16 + (e & 7));
      Chk c, cx;
#pragma unroll
      for (int k = 0; k < 8; ++k) c.add(gs[k], tag);
      cx.add(gs[8], tag);
      have_x2 = cx.all_ok();
      if (c.all_ok()) break;
    }
    float sb = rbf(fadd(fadd(gval(gs[0]), gval(gs[1])), fadd(gval(gs[2]), gval(gs[3]))));
    float c1b = rbf(1.f + rbf(fadd(fadd(gval(gs[4]), gval(gs[5])), fadd(gval(gs[6]), gval(gs[7])))));
    float on = (lane & 15) < 9 ? 64.f : 0.f;
    PIN(sb); PIN(c1b); PIN(on);
    STAMP(6);
    u64 gx[1] = {gs[8]};
    if (!have_x2) gather<1>(ws, tag, gx, [&](int) { return G_X2 + (e >> 3) * 16 + (e & 7); });
    STAMP(7);
    const float xv = gval(gx[0]);
    const float sh = __uint_as_float(__builtin_amdgcn_readfirstlane(__float_as_uint(xv)));
    const float d = xv - sh;
    float s1 = d, s2 = d * d;
    s1 += dpp<ROR8>(s1); s2 += dpp<ROR8>(s2); s1 += dpp<ROR4>(s1); s2 += dpp<ROR4>(s2);
    s1 += dpp<ROR2>(s1); s2 += dpp<ROR2>(s2); s1 += dpp<ROR1>(s1); s2 += dpp<ROR1>(s2);
    s1 = x16_sum(s1); s2 = x16_sum(s2);
    const float dm = s1 * (1.f / 64.f);
    if (lane == 0) *(LAS f32x2*)(L + stw) = f32x2{sh + dm, fmaf(-dm, s1, s2)};
    bar();
    const f32x2 st = *(const LAS f32x2*)(L + str_);
    {
      const rsrc_t rd = mk_rsrc(p.wd, FF * D * 2);
      const unsigned v = (lane >> 3) * (D * 2) + (lane & 7) * 16, s = f * 64 * (D * 2) + wave * 128;
#pragma unroll
      for (int t = DN_FIRST; t < 8; ++t) wd[t] = ldb(rd, v, s + t * 8 * (D * 2));
      __builtin_amdgcn_sched_barrier(0);
    }
    const float mf = row_sum(st[0]) * (1.f / 9.f);
    const float dv = st[0] - mf;
    const float m2 = row_sum(fmaf(on * dv, dv, st[1]));
    *(LAS unsigned*)(L + h2w) = ln_apply_pk(xv, rbf(mf), ln_rden(m2 * (1.f / D)), c1b, sb);
  }
  bar();
  STAMP(8);
  if (wave < 8) {
    const LAS v4u* hp = (const LAS v4u*)(L + h2r);
    const v4u h0 = hp[0], h1 = hp[1];
    const unsigned h8 = ((const LAS unsigned*)hp)[8];
    f32x4 dg = {0.f, 0.f, 0.f, 0.f}, du = {0.f, 0.f, 0.f, 0.f};
#pragma unroll
    for (int t = 0; t < 9; ++t) {
      const v4u a = afrag(t < 4 ? h0[t & 3] : t < 8 ? h1[t & 3] : h8, mk);
      dg = mfma(a, wg[t], dg);
      du = mfma(a, wu[t], du);
    }
    *(LAS f32x4*)(L + guw0) = dg;
    *(LAS f32x4*)(L + guw0 + ((lane >> 5) == ((lane >> 3) & 1) ? 512 : 0)) = du;
  }
  bar();
  if (wave < 4) {
    const LAS float* r0 = (const LAS float*)(L + gur);
    float g = faddn(fadd(r0[0], r0[64]), fadd(r0[260], r0[324])), u = faddn(fadd(r0[128], r0[192]), fadd(r0[388], r0[452]));
    g += dpp<XOR1>(g); u += dpp<XOR1>(u);
    g += dpp<XOR2>(g); u += dpp<XOR2>(u);
    g = rbf(g); u = rbf(u);
    const float y = g * fmaf(g * g, 0.044715f * 0.7978845608028654f * 2.f * LOG2E, 0.7978845608028654f * 2.f * LOG2E);
    const float th = fmaf(-2.f, rcp_f(__builtin_amdgcn_exp2f(y) + 1.f), 1.f);
    const float act = rbf(0.5f * g * (1.f + th)) * u;
    *(LAS unsigned*)(L + actw) = pkr(act);
  }
  bar();
  STAMP(9);
  {
    const LAS v4u* ap = (const LAS v4u*)(L + actr);
    const v4u a0 = ap[0], a1 = ap[1];
    f32x4 d0 = {0.f, 0.f, 0.f, 0.f}, d1 = {0.f, 0.f, 0.f, 0.f};
#pragma unroll
    for (int t = 0; t < 4; ++t) { d0 = mfma(afrag(a0[t], mk), wd[t], d0); d1 = mfma(afrag(a1[t], mk), wd[4 + t], d1); }
    *(LAS f32x4*)(L + trw) = d0 + d1;
    lds_wait();
    pubp(ddst, fadd(*(const LAS float*)(L + trr), *(const LAS float*)(L + trr + 256)), tag);
  }
  STAMP(10);
}

DEV void role_modb(const Params& p, LAS unsigned char* L, int x, int wave, int lane, u64 t0, u64 code0) {
  if (CODE_PF && wave == 8) {
    const unsigned dump = (unsigned)(uintptr_t)(L + L_PF);
#pragma unroll
    for (int k = 0; k < (CODE_PF + 8191) / 8192; ++k) {
      const unsigned o = k * 8192 + lane * 128;
      asm volatile("s_mov_b32 m0, %0\n\ts_nop 0\n\tglobal_load_lds_dword %1, off" :: "s"(dump), "v"(code0 + (o < CODE_PF - 128 ? o : CODE_PF - 128)) : "m0", "memory");
    }
  }
  const Mask mk = make_mask(lane, true);
  if (wave == 0) tag_put(L, load_state(p.ws), lane);
  bar();
  const Tag tag = tag_get(L);
  gate_wait(t0, GATE_MODB);
  for (int k = wave; k < 12; k += 9) {
    ModA ma;
    moda_load(p, 120 + x + 8 * k, lane, ma);
    __builtin_amdgcn_sched_barrier(0);
    moda_finish(L, p.ws, 120 + x + 8 * k, lane, ma, mk, (LAS float*)(L + wave * 1024), tag);
  }
  STAMP(2);
}

__global__ void __launch_bounds__(NTHR) adaln_block_kernel(Params p) {
  __shared__ __attribute__((aligned(16))) unsigned char smem[L_END + L_EXTRA];
  LAS unsigned char* L = (LAS unsigned char*)smem;
  const int lane = threadIdx.x & 63, wave = __builtin_amdgcn_readfirstlane(threadIdx.x >> 6);
  u64 code0;
  asm volatile("s_getpc_b64 %0" : "=s"(code0));
  code0 &= ~0xFFull;
  const u64 t0 = __builtin_amdgcn_s_memrealtime();
  STAMP_INIT();
  STAMP(0);
  const int x = blockIdx.x & 7, idx = blockIdx.x >> 3;
  if (idx < 22) role_att(p, L, x, idx, wave, lane, t0);
  else if (idx < 31) role_ffn(p, L, x, idx - 22, wave, lane, t0);
  else role_modb(p, L, x, wave, lane, t0, code0);
  STAMP_DUMP();
}

extern "C" void kernel_launch(void* const* d_in, const int* in_sizes, int n_in, void* d_out, int out_size, void* d_ws,
                              size_t ws_size, hipStream_t stream) {
  Params p;
  p.x = (const u16*)d_in[0];
  p.kc = (const u16*)d_in[1];
  p.vc = (const u16*)d_in[2];
  p.wq = (const u16*)d_in[3];
  p.wo = (const u16*)d_in[4];
  p.wg = (const u16*)d_in[6];
  p.wu = (const u16*)d_in[7];
  p.wd = (const u16*)d_in[8];
  p.c = (const u16*)d_in[9];
  p.wmod = (const u16*)d_in[10];
  p.out = (u16*)d_out;
  p.ws = (u64*)d_ws;
  hipLaunchKernelGGL(adaln_block_kernel, dim3(256), dim3(NTHR), 0, stream, p);
}
```

```cpp
#include <hip/hip_runtime.h>
#include <stdint.h>

typedef unsigned short u16;
typedef unsigned long long u64;
typedef unsigned v4u __attribute__((ext_vector_type(4)));
typedef float f32x4 __attribute__((ext_vector_type(4)));
typedef float f32x2 __attribute__((ext_vector_type(2)));
typedef __bf16 bf16x2 __attribute__((ext_vector_type(2)));
typedef short bf16x8 __attribute__((ext_vector_type(8)));
typedef __amdgpu_buffer_rsrc_t rsrc_t;
#define GAS __attribute__((address_space(1)))
#define LAS __attribute__((address_space(3)))
#define DEV __device__ __forceinline__
typedef GAS u64 gu64;
#define RLX_AGENT __ATOMIC_RELAXED, __HIP_MEMORY_SCOPE_AGENT

constexpr int D = 576, SK = 65536, WIN = 4096, DH = 128, FF = 4608, NMOD = 6 * D;
constexpr int NSPLIT = 11;
constexpr int NTHR = 576;
constexpr int SPIN_CAP = 1 << 16;
#define GATE_FFN 790
#define GATE_MODB 200
#define GATE_ATT 140
#define GATE_Q 470
#define GATE_FFN2 0
#define F_N1 0
#define F_D1 2
#define GATE_X2 0
#define POLL_SLEEP 0
#define Q_STAGGER 16
#define GATE_H1 260
#define DN_FIRST 4
#define MODA_HOLD 0
#define GATE_MODB2 120
#define CODE_PF 22528
#define DIAG 0
#define V_N1 0
#define V_D1 2
#define V_D2 24
#define ATT_N1 5
#define ATT_D1 2
#define ATT_D2 24

constexpr int G_EPOCH = 0;
constexpr int G_MOD = 64;
constexpr int G_Q = G_MOD + 4 * NMOD;
constexpr int G_O = G_Q + 32 * 2 * 128;
constexpr int G_WO = G_O + 16 * 8 * NSPLIT * 64;
constexpr int G_X2 = G_WO + 128 * 576;
constexpr int G_DN = G_X2 + 72 * 16;
constexpr int G_T0 = G_DN + 72 * 576;
constexpr int G_END = G_T0 + 64;
constexpr size_t G_STAMP = 1 << 20;

constexpr int L_K = 0;
constexpr int L_OL = 98304;
constexpr int L_Q = L_OL + 9216;
constexpr int L_MX = L_Q + 512;
constexpr int L_LS = L_MX + 64;
constexpr int L_OB = L_LS + 64;
constexpr int L_PF = L_OB + 128;
constexpr int L_WO = L_PF + 256;
constexpr int L_MA = L_WO + 36864;
constexpr int L_TAG = L_MA + 1536 + 16;
constexpr int L_END = L_TAG + 16;
constexpr int F_RED = 0;
constexpr int F_H = 16384;
constexpr int F_H2 = 17664;
constexpr int F_ACT = 20736;
constexpr int F_TR = 21248;
constexpr int F_ST = 27136;

struct Params {
  const u16 *x, *kc, *vc, *wq, *wo, *wg, *wu, *wd, *c, *wmod;
  u16* out;
  u64* ws;
};

#define STAMP(i) do {} while (0)
#define STAMP_INIT() do {} while (0)
#define STAMP_DUMP() do {} while (0)
#define L_EXTRA 0

DEV float bf2f(unsigned b) { return __uint_as_float(b << 16); }
DEV unsigned cvt2(float lo, float hi) {
  const f32x2 v = {lo, hi};
  return __builtin_bit_cast(unsigned, __builtin_convertvector(v, bf16x2));
}
DEV unsigned pkr(float f) { return cvt2(f, f); }
DEV float rbf(float f) { return __uint_as_float(cvt2(f, f) & 0xFFFF0000u); }
#define TAG_LO_BITS 4
constexpr unsigned TAGM = (1u << TAG_LO_BITS) - 1u;
struct Tag { unsigned hi, lo, rest; };
DEV Tag make_tag(u64 st) {
  const u64 nx = st * 6364136223846793005ull + 1442695040888963407ull;
  Tag t;
  t.hi = __builtin_amdgcn_readfirstlane((unsigned)(nx >> 32));
  t.rest = __builtin_amdgcn_readfirstlane((unsigned)nx);
  t.lo = t.rest >> (32 - TAG_LO_BITS);
  return t;
}
DEV u64 enc(float v, unsigned hi, unsigned lo) {
  const unsigned u = (__float_as_uint(v) + (TAGM + 1) / 2) & ~TAGM;
  return ((u64)hi << 32) | (u64)(u | lo);
}
DEV Tag tag_put(LAS unsigned char* L, u64 st, int lane) {
  const Tag t = make_tag(st);
  if (lane == 0) *(LAS v4u*)(L + L_TAG) = v4u{t.hi, t.lo, t.rest, 0u};
  return t;
}
DEV Tag tag_get(const LAS unsigned char* L) {
  const v4u v = *(const LAS v4u*)(L + L_TAG);
  Tag t; t.hi = __builtin_amdgcn_readfirstlane(v[0]); t.lo = __builtin_amdgcn_readfirstlane(v[1]); t.rest = __builtin_amdgcn_readfirstlane(v[2]);
  return t;
}
DEV unsigned or_xor(unsigned a, unsigned b, unsigned c) { return __builtin_amdgcn_bitop3_b32(a, b, c, 0xF6); }
struct Chk {
  unsigned a = 0, b = 0;
  DEV void add(u64 g, const Tag& t) { a = or_xor(a, (unsigned)(g >> 32), t.hi); b = or_xor(b, (unsigned)g, t.lo); }
  DEV bool all_ok() const { return __builtin_amdgcn_ballot_w64((a | (b & TAGM)) != 0u) == 0ull; }
};
DEV float gval(u64 g) { return __uint_as_float((unsigned)g & ~TAGM); }
DEV void pub(u64* ws, int idx, float v, const Tag& t) { __hip_atomic_store((gu64*)ws + idx, enc(v, t.hi, t.lo), RLX_AGENT); }
DEV void pubp(gu64* dst, float v, const Tag& t) { __hip_atomic_store(dst, enc(v, t.hi, t.lo), RLX_AGENT); }
DEV u64 peek(u64* ws, int idx) { return __hip_atomic_load((gu64*)ws + idx, RLX_AGENT); }
template <int N, class F> DEV void gather(u64* ws, const Tag& t, u64 (&g)[N], F idx) {
  for (int spin = 0; spin < SPIN_CAP; ++spin) {
#pragma unroll
    for (int k = 0; k < N; ++k) g[k] = peek(ws, idx(k));
    Chk c;
#pragma unroll
    for (int k = 0; k < N; ++k) c.add(g[k], t);
    if (c.all_ok()) break;
    if (POLL_SLEEP) __builtin_amdgcn_s_sleep(POLL_SLEEP);
  }
}

struct G2 { v4u v; DEV float lo() const { return __uint_as_float(v[0] & ~TAGM); } DEV float hi() const { return __uint_as_float(v[2] & ~TAGM); } };
template <int N, class F> DEV void gather2(rsrc_t rw, const Tag& t, G2 (&g)[N], F idx) {
  for (int spin = 0; spin < SPIN_CAP; ++spin) {
    asm volatile("" ::: "memory");
#pragma unroll
    for (int k = 0; k < N; ++k) g[k].v = __builtin_amdgcn_raw_buffer_load_b128(rw, idx(k) * 8, 0, 16);
    unsigned a = 0, b = 0;
#pragma unroll
    for (int k = 0; k < N; ++k) { a = or_xor(or_xor(a, g[k].v[1], t.hi), g[k].v[3], t.hi); b = or_xor(or_xor(b, g[k].v[0], t.lo), g[k].v[2], t.lo); }
    if (__builtin_amdgcn_ballot_w64((a | (b & TAGM)) != 0u) == 0ull) break;
    if (POLL_SLEEP) __builtin_amdgcn_s_sleep(POLL_SLEEP);
  }
}
template <int N, class F> DEV void pretouch(LAS unsigned char* L, u64* ws, F idx) {
  const unsigned dump = (unsigned)(uintptr_t)(L + L_PF);
#pragma unroll
  for (int k = 0; k < N; ++k)
    asm volatile("s_mov_b32 m0, %0\n\ts_nop 0\n\tglobal_load_lds_dword %1, off sc1" :: "s"(dump), "v"((gu64*)ws + idx(k)) : "m0", "memory");
}
DEV f32x4 mfma(v4u a, v4u b, f32x4 c) {
  return __builtin_amdgcn_mfma_f32_16x16x32_bf16(__builtin_bit_cast(bf16x8, a), __builtin_bit_cast(bf16x8, b), c, 0, 0, 0);
}
DEV rsrc_t mk_rsrc(const void* p, unsigned bytes) { return __builtin_amdgcn_make_buffer_rsrc((void*)p, 0, (int)bytes, 0x00020000); }
DEV v4u ldb(rsrc_t r, unsigned voff, unsigned soff) { return __builtin_amdgcn_raw_buffer_load_b128(r, (int)voff, (int)soff, 2); }
#define SMALL_AUX 0
DEV unsigned ldb16(rsrc_t r, unsigned voff) { return __builtin_amdgcn_raw_buffer_load_b16(r, (int)voff, 0, SMALL_AUX); }
#define E_AUX 16
DEV u64 load_state(u64* ws) {
  int zoff = G_EPOCH * 8;
  asm volatile("" : "+v"(zoff));
  typedef unsigned v2u_ __attribute__((ext_vector_type(2)));
  const v2u_ v = __builtin_amdgcn_raw_buffer_load_b64(mk_rsrc(ws, 64), zoff, 0, E_AUX);
  return ((u64)v[1] << 32) | v[0];
}
DEV void bar() {
  asm volatile("s_waitcnt lgkmcnt(0)" ::: "memory");
  __builtin_amdgcn_s_barrier();
  asm volatile("" ::: "memory");
}
#define PIN(x) asm volatile("" : "+v"(x))
#define PINS(x) asm volatile("" : "+s"(x))
DEV void lds_wait() { asm volatile("s_waitcnt lgkmcnt(0)" ::: "memory"); }
template <int CTRL> DEV float dpp(float x) {
  return __builtin_bit_cast(float, __builtin_amdgcn_mov_dpp(__builtin_bit_cast(int, x), CTRL, 0xf, 0xf, true));
}
constexpr int ROR8 = 0x128, ROR4 = 0x124, ROR2 = 0x122, ROR1 = 0x121, XOR1 = 0xB1, XOR2 = 0x4E;
DEV float fmx(float a, float b) { float r; asm("v_max_f32 %0, %1, %2\n\ts_nop 1" : "=v"(r) : "v"(a), "v"(b)); return r; }
DEV float fmx3(float a, float b, float c) { float r; asm("v_max3_f32 %0, %1, %2, %3" : "=v"(r) : "v"(a), "v"(b), "v"(c)); return r; }
DEV float fmx3n(float a, float b, float c) { float r; asm("v_max3_f32 %0, %1, %2, %3\n\ts_nop 1" : "=v"(r) : "v"(a), "v"(b), "v"(c)); return r; }
DEV float x16_sum(float x) {
  auto s = __builtin_amdgcn_permlane16_swap(__float_as_uint(x), __float_as_uint(x), false, false);
  x = __uint_as_float(s[0]) + __uint_as_float(s[1]);
  auto t = __builtin_amdgcn_permlane32_swap(__float_as_uint(x), __float_as_uint(x), false, false);
  return __uint_as_float(t[0]) + __uint_as_float(t[1]);
}
DEV float x16_max(float x) {
  auto s = __builtin_amdgcn_permlane16_swap(__float_as_uint(x), __float_as_uint(x), false, false);
  x = fmaxf(__uint_as_float(s[0]), __uint_as_float(s[1]));
  auto t = __builtin_amdgcn_permlane32_swap(__float_as_uint(x), __float_as_uint(x), false, false);
  return fmaxf(__uint_as_float(t[0]), __uint_as_float(t[1]));
}
DEV float row_sum(float x) { x += dpp<ROR8>(x); x += dpp<ROR4>(x); x += dpp<ROR2>(x); x += dpp<ROR1>(x); return x; }
DEV float row_max(float x) { x = fmaxf(x, dpp<ROR8>(x)); x = fmaxf(x, dpp<ROR4>(x)); x = fmaxf(x, dpp<ROR2>(x)); x = fmaxf(x, dpp<ROR1>(x)); return x; }
DEV float wave_sum(float x) { return x16_sum(row_sum(x)); }
constexpr float LOG2E = 1.4426950408889634f;
DEV float fadd(float a, float b) { float r; asm("v_add_f32 %0, %1, %2" : "=v"(r) : "v"(a), "v"(b)); return r; }
DEV float faddn(float a, float b) { float r; asm("v_add_f32 %0, %1, %2\n\ts_nop 1" : "=v"(r) : "v"(a), "v"(b)); return r; }
DEV float exp_f(float x) { return __builtin_amdgcn_exp2f(x * LOG2E); }
DEV float rcp_f(float x) { return __builtin_amdgcn_rcpf(x); }
DEV float pair_pick(float a, float b, int lane) {
  const float a2 = dpp<0x50>(a), b2 = dpp<0x50>(b);
  const float a3 = dpp<0xFA>(a), b3 = dpp<0xFA>(b);
  const float lo = (lane & 1) ? b2 : a2, hi = (lane & 1) ? b3 : a3;
  return (lane & 4) ? hi : lo;
}
DEV float rdlane(float x, int l) { return __uint_as_float(__builtin_amdgcn_readlane(__float_as_uint(x), l)); }
DEV void gate_wait(u64 t0, unsigned ticks) { while (__builtin_amdgcn_s_memrealtime() - t0 < (u64)ticks) __builtin_amdgcn_s_sleep(2); }

struct Mask { unsigned m[4]; };
DEV Mask make_mask(int lane, bool on) {
  Mask k;
  const unsigned h = on ? ((lane & 1) ? 0xFFFF0000u : 0x0000FFFFu) : 0u;
#pragma unroll
  for (int r = 0; r < 4; ++r) k.m[r] = (((lane & 7) >> 1) == r) ? h : 0u;
  return k;
}
DEV v4u afrag(unsigned pp, const Mask& k) { return v4u{pp & k.m[0], pp & k.m[1], pp & k.m[2], pp & k.m[3]}; }

DEV unsigned ln_apply_pk(float x, float mean_b, float rden, float c1, float shift) {
  const float ln = rbf(rbf(x - mean_b) * rden);
  return pkr(rbf(ln * c1) + shift);
}
DEV float ln_rden(float var) { return rcp_f(rbf(__builtin_amdgcn_sqrtf(rbf(rbf(var) + 1e-5f)))); }

DEV void red_store(LAS float* area, f32x4 d, int lane) {
  const int u = (lane >> 3) & 1;
  if ((lane >> 5) == u) *(LAS f32x4*)(area + u * 64 + 8 * (lane & 7) + 4 * ((lane >> 4) & 1)) = d;
}
DEV unsigned red_off(int lane, int dump) {
  const int u = (lane >> 3) & 1;
  return 4 * ((lane >> 5) == u ? u * 64 + 8 * (lane & 7) + 4 * ((lane >> 4) & 1) : dump);
}
DEV unsigned silu_pk(unsigned craw) { const float v = bf2f(craw); return pkr(v * rcp_f(1.f + exp_f(-v))); }

struct WoAddr { unsigned tro, tri, obo, wro; gu64* dst; };
DEV WoAddr wo_prep(u64* ws, int pidx, int wave, int lane) {
  WoAddr a;
  a.tro = L_OL + wave * 640 + red_off(lane, 128); a.tri = L_OL + wave * 640 + lane * 4; a.obo = L_OB + (lane >> 3) * 16; a.wro = L_WO + wave * 4096 + lane * 16;
  a.dst = (gu64*)ws + G_WO + pidx * 576 + wave * 64 + lane;
  PIN(a.tro); PIN(a.tri); PIN(a.obo); PIN(a.wro); PIN(a.dst);
  return a;
}
DEV void wo_phase(LAS unsigned char* L, const WoAddr& a, const Tag& tag, const Mask& mk) {
  bar();
  v4u ob = *(const LAS v4u*)(L + a.obo);
  const LAS v4u* wr = (const LAS v4u*)(L + a.wro);
  v4u w0 = wr[0], w1 = wr[64], w2 = wr[128], w3 = wr[192];
  PIN(ob); PIN(w0); PIN(w1); PIN(w2); PIN(w3);
  __builtin_amdgcn_sched_barrier(0);
  f32x4 d0 = {0.f, 0.f, 0.f, 0.f}, d1 = {0.f, 0.f, 0.f, 0.f};
  d0 = mfma(afrag(ob[0], mk), w0, d0); d1 = mfma(afrag(ob[1], mk), w1, d1);
  d0 = mfma(afrag(ob[2], mk), w2, d0); d1 = mfma(afrag(ob[3], mk), w3, d1);
  *(LAS f32x4*)(L + a.tro) = d0 + d1;
  lds_wait();
  pubp(a.dst, fadd(*(const LAS float*)(L + a.tri), *(const LAS float*)(L + a.tri + 256)), tag);
}

struct ModA { v4u w[18]; unsigned c[3]; };
DEV void moda_load(const Params& p, int mt, int lane, ModA& m) {
  const rsrc_t rm = mk_rsrc(p.wmod, D * NMOD * 2), rc = mk_rsrc(p.c, D * 2);
  const int r0 = (mt & 3) * 144;
#pragma unroll
  for (int k = 0; k < 3; ++k) m.c[k] = ldb16(rc, (r0 + (k < 2 || lane < 16 ? lane + 64 * k : 143)) * 2);
  const unsigned v = (r0 + (lane >> 3)) * (NMOD * 2) + (lane & 7) * 16;
#pragma unroll
  for (int t = 0; t < 18; ++t) m.w[t] = ldb(rm, v, (mt >> 2) * 128 + t * 8 * (NMOD * 2));
}
DEV void moda_finish(LAS unsigned char* L, u64* ws, int mt, int lane, ModA& m, const Mask& mk, LAS float* tr, const Tag& tag) {
  unsigned sp[18];
  {
    const unsigned s0 = silu_pk(m.c[0]), s1 = silu_pk(m.c[1]), s2 = silu_pk(m.c[2]);
#pragma unroll
    for (int t = 0; t < 18; ++t) sp[t] = __builtin_amdgcn_ds_bpermute(((8 * t + (lane >> 3)) & 63) * 4, t < 8 ? s0 : t < 16 ? s1 : s2);
  }
  gu64* dst = (gu64*)ws + G_MOD + (mt & 3) * NMOD + (mt >> 2) * 64 + lane;
  unsigned tro = red_off(lane, 128);
  PIN(dst); PIN(tro);
#pragma unroll
  for (int t = 0; t < 18; ++t) PIN(sp[t]);
  __builtin_amdgcn_sched_barrier(0);
  f32x4 d0 = {0.f, 0.f, 0.f, 0.f}, d1 = {0.f, 0.f, 0.f, 0.f};
#pragma unroll
  for (int t = 0; t < 18; t += 2) {
    d0 = mfma(afrag(sp[t], mk), m.w[t], d0);
    __builtin_amdgcn_sched_barrier(0);
    d1 = mfma(afrag(sp[t + 1], mk), m.w[t + 1], d1);
    __builtin_amdgcn_sched_barrier(0);
  }
  *(LAS f32x4*)((LAS unsigned char*)tr + tro) = d0 + d1;
  lds_wait();
  pubp(dst, fadd(tr[lane], tr[64 + lane]), tag);
}

DEV void role_att(const Params& p, LAS unsigned char* L, int x, int i, int wave, int lane, u64 t0) {
  const int g = 2 * x + (i >= NSPLIT ? 1 : 0), j = i >= NSPLIT ? i - NSPLIT : i;
  const int ntile = j < 8 ? 23 : 24, tile0 = j < 8 ? 23 * j : 184 + 24 * (j - 8);
  const bool owner = j < 8;
  int redc = -1;
  if (j >= 8) redc = 9 * x + (g & 1) * 3 + (j - 8);
  else if (j == 0) redc = 9 * x + 6 + (g & 1);
  else if (j == 1 && (g & 1) == 0) redc = 9 * x + 8;
  u64* ws = p.ws;
  const Mask mk = make_mask(lane, true);
  const int kap = lane >> 4, cc = lane & 15;
  const bool moda = i < 9;

  if (wave < 8) {
    const size_t hb = ((size_t)g * SK + (SK - WIN)) * DH;
    const rsrc_t rk = mk_rsrc(p.kc + hb, WIN * DH * 2), rv = mk_rsrc(p.vc + hb, WIN * DH * 2);
    const bool three = wave + 16 < ntile;
    const unsigned tso = (tile0 + wave) * 4096;
    const unsigned ldsk = (unsigned)(uintptr_t)(L + L_K) + wave * 3 * 4096;
    unsigned vk[4];
#pragma unroll
    for (int q = 0; q < 4; ++q) vk[q] = (4 * q + kap) * 256 + ((cc ^ (4 * q + kap)) << 4);
#define KDMA(s, q) asm volatile("s_mov_b32 m0, %0\n\ts_nop 0\n\tbuffer_load_dwordx4 %1, %2, %3 offen nt lds" \
                     :: "s"(ldsk + (s) * 4096 + (q) * 1024), "v"(vk[q]), "s"(rk), "s"(tso + (s) * 32768) : "m0", "memory")
#define THR(n) do { if ((n) < ATT_N1) { if ((n) >= ATT_D1 && moda) asm volatile("s_waitcnt vmcnt(%0)" :: "n"(ATT_D1 - 1) : "memory"); } \
                     else if ((n) >= ATT_D2) asm volatile("s_waitcnt vmcnt(%0)" :: "n"(ATT_D2 - 1) : "memory"); } while (0)
#define THR_DEPTH(n) ((n) < ATT_N1 ? ATT_D1 : ATT_D2)
#define THRV(t) asm volatile("s_waitcnt vmcnt(%0)" :: "n"(((t) < V_N1 ? V_D1 : V_D2) - 1) : "memory")
    if (moda) __builtin_amdgcn_s_barrier();
    __builtin_amdgcn_s_setprio(2);
    KDMA(0, 0);
    if (GATE_ATT) gate_wait(t0, GATE_ATT);
    if (MODA_HOLD && moda) __builtin_amdgcn_s_barrier();
    THR(1); KDMA(0, 1); THR(2); KDMA(0, 2); THR(3); KDMA(0, 3);
    THR(4); KDMA(1, 0); THR(5); KDMA(1, 1); THR(6); KDMA(1, 2); THR(7); KDMA(1, 3);
    if (three) { THR(8); KDMA(2, 0); THR(9); KDMA(2, 1); THR(10); KDMA(2, 2); THR(11); KDMA(2, 3); }
#undef KDMA
    __builtin_amdgcn_s_setprio(0);
    v4u vr[12];
    const unsigned vv = kap * 1024 + cc * 16;
#pragma unroll
    for (int t = 0; t < 8; ++t) { THRV(t); vr[t] = ldb(rv, vv + (t & 3) * 256, tso + (t >> 2) * 32768); }
    if (three) {
#pragma unroll
      for (int t = 8; t < 12; ++t) { THRV(t); vr[t] = ldb(rv, vv + (t & 3) * 256, tso + 65536); }
    } else {
#pragma unroll
      for (int t = 8; t < 12; ++t) vr[t] = v4u{0u, 0u, 0u, 0u};
    }
#undef THR
#undef THRV
#undef THR_DEPTH
    __builtin_amdgcn_sched_barrier(0);
    STAMP(1);
    if (three) asm volatile("s_waitcnt vmcnt(12)" ::: "memory");
    else asm volatile("s_waitcnt vmcnt(8)" ::: "memory");
    v4u kf[12];
#pragma unroll
    for (int s = 0; s < 3; ++s)
#pragma unroll
      for (int ks = 0; ks < 4; ++ks)
        kf[s * 4 + ks] = *(const LAS v4u*)(L + L_K + (wave * 3 + (s < 2 || three ? s : 0)) * 4096 + cc * 256 + (((4 * ks + kap) ^ cc) << 4));
#pragma unroll
    for (int t = 0; t < 12; ++t) PIN(kf[t]);
    const int head = (lane >> 3) & 1;
    const int me = (wave & 3) * 64 + lane;
    unsigned qsrc = L_Q + head * 256 + kap * 16, mxw = L_MX + (head * 8 + wave) * 4, mxr = L_MX + head * 32, olw = L_OL + wave * 1024 + lane * 16;
    unsigned msrc = L_OL + ((16 * (((me >> 7) * 8 + (me & 7)) >> 2) + ((me & 127) >> 3)) * 4 + (me & 3)) * 4;
    gu64* mdst = (gu64*)ws + G_O + ((g * 8 + (me >> 5)) * NSPLIT + j) * 64 + (me & 31);
    bool lead = (lane & 0x37) == 0;
    PIN(qsrc); PIN(mxw); PIN(mxr); PIN(olw); PIN(msrc); PIN(mdst);
    STAMP(3);
    bar();
    STAMP(2);
    v4u qf[4];
#pragma unroll
    for (int ks = 0; ks < 4; ++ks) qf[ks] = *(const LAS v4u*)(L + qsrc + ks * 64);
#pragma unroll
    for (int ks = 0; ks < 4; ++ks) PIN(qf[ks]);
    __builtin_amdgcn_sched_barrier(0);
    f32x4 acc[3] = {{0.f, 0.f, 0.f, 0.f}, {0.f, 0.f, 0.f, 0.f}, {0.f, 0.f, 0.f, 0.f}};
#pragma unroll
    for (int ks = 0; ks < 4; ++ks)
#pragma unroll
      for (int s = 0; s < 3; ++s) acc[s] = mfma(kf[s * 4 + ks], qf[ks], acc[s]);
    __builtin_amdgcn_sched_barrier(0);
    float sc[12];
#pragma unroll
    for (int t = 0; t < 12; t += 2) {
      const unsigned u = cvt2(acc[t >> 2][t & 3], acc[t >> 2][(t & 3) + 1]);
      const unsigned w = cvt2(__uint_as_float(u << 16) * 0.08838834764831845f, __uint_as_float(u & 0xFFFF0000u) * 0.08838834764831845f);
      sc[t] = __uint_as_float(w << 16);
      sc[t + 1] = __uint_as_float(w & 0xFFFF0000u);
    }
    if (!three) {
#pragma unroll
      for (int t = 8; t < 12; ++t) sc[t] = -INFINITY;
    }
    float mx = fmx3n(fmx3(fmx3(sc[0], sc[1], sc[2]), fmx3(sc[3], sc[4], sc[5]), fmx3(sc[6], sc[7], sc[8])), fmx3(sc[9], sc[10], sc[11]), sc[0]);
    mx = x16_max(mx);
    if (lead) *(LAS float*)(L + mxw) = mx;
    bar();
    float m;
    {
      const f32x4 a = *(const LAS f32x4*)(L + mxr), b = *(const LAS f32x4*)(L + mxr + 16);
      m = fmx3(fmx3(a[0], a[1], a[2]), fmx3(a[3], b[0], b[1]), fmx(b[2], b[3]));
    }
    v4u af[12];
    float ls0 = 0.f, ls1 = 0.f;
    const float mneg = -m * LOG2E;
#pragma unroll
    for (int t = 0; t < 12; t += 2) {
      const float e0 = __builtin_amdgcn_exp2f(fmaf(sc[t], LOG2E, mneg)), e1 = __builtin_amdgcn_exp2f(fmaf(sc[t + 1], LOG2E, mneg));
      ls0 += e0; ls1 += e1;
      af[t] = afrag(pkr(e0), mk); af[t + 1] = afrag(pkr(e1), mk);
    }
    const float ls = x16_sum(ls0 + ls1);
    if (lead) *(LAS float*)(L + mxw + 64) = ls;
    const Tag tag = tag_get(L);
    __builtin_amdgcn_sched_barrier(0);
    STAMP(4);
    f32x4 o0 = {0.f, 0.f, 0.f, 0.f}, o1 = {0.f, 0.f, 0.f, 0.f};
#pragma unroll
    for (int t = 0; t < 12; t += 2) { o0 = mfma(af[t], vr[t], o0); o1 = mfma(af[t + 1], vr[t + 1], o1); }
    *(LAS f32x4*)(L + olw) = o0 + o1;
    STAMP(5);
    bar();
    if (wave < 4) {
      const LAS float* src = (const LAS float*)(L + msrc);
      float s[8];
#pragma unroll
      for (int w = 0; w < 8; ++w) s[w] = src[w * 256];
      pubp(mdst, fadd(fadd(fadd(s[0], s[1]), fadd(s[2], s[3])), fadd(fadd(s[4], s[5]), fadd(s[6], s[7]))), tag);
    } else if (wave == 4 && lane < 16) {
      const int u = lane >> 1, hh = u >> 2;
      const f32x4 a = *(const LAS f32x4*)(L + ((lane & 1) ? L_LS : L_MX) + hh * 32), b = *(const LAS f32x4*)(L + ((lane & 1) ? L_LS : L_MX) + hh * 32 + 16);
      const float vs = ((a[0] + a[1]) + (a[2] + a[3])) + ((b[0] + b[1]) + (b[2] + b[3]));
      const float vm = fmaxf(fmaxf(fmaxf(a[0], a[1]), fmaxf(a[2], a[3])), fmaxf(fmaxf(b[0], b[1]), fmaxf(b[2], b[3])));
      pub(ws, G_O + ((g * 8 + u) * NSPLIT + j) * 64 + 32 + (lane & 1), (lane & 1) ? vs : vm, tag);
    }
    STAMP(6);
    if (owner) {
      wo_phase(L, wo_prep(ws, g * 8 + j, wave, lane), tag, mk);
      STAMP(8);
    }
  } else {
    __builtin_amdgcn_s_setprio(3);
    Tag tag;
    {
      if (moda) {
        u64 st = load_state(ws);
        ModA ma;
        moda_load(p, 9 * x + i, lane, ma);
        __builtin_amdgcn_sched_barrier(0);
        __builtin_amdgcn_s_barrier();
        asm volatile("" : "+v"(st));
        tag = tag_put(L, st, lane);
        moda_finish(L, ws, 9 * x + i, lane, ma, mk, (LAS float*)(L + L_MA), tag);
        STAMP(9);
        if (DIAG == 3 && x == 0 && i == 0) { const unsigned dt = (unsigned)(__builtin_amdgcn_s_memrealtime() - t0); if (lane == 0) __hip_atomic_store((gu64*)ws + G_T0, ((u64)tag.hi << 32) | (u64)((dt << 8) | tag.lo), RLX_AGENT); }
        if (MODA_HOLD) __builtin_amdgcn_s_barrier();
      } else {
        tag = tag_put(L, load_state(ws), lane);
      }
    }
    float xr = 0.f;
    if (redc >= 0) xr = bf2f(p.x[redc * 8 + (lane & 7)]);
    if (GATE_Q) gate_wait(t0, GATE_Q);
    const rsrc_t rw = mk_rsrc(ws, G_END * 8);
    {
      G2 gq[4];
      gather2<4>(rw, tag, gq, [&](int k) { return G_Q + ((2 * g + (k >> 1)) * 2 + (k & 1)) * 128 + 2 * lane; });
      ((LAS unsigned*)(L + L_Q))[lane] = cvt2(fadd(gq[0].lo(), gq[1].lo()), fadd(gq[0].hi(), gq[1].hi()));
      ((LAS unsigned*)(L + L_Q))[64 + lane] = cvt2(fadd(gq[2].lo(), gq[3].lo()), fadd(gq[2].hi(), gq[3].hi()));
    }
    STAMP(1);
    if (DIAG == 4 && x == 0 && i == 0) { const unsigned dt = (unsigned)(__builtin_amdgcn_s_memrealtime() - t0); if (lane == 0) __hip_atomic_store((gu64*)ws + G_T0, ((u64)tag.hi << 32) | (u64)((dt << 8) | tag.lo), RLX_AGENT); }
    if (owner) {
      const int ob = G_O + (g * 8 + j) * NSPLIT * 64;
      pretouch<1>(L, ws, [&](int) { const int q_ = lane < 33 ? lane : 32; return ob + (q_ / 3) * 64 + (q_ % 3) * 16; });
    }
    if (redc >= 0) {
      pretouch<2>(L, ws, [&](int s) { return G_WO + (s * 64 + lane) * 576 + redc * 8; });
      pretouch<2>(L, ws, [&](int s) { const int f_ = s * 64 + lane; return G_DN + (f_ < 72 ? f_ : 71) * 576 + redc * 8; });
    }
    bar();
    if (owner) {
      const rsrc_t rwo = mk_rsrc(p.wo, 4096 * D * 2);
      const unsigned wo_v = (lane >> 3) * (D * 2) + (lane & 7) * 16;
      const unsigned wo_s = (g * 256 + j * 32) * (D * 2), ldsw = (unsigned)(uintptr_t)(L + L_WO);
#pragma unroll
      for (int w = 0; w < 9; ++w)
#pragma unroll
        for (int t = 0; t < 4; ++t)
          asm volatile("s_mov_b32 m0, %0\n\ts_nop 0\n\tbuffer_load_dwordx4 %1, %2, %3 offen nt lds"
                       :: "s"(ldsw + (w * 4 + t) * 1024), "v"(wo_v), "s"(rwo), "s"(wo_s + w * 128 + t * 8 * (D * 2)) : "m0", "memory");
    }
    bar();
    bar();
    STAMP(2);
    if (owner) {
      const int ob = G_O + (g * 8 + j) * NSPLIT * 64;
      const int k2 = kap < 3 ? 8 + kap : 10;
      unsigned obw = L_OB + (((2 * cc) & 7) * 4 + (cc >> 2)) * 4;
      float live = kap < 3 ? 1.f : 0.f;
      PIN(obw); PIN(live);
      const WoAddr wa = wo_prep(ws, g * 8 + j, 8, lane);
      G2 go[6];
      gather2<6>(rw, tag, go, [&](int k) { const int sp = k % 3 == 0 ? kap : k % 3 == 1 ? 4 + kap : k2; return ob + sp * 64 + (k < 3 ? 2 * cc : 32); });
      STAMP(3);
      const float M = x16_max(fmaxf(fmaxf(go[3].lo(), go[4].lo()), go[5].lo()));
      const float Mn = -M * LOG2E;
      const float w0 = __builtin_amdgcn_exp2f(fmaf(go[3].lo(), LOG2E, Mn)), w1 = __builtin_amdgcn_exp2f(fmaf(go[4].lo(), LOG2E, Mn));
      const float w2 = __builtin_amdgcn_exp2f(fmaf(go[5].lo(), LOG2E, Mn)) * live;
      const float Ls = x16_sum(fmaf(w2, go[5].hi(), fmaf(w1, go[4].hi(), w0 * go[3].hi())));
      const float o0 = x16_sum(fmaf(w2, go[2].lo(), fmaf(w1, go[1].lo(), w0 * go[0].lo())));
      const float o1 = x16_sum(fmaf(w2, go[2].hi(), fmaf(w1, go[1].hi(), w0 * go[0].hi())));
      const float rl = rcp_f(Ls);
      *(LAS unsigned*)(L + obw) = pkr(o0 * rl);
      *(LAS unsigned*)(L + obw + 16) = pkr(o1 * rl);
      asm volatile("s_waitcnt vmcnt(0)" ::: "memory");
      wo_phase(L, wa, tag, mk);
      STAMP(4);
    }
    if (redc >= 0) {
      u64 gg[8];
      gather<8>(ws, tag, gg, [&](int k) { return G_MOD + (k & 3) * NMOD + (k < 4 ? 2 : 5) * D + redc * 8 + (lane & 7); });
      float ga = rbf(fadd(fadd(gval(gg[0]), gval(gg[1])), fadd(gval(gg[2]), gval(gg[3]))));
      float gb = rbf(fadd(fadd(gval(gg[4]), gval(gg[5])), fadd(gval(gg[6]), gval(gg[7]))));
      gu64* xdst = (gu64*)ws + G_X2 + redc * 16 + (lane & 7);
      GAS u16* odst = (GAS u16*)p.out + redc * 8 + (lane & 7);
      PIN(ga); PIN(gb); PIN(xr); PIN(xdst); PIN(odst);
      float x2;
      {
        G2 gw[8];
        gather2<8>(rw, tag, gw, [&](int s) { return G_WO + (s * 16 + (lane >> 2)) * 576 + redc * 8 + 2 * (lane & 3); });
        STAMP(5);
        float t0_ = faddn(fadd(fadd(gw[0].lo(), gw[1].lo()), fadd(gw[2].lo(), gw[3].lo())), fadd(fadd(gw[4].lo(), gw[5].lo()), fadd(gw[6].lo(), gw[7].lo())));
        float t1_ = faddn(fadd(fadd(gw[0].hi(), gw[1].hi()), fadd(gw[2].hi(), gw[3].hi())), fadd(fadd(gw[4].hi(), gw[5].hi()), fadd(gw[6].hi(), gw[7].hi())));
        t0_ += dpp<ROR8>(t0_); t1_ += dpp<ROR8>(t1_); t0_ += dpp<ROR4>(t0_); t1_ += dpp<ROR4>(t1_);
        t0_ = x16_sum(t0_); t1_ = x16_sum(t1_);
        const float ts = pair_pick(t0_, t1_, lane);
        x2 = rbf(xr + rbf(ga * rbf(ts)));
        if (lane < 8) pubp(xdst, x2, tag);
      }
      STAMP(6);
      {
        G2 gd[5];
        float w4 = lane < 32 ? 1.f : 0.f;
        PIN(w4);
        gather2<5>(rw, tag, gd, [&](int s) { const int f_ = s * 16 + (lane >> 2); return G_DN + (f_ < 72 ? f_ : 71) * 576 + redc * 8 + 2 * (lane & 3); });
        STAMP(7);
        float t0_ = fmaf(w4, gd[4].lo(), fadd(fadd(gd[0].lo(), gd[1].lo()), fadd(gd[2].lo(), gd[3].lo())));
        float t1_ = fmaf(w4, gd[4].hi(), fadd(fadd(gd[0].hi(), gd[1].hi()), fadd(gd[2].hi(), gd[3].hi())));
        t0_ += dpp<ROR8>(t0_); t1_ += dpp<ROR8>(t1_); t0_ += dpp<ROR4>(t0_); t1_ += dpp<ROR4>(t1_);
        t0_ = x16_sum(t0_); t1_ = x16_sum(t1_);
        const float ts = pair_pick(t0_, t1_, lane);
        const unsigned o = pkr(x2 + rbf(gb * rbf(ts)));
        if (lane < 8) *odst = (u16)o;
        if (DIAG && redc == 0) {
          u64 gt[1];
          gather<1>(ws, tag, gt, [&](int) { return G_T0; });
          const unsigned dt = __builtin_amdgcn_readfirstlane((unsigned)gt[0] >> 8);
          const u64 tw = __builtin_amdgcn_s_memrealtime();
          while (__builtin_amdgcn_s_memrealtime() - tw < (u64)dt * 10) __builtin_amdgcn_s_sleep(1);
        }
        if (redc == 0 && lane == 0) __hip_atomic_store((gu64*)ws + G_EPOCH, ((u64)tag.hi << 32) | (u64)tag.rest, RLX_AGENT);
      }
      STAMP(8);
    }
  }
}

DEV void role_ffn(const Params& p, LAS unsigned char* L, int x, int i, int wave, int lane, u64 t0) {
  const int f = 9 * x + i;
  u64* ws = p.ws;
  const Mask mk = make_mask(lane, true);
  LAS float* red = (LAS float*)(L + F_RED);
  const rsrc_t rm = mk_rsrc(p.wmod, D * NMOD * 2), rc = mk_rsrc(p.c, D * 2), rx = mk_rsrc(p.x, D * 2);
  const int hq = 4 * x + (i >> 1), qr = i & 1;
  const rsrc_t rw = mk_rsrc(ws, G_END * 8);
  Tag tag;
  if (i < 8) {
    v4u wa[12];
    if (wave >= 3) {
      const rsrc_t rq = mk_rsrc(p.wq, D * 4096 * 2);
      const unsigned v = (lane >> 4) * 8192 + (lane & 15) * 16, s = (qr * 288 + (wave - 3) * 48) * 8192 + hq * 256;
#pragma unroll
      for (int t = 0; t < 12; ++t) wa[t] = ldb(rq, v, s + t * 4 * 8192);
      __builtin_amdgcn_sched_barrier(0);
      STAMP(1);
    } else {
      __builtin_amdgcn_s_setprio(3);
      const u64 st = load_state(ws);
      float xv[9];
#pragma unroll
      for (int k = 0; k < 9; ++k) xv[k] = bf2f(ldb16(rx, (lane + 64 * k) * 2));
      const int p0 = wave * 64 + lane, pr = p0 < 144 ? p0 : 143;
      const unsigned xo = __builtin_amdgcn_raw_buffer_load_b32(rx, (qr * 288 + 2 * pr) * 2, 0, 0);
      float s = 0.f;
#pragma unroll
      for (int k = 0; k < 9; ++k) s += xv[k];
      const float mf = wave_sum(s) * (1.f / D);
      float v = 0.f;
#pragma unroll
      for (int k = 0; k < 9; ++k) { const float d = xv[k] - mf; v = fmaf(d, d, v); }
      const float rden = ln_rden(wave_sum(v) * (1.f / D)), mean = rbf(mf);
      tag = wave == 0 ? tag_put(L, st, lane) : make_tag(st);
      const int hw = pr / 24, hr = 2 * pr - hw * 48;
      unsigned hdst = F_H + ((hw * 4 + (hr & 3)) * 12 + (hr >> 2)) * 4;
      float ln0 = rbf(rbf(__uint_as_float(xo << 16) - mean) * rden), ln1 = rbf(rbf(__uint_as_float(xo & 0xFFFF0000u) - mean) * rden);
      PIN(hdst); PIN(ln0); PIN(ln1);
      STAMP(2);
      if (GATE_H1) gate_wait(t0, GATE_H1);
      G2 ga[8];
      gather2<8>(rw, tag, ga, [&](int k) { return G_MOD + (k & 3) * NMOD + (k >> 2) * D + qr * 288 + 2 * pr; });
      STAMP(3);
      const float sa0 = rbf(fadd(fadd(ga[0].lo(), ga[1].lo()), fadd(ga[2].lo(), ga[3].lo()))), sa1 = rbf(fadd(fadd(ga[0].hi(), ga[1].hi()), fadd(ga[2].hi(), ga[3].hi())));
      const float ca0 = rbf(fadd(fadd(ga[4].lo(), ga[5].lo()), fadd(ga[6].lo(), ga[7].lo()))), ca1 = rbf(fadd(fadd(ga[4].hi(), ga[5].hi()), fadd(ga[6].hi(), ga[7].hi())));
      *(LAS unsigned*)(L + hdst) = pkr(rbf(ln0 * rbf(1.f + ca0)) + sa0);
      *(LAS unsigned*)(L + hdst + 48) = pkr(rbf(ln1 * rbf(1.f + ca1)) + sa1);
      __builtin_amdgcn_s_setprio(0);
    }
    Mask mq = make_mask(lane, (lane & 8) == 0);
    const int qe = (wave & 1) * 64 + lane;
    unsigned hsrc = F_H + ((wave - 3) * 4 + (lane >> 4)) * 48, qst = F_RED + 4608 + (wave - 3) * 1024 + lane * 16;
    unsigned qsrc = F_RED + 4608 + ((16 * ((qe & 7) >> 2) + (qe >> 3)) * 4 + (qe & 3)) * 4;
    gu64* qdst = (gu64*)ws + G_Q + (hq * 2 + qr) * 128 + qe;
    PIN(mq.m[0]); PIN(mq.m[1]); PIN(mq.m[2]); PIN(mq.m[3]); PIN(hsrc); PIN(qst); PIN(qsrc); PIN(qdst);
    bar();
    if (wave >= 3) {
      const LAS v4u* hp = (const LAS v4u*)(L + hsrc);
      const v4u h0 = hp[0], h1 = hp[1], h2 = hp[2];
      f32x4 d0 = {0.f, 0.f, 0.f, 0.f}, d1 = {0.f, 0.f, 0.f, 0.f};
#pragma unroll
      for (int t = 0; t < 4; ++t) { d0 = mfma(afrag(h0[t], mq), wa[t], d0); d1 = mfma(afrag(h1[t], mq), wa[4 + t], d1); }
#pragma unroll
      for (int t = 0; t < 4; t += 2) { d0 = mfma(afrag(h2[t], mq), wa[8 + t], d0); d1 = mfma(afrag(h2[t + 1], mq), wa[9 + t], d1); }
      *(LAS f32x4*)(L + qst) = d0 + d1;
    }
    bar();
    if (wave < 2) {
      const LAS float* src = (const LAS float*)(L + qsrc);
      pubp(qdst, fadd(fadd(fadd(src[0], src[256]), fadd(src[512], src[768])), fadd(src[1024], src[1280])), tag);
    }
  } else {
    if (wave == 0) tag_put(L, load_state(ws), lane);
    if (GATE_MODB2) gate_wait(t0, GATE_MODB2);
    ModA ma;
    if (wave < 6) moda_load(p, 72 + 6 * x + wave, lane, ma);
    __builtin_amdgcn_sched_barrier(0);
    bar();
    if (wave < 6) {
      tag = tag_get(L);
      moda_finish(L, ws, 72 + 6 * x + wave, lane, ma, mk, (LAS float*)(L + F_RED + wave * 1024), tag);
    }
  }
  tag = tag_get(L);
  STAMP(4);
  pretouch<1>(L, ws, [&](int) { const int e = wave * 64 + lane; return G_X2 + (e >> 3) * 16 + (e & 7); });
  const bool slow_tile = ((((unsigned)(uintptr_t)p.wg >> 7) + f) & 7) == 3;
  if (!slow_tile) gate_wait(t0, GATE_FFN);
#define THRF(n) do { if ((n) < F_N1 && !slow_tile && __builtin_amdgcn_s_memrealtime() - t0 < (u64)GATE_FFN2) asm volatile("s_waitcnt vmcnt(%0)" :: "n"(F_D1 - 1) : "memory"); } while (0)
  v4u wg[9], wu[9], wd[8];
  if (wave < 8) {
    const rsrc_t rg = mk_rsrc(p.wg, D * FF * 2), ru = mk_rsrc(p.wu, D * FF * 2);
    const unsigned v = (lane >> 3) * (FF * 2) + (lane & 7) * 16, s = wave * 72 * (FF * 2) + f * 128;
#pragma unroll
    for (int t = 0; t < 9; ++t) { THRF(2 * t); wg[t] = ldb(rg, v, s + t * 8 * (FF * 2)); THRF(2 * t + 1); wu[t] = ldb(ru, v, s + t * 8 * (FF * 2)); }
  }
  {
    const rsrc_t rd = mk_rsrc(p.wd, FF * D * 2);
    const unsigned v = (lane >> 3) * (D * 2) + (lane & 7) * 16, s = f * 64 * (D * 2) + wave * 128;
#pragma unroll
    for (int t = 0; t < DN_FIRST; ++t) { THRF(18 + t); wd[t] = ldb(rd, v, s + t * 8 * (D * 2)); }
  }
  __builtin_amdgcn_sched_barrier(0);
  STAMP(5);
  unsigned h2w, h2r, stw, str_, gur, guw0, actw, actr, trw, trr;
  gu64* ddst;
  {
    const int e = wave * 64 + lane, hw = e / 72, rem = e - hw * 72, c = (wave & 3) * 16 + (lane >> 2);
    h2w = F_H2 + ((hw * 8 + (rem & 7)) * 12 + (rem >> 3)) * 4;
    h2r = F_H2 + ((wave & 7) * 8 + (lane >> 3)) * 48;
    stw = F_ST + wave * 8;
    str_ = F_ST + ((lane & 15) < 9 ? (lane & 15) : 9) * 8;
    guw0 = F_RED + (wave & 7) * 1040 + red_off(lane, 256);
    gur = F_RED + ((lane & 3) * 520 + c) * 4;
    actw = F_ACT + ((lane & 3) == 0 ? (c & 7) * 8 + (c >> 3) : 64) * 4;
    actr = F_ACT + (lane >> 3) * 32;
    trw = F_TR + wave * 640 + red_off(lane, 128);
    trr = F_TR + wave * 640 + lane * 4;
    ddst = (gu64*)ws + G_DN + f * 576 + wave * 64 + lane;
    if (wave == 0 && lane == 0) *(LAS f32x2*)(L + F_ST + 72) = f32x2{0.f, 0.f};
  }
  PIN(h2w); PIN(h2r); PIN(stw); PIN(str_); PIN(guw0); PIN(gur); PIN(actw); PIN(actr); PIN(trw); PIN(trr); PIN(ddst);
  if (GATE_X2) gate_wait(t0, GATE_X2);
  {
    const int e = wave * 64 + lane;
    u64 gs[9];
    bool have_x2 = false;
    for (int spin = 0; spin < SPIN_CAP; ++spin) {
#pragma unroll
      for (int k = 0; k < 8; ++k) gs[k] = peek(ws, G_MOD + (k & 3) * NMOD + (k < 4 ? 3 : 4) * D + e);
      gs[8] = peek(ws, G_X2 + (e >> 3) * 16 + (e & 7));
      Chk c, cx;
#pragma unroll
      for (int k = 0; k < 8; ++k) c.add(gs[k], tag);
      cx.add(gs[8], tag);
      have_x2 = cx.all_ok();
      if (c.all_ok()) break;
    }
    float sb = rbf(fadd(fadd(gval(gs[0]), gval(gs[1])), fadd(gval(gs[2]), gval(gs[3]))));
    float c1b = rbf(1.f + rbf(fadd(fadd(gval(gs[4]), gval(gs[5])), fadd(gval(gs[6]), gval(gs[7])))));
    float on = (lane & 15) < 9 ? 64.f : 0.f;
    PIN(sb); PIN(c1b); PIN(on);
    STAMP(6);
    u64 gx[1] = {gs[8]};
    if (!have_x2) gather<1>(ws, tag, gx, [&](int) { return G_X2 + (e >> 3) * 16 + (e & 7); });
    STAMP(7);
    const float xv = gval(gx[0]);
    const float sh = __uint_as_float(__builtin_amdgcn_readfirstlane(__float_as_uint(xv)));
    const float d = xv - sh;
    float s1 = d, s2 = d * d;
    s1 += dpp<ROR8>(s1); s2 += dpp<ROR8>(s2); s1 += dpp<ROR4>(s1); s2 += dpp<ROR4>(s2);
    s1 += dpp<ROR2>(s1); s2 += dpp<ROR2>(s2); s1 += dpp<ROR1>(s1); s2 += dpp<ROR1>(s2);
    s1 = x16_sum(s1); s2 = x16_sum(s2);
    const float dm = s1 * (1.f / 64.f);
    if (lane == 0) *(LAS f32x2*)(L + stw) = f32x2{sh + dm, fmaf(-dm, s1, s2)};
    bar();
    const f32x2 st = *(const LAS f32x2*)(L + str_);
    {
      const rsrc_t rd = mk_rsrc(p.wd, FF * D * 2);
      const unsigned v = (lane >> 3) * (D * 2) + (lane & 7) * 16, s = f * 64 * (D * 2) + wave * 128;
#pragma unroll
      for (int t = DN_FIRST; t < 8; ++t) wd[t] = ldb(rd, v, s + t * 8 * (D * 2));
      __builtin_amdgcn_sched_barrier(0);
    }
    const float mf = row_sum(st[0]) * (1.f / 9.f);
    const float dv = st[0] - mf;
    const float m2 = row_sum(fmaf(on * dv, dv, st[1]));
    *(LAS unsigned*)(L + h2w) = ln_apply_pk(xv, rbf(mf), ln_rden(m2 * (1.f / D)), c1b, sb);
  }
  bar();
  STAMP(8);
  if (wave < 8) {
    const LAS v4u* hp = (const LAS v4u*)(L + h2r);
    const v4u h0 = hp[0], h1 = hp[1];
    const unsigned h8 = ((const LAS unsigned*)hp)[8];
    f32x4 dg = {0.f, 0.f, 0.f, 0.f}, du = {0.f, 0.f, 0.f, 0.f};
#pragma unroll
    for (int t = 0; t < 9; ++t) {
      const v4u a = afrag(t < 4 ? h0[t & 3] : t < 8 ? h1[t & 3] : h8, mk);
      dg = mfma(a, wg[t], dg);
      du = mfma(a, wu[t], du);
    }
    *(LAS f32x4*)(L + guw0) = dg;
    *(LAS f32x4*)(L + guw0 + ((lane >> 5) == ((lane >> 3) & 1) ? 512 : 0)) = du;
  }
  bar();
  if (wave < 4) {
    const LAS float* r0 = (const LAS float*)(L + gur);
    float g = faddn(fadd(r0[0], r0[64]), fadd(r0[260], r0[324])), u = faddn(fadd(r0[128], r0[192]), fadd(r0[388], r0[452]));
    g += dpp<XOR1>(g); u += dpp<XOR1>(u);
    g += dpp<XOR2>(g); u += dpp<XOR2>(u);
    g = rbf(g); u = rbf(u);
    const float y = g * fmaf(g * g, 0.044715f * 0.7978845608028654f * 2.f * LOG2E, 0.7978845608028654f * 2.f * LOG2E);
    const float th = fmaf(-2.f, rcp_f(__builtin_amdgcn_exp2f(y) + 1.f), 1.f);
    const float act = rbf(0.5f * g * (1.f + th)) * u;
    *(LAS unsigned*)(L + actw) = pkr(act);
  }
  bar();
  STAMP(9);
  {
    const LAS v4u* ap = (const LAS v4u*)(L + actr);
    const v4u a0 = ap[0], a1 = ap[1];
    f32x4 d0 = {0.f, 0.f, 0.f, 0.f}, d1 = {0.f, 0.f, 0.f, 0.f};
#pragma unroll
    for (int t = 0; t < 4; ++t) { d0 = mfma(afrag(a0[t], mk), wd[t], d0); d1 = mfma(afrag(a1[t], mk), wd[4 + t], d1); }
    *(LAS f32x4*)(L + trw) = d0 + d1;
    lds_wait();
    pubp(ddst, fadd(*(const LAS float*)(L + trr), *(const LAS float*)(L + trr + 256)), tag);
  }
  STAMP(10);
}

DEV void role_modb(const Params& p, LAS unsigned char* L, int x, int wave, int lane, u64 t0, u64 code0) {
  if (CODE_PF && wave == 8) {
    const unsigned dump = (unsigned)(uintptr_t)(L + L_PF);
#pragma unroll
    for (int k = 0; k < (CODE_PF + 8191) / 8192; ++k) {
      const unsigned o = k * 8192 + lane * 128;
      asm volatile("s_mov_b32 m0, %0\n\ts_nop 0\n\tglobal_load_lds_dword %1, off" :: "s"(dump), "v"(code0 + (o < CODE_PF - 128 ? o : CODE_PF - 128)) : "m0", "memory");
    }
  }
  const Mask mk = make_mask(lane, true);
  if (wave == 0) tag_put(L, load_state(p.ws), lane);
  bar();
  const Tag tag = tag_get(L);
  gate_wait(t0, GATE_MODB);
  for (int k = wave; k < 12; k += 9) {
    ModA ma;
    moda_load(p, 120 + x + 8 * k, lane, ma);
    __builtin_amdgcn_sched_barrier(0);
    moda_finish(L, p.ws, 120 + x + 8 * k, lane, ma, mk, (LAS float*)(L + wave * 1024), tag);
  }
  STAMP(2);
}

__global__ void __launch_bounds__(NTHR) adaln_block_kernel(Params p) {
  __shared__ __attribute__((aligned(16))) unsigned char smem[L_END + L_EXTRA];
  LAS unsigned char* L = (LAS unsigned char*)smem;
  const int lane = threadIdx.x & 63, wave = __builtin_amdgcn_readfirstlane(threadIdx.x >> 6);
  u64 code0;
  asm volatile("s_getpc_b64 %0" : "=s"(code0));
  code0 &= ~0xFFull;
  const u64 t0 = __builtin_amdgcn_s_memrealtime();
  STAMP_INIT();
  STAMP(0);
  const int x = blockIdx.x & 7, idx = blockIdx.x >> 3;
  if (idx < 22) role_att(p, L, x, idx, wave, lane, t0);
  else if (idx < 31) role_ffn(p, L, x, idx - 22, wave, lane, t0);
  else role_modb(p, L, x, wave, lane, t0, code0);
  STAMP_DUMP();
}

extern "C" void kernel_launch(void* const* d_in, const int* in_sizes, int n_in, void* d_out, int out_size, void* d_ws,
                              size_t ws_size, hipStream_t stream) {
  Params p;
  p.x = (const u16*)d_in[0];
  p.kc = (const u16*)d_in[1];
  p.vc = (const u16*)d_in[2];
  p.wq = (const u16*)d_in[3];
  p.wo = (const u16*)d_in[4];
  p.wg = (const u16*)d_in[6];
  p.wu = (const u16*)d_in[7];
  p.wd = (const u16*)d_in[8];
  p.c = (const u16*)d_in[9];
  p.wmod = (const u16*)d_in[10];
  p.out = (u16*)d_out;
  p.ws = (u64*)d_ws;
  hipLaunchKernelGGL(adaln_block_kernel, dim3(256), dim3(NTHR), 0, stream, p);
}
```

```cpp
#include <hip/hip_runtime.h>
#include <stdint.h>

typedef unsigned short u16;
typedef unsigned long long u64;
typedef unsigned v4u __attribute__((ext_vector_type(4)));
typedef float f32x4 __attribute__((ext_vector_type(4)));
typedef float f32x2 __attribute__((ext_vector_type(2)));
typedef __bf16 bf16x2 __attribute__((ext_vector_type(2)));
typedef short bf16x8 __attribute__((ext_vector_type(8)));
typedef __amdgpu_buffer_rsrc_t rsrc_t;
#define GAS __attribute__((address_space(1)))
#define LAS __attribute__((address_space(3)))
#define DEV __device__ __forceinline__
typedef GAS u64 gu64;
#define RLX_AGENT __ATOMIC_RELAXED, __HIP_MEMORY_SCOPE_AGENT

constexpr int D = 576, SK = 65536, WIN = 4096, DH = 128, FF = 4608, NMOD = 6 * D;
constexpr int NSPLIT = 11;
constexpr int NTHR = 576;
constexpr int SPIN_CAP = 1 << 16;
#define GATE_FFN 790
#define GATE_MODB 200
#define GATE_ATT 140
#define GATE_Q 500
#define GATE_FFN2 0
#define F_N1 0
#define F_D1 2
#define GATE_X2 0
#define POLL_SLEEP 0
#define Q_STAGGER 16
#define GATE_H1 260
#define DN_FIRST 0
#define MODA_HOLD 0
#define GATE_MODB2 120
#define CODE_PF 21504
#define GATE_DN 0
#define DN_SECOND 4
#define V_N1 0
#define V_D1 2
#define V_D2 24
#define ATT_N1 5
#define ATT_D1 2
#define ATT_D2 24

constexpr int G_EPOCH = 0;
constexpr int G_MOD = 64;
constexpr int G_Q = G_MOD + 4 * NMOD;
constexpr int G_O = G_Q + 32 * 2 * 128;
constexpr int G_WO = G_O + 16 * 8 * NSPLIT * 64;
constexpr int G_X2 = G_WO + 128 * 576;
constexpr int G_DN = G_X2 + 72 * 16;
constexpr int G_END = G_DN + 72 * 576;
constexpr size_t G_STAMP = 1 << 20;

constexpr int L_K = 0;
constexpr int L_OL = 98304;
constexpr int L_Q = L_OL + 9216;
constexpr int L_MX = L_Q + 512;
constexpr int L_LS = L_MX + 64;
constexpr int L_OB = L_LS + 64;
constexpr int L_PF = L_OB + 128;
constexpr int L_WO = L_PF + 256;
constexpr int L_MA = L_WO + 36864;
constexpr int L_CNT = L_MA + 1536;
constexpr int L_TAG = L_MA + 1536 + 16;
constexpr int L_END = L_TAG + 16;
constexpr int F_RED = 0;
constexpr int F_H = 16384;
constexpr int F_H2 = 17664;
constexpr int F_ACT = 20736;
constexpr int F_TR = 21248;
constexpr int F_ST = 27136;

struct Params {
  const u16 *x, *kc, *vc, *wq, *wo, *wg, *wu, *wd, *c, *wmod;
  u16* out;
  u64* ws;
};

#define STAMP(i) do {} while (0)
#define STAMP_INIT() do {} while (0)
#define STAMP_DUMP() do {} while (0)
#define L_EXTRA 0

DEV float bf2f(unsigned b) { return __uint_as_float(b << 16); }
DEV unsigned cvt2(float lo, float hi) {
  const f32x2 v = {lo, hi};
  return __builtin_bit_cast(unsigned, __builtin_convertvector(v, bf16x2));
}
DEV unsigned pkr(float f) { return cvt2(f, f); }
DEV float rbf(float f) { return __uint_as_float(cvt2(f, f) & 0xFFFF0000u); }
#define TAG_LO_BITS 4
constexpr unsigned TAGM = (1u << TAG_LO_BITS) - 1u;
struct Tag { unsigned hi, lo, rest; };
DEV Tag make_tag(u64 st) {
  const u64 nx = st * 6364136223846793005ull + 1442695040888963407ull;
  Tag t;
  t.hi = __builtin_amdgcn_readfirstlane((unsigned)(nx >> 32));
  t.rest = __builtin_amdgcn_readfirstlane((unsigned)nx);
  t.lo = t.rest >> (32 - TAG_LO_BITS);
  return t;
}
DEV u64 enc(float v, unsigned hi, unsigned lo) {
  const unsigned u = (__float_as_uint(v) + (TAGM + 1) / 2) & ~TAGM;
  return ((u64)hi << 32) | (u64)(u | lo);
}
DEV Tag tag_put(LAS unsigned char* L, u64 st, int lane) {
  const Tag t = make_tag(st);
  if (lane == 0) *(LAS v4u*)(L + L_TAG) = v4u{t.hi, t.lo, t.rest, 0u};
  return t;
}
DEV Tag tag_get(const LAS unsigned char* L) {
  const v4u v = *(const LAS v4u*)(L + L_TAG);
  Tag t; t.hi = __builtin_amdgcn_readfirstlane(v[0]); t.lo = __builtin_amdgcn_readfirstlane(v[1]); t.rest = __builtin_amdgcn_readfirstlane(v[2]);
  return t;
}
DEV unsigned or_xor(unsigned a, unsigned b, unsigned c) { return __builtin_amdgcn_bitop3_b32(a, b, c, 0xF6); }
struct Chk {
  unsigned a = 0, b = 0;
  DEV void add(u64 g, const Tag& t) { a = or_xor(a, (unsigned)(g >> 32), t.hi); b = or_xor(b, (unsigned)g, t.lo); }
  DEV bool all_ok() const { return __builtin_amdgcn_ballot_w64((a | (b & TAGM)) != 0u) == 0ull; }
};
DEV float gval(u64 g) { return __uint_as_float((unsigned)g & ~TAGM); }
DEV void pub(u64* ws, int idx, float v, const Tag& t) { __hip_atomic_store((gu64*)ws + idx, enc(v, t.hi, t.lo), RLX_AGENT); }
DEV void pubp(gu64* dst, float v, const Tag& t) { __hip_atomic_store(dst, enc(v, t.hi, t.lo), RLX_AGENT); }
DEV u64 peek(u64* ws, int idx) { return __hip_atomic_load((gu64*)ws + idx, RLX_AGENT); }
template <int N, class F> DEV void gather(u64* ws, const Tag& t, u64 (&g)[N], F idx) {
  for (int spin = 0; spin < SPIN_CAP; ++spin) {
#pragma unroll
    for (int k = 0; k < N; ++k) g[k] = peek(ws, idx(k));
    Chk c;
#pragma unroll
    for (int k = 0; k < N; ++k) c.add(g[k], t);
    if (c.all_ok()) break;
    if (POLL_SLEEP) __builtin_amdgcn_s_sleep(POLL_SLEEP);
  }
}

struct G2 { v4u v; DEV float lo() const { return __uint_as_float(v[0] & ~TAGM); } DEV float hi() const { return __uint_as_float(v[2] & ~TAGM); } };
template <int N, class F> DEV void gather2(rsrc_t rw, const Tag& t, G2 (&g)[N], F idx) {
  for (int spin = 0; spin < SPIN_CAP; ++spin) {
    asm volatile("" ::: "memory");
#pragma unroll
    for (int k = 0; k < N; ++k) g[k].v = __builtin_amdgcn_raw_buffer_load_b128(rw, idx(k) * 8, 0, 16);
    unsigned a = 0, b = 0;
#pragma unroll
    for (int k = 0; k < N; ++k) { a = or_xor(or_xor(a, g[k].v[1], t.hi), g[k].v[3], t.hi); b = or_xor(or_xor(b, g[k].v[0], t.lo), g[k].v[2], t.lo); }
    if (__builtin_amdgcn_ballot_w64((a | (b & TAGM)) != 0u) == 0ull) break;
    if (POLL_SLEEP) __builtin_amdgcn_s_sleep(POLL_SLEEP);
  }
}
template <int N, class F> DEV void pretouch(LAS unsigned char* L, u64* ws, F idx) {
  const unsigned dump = (unsigned)(uintptr_t)(L + L_PF);
#pragma unroll
  for (int k = 0; k < N; ++k)
    asm volatile("s_mov_b32 m0, %0\n\ts_nop 0\n\tglobal_load_lds_dword %1, off sc1" :: "s"(dump), "v"((gu64*)ws + idx(k)) : "m0", "memory");
}
DEV f32x4 mfma(v4u a, v4u b, f32x4 c) {
  return __builtin_amdgcn_mfma_f32_16x16x32_bf16(__builtin_bit_cast(bf16x8, a), __builtin_bit_cast(bf16x8, b), c, 0, 0, 0);
}
DEV rsrc_t mk_rsrc(const void* p, unsigned bytes) { return __builtin_amdgcn_make_buffer_rsrc((void*)p, 0, (int)bytes, 0x00020000); }
DEV v4u ldb(rsrc_t r, unsigned voff, unsigned soff) { return __builtin_amdgcn_raw_buffer_load_b128(r, (int)voff, (int)soff, 2); }
#define SMALL_AUX 0
DEV unsigned ldb16(rsrc_t r, unsigned voff) { return __builtin_amdgcn_raw_buffer_load_b16(r, (int)voff, 0, SMALL_AUX); }
#define E_AUX 16
DEV u64 load_state(u64* ws) {
  int zoff = G_EPOCH * 8;
  asm volatile("" : "+v"(zoff));
  typedef unsigned v2u_ __attribute__((ext_vector_type(2)));
  const v2u_ v = __builtin_amdgcn_raw_buffer_load_b64(mk_rsrc(ws, 64), zoff, 0, E_AUX);
  return ((u64)v[1] << 32) | v[0];
}
DEV void bar() {
  asm volatile("s_waitcnt lgkmcnt(0)" ::: "memory");
  __builtin_amdgcn_s_barrier();
  asm volatile("" ::: "memory");
}
#define PIN(x) asm volatile("" : "+v"(x))
#define PINS(x) asm volatile("" : "+s"(x))
DEV void lds_wait() { asm volatile("s_waitcnt lgkmcnt(0)" ::: "memory"); }
template <int CTRL> DEV float dpp(float x) {
  return __builtin_bit_cast(float, __builtin_amdgcn_mov_dpp(__builtin_bit_cast(int, x), CTRL, 0xf, 0xf, true));
}
constexpr int ROR8 = 0x128, ROR4 = 0x124, ROR2 = 0x122, ROR1 = 0x121, XOR1 = 0xB1, XOR2 = 0x4E;
DEV float fmx(float a, float b) { float r; asm("v_max_f32 %0, %1, %2\n\ts_nop 1" : "=v"(r) : "v"(a), "v"(b)); return r; }
DEV float fmx3(float a, float b, float c) { float r; asm("v_max3_f32 %0, %1, %2, %3" : "=v"(r) : "v"(a), "v"(b), "v"(c)); return r; }
DEV float fmx3n(float a, float b, float c) { float r; asm("v_max3_f32 %0, %1, %2, %3\n\ts_nop 1" : "=v"(r) : "v"(a), "v"(b), "v"(c)); return r; }
DEV float x16_sum(float x) {
  auto s = __builtin_amdgcn_permlane16_swap(__float_as_uint(x), __float_as_uint(x), false, false);
  x = __uint_as_float(s[0]) + __uint_as_float(s[1]);
  auto t = __builtin_amdgcn_permlane32_swap(__float_as_uint(x), __float_as_uint(x), false, false);
  return __uint_as_float(t[0]) + __uint_as_float(t[1]);
}
DEV float x16_max(float x) {
  auto s = __builtin_amdgcn_permlane16_swap(__float_as_uint(x), __float_as_uint(x), false, false);
  x = fmaxf(__uint_as_float(s[0]), __uint_as_float(s[1]));
  auto t = __builtin_amdgcn_permlane32_swap(__float_as_uint(x), __float_as_uint(x), false, false);
  return fmaxf(__uint_as_float(t[0]), __uint_as_float(t[1]));
}
DEV float row_sum(float x) { x += dpp<ROR8>(x); x += dpp<ROR4>(x); x += dpp<ROR2>(x); x += dpp<ROR1>(x); return x; }
DEV float row_max(float x) { x = fmaxf(x, dpp<ROR8>(x)); x = fmaxf(x, dpp<ROR4>(x)); x = fmaxf(x, dpp<ROR2>(x)); x = fmaxf(x, dpp<ROR1>(x)); return x; }
DEV float wave_sum(float x) { return x16_sum(row_sum(x)); }
constexpr float LOG2E = 1.4426950408889634f;
DEV float fadd(float a, float b) { float r; asm("v_add_f32 %0, %1, %2" : "=v"(r) : "v"(a), "v"(b)); return r; }
DEV float faddn(float a, float b) { float r; asm("v_add_f32 %0, %1, %2\n\ts_nop 1" : "=v"(r) : "v"(a), "v"(b)); return r; }
DEV float exp_f(float x) { return __builtin_amdgcn_exp2f(x * LOG2E); }
DEV float rcp_f(float x) { return __builtin_amdgcn_rcpf(x); }
DEV float pair_pick(float a, float b, int lane) {
  const float a2 = dpp<0x50>(a), b2 = dpp<0x50>(b);
  const float a3 = dpp<0xFA>(a), b3 = dpp<0xFA>(b);
  const float lo = (lane & 1) ? b2 : a2, hi = (lane & 1) ? b3 : a3;
  return (lane & 4) ? hi : lo;
}
DEV float rdlane(float x, int l) { return __uint_as_float(__builtin_amdgcn_readlane(__float_as_uint(x), l)); }
DEV void gate_wait(u64 t0, unsigned ticks) { while (__builtin_amdgcn_s_memrealtime() - t0 < (u64)ticks) __builtin_amdgcn_s_sleep(2); }

struct Mask { unsigned m[4]; };
DEV Mask make_mask(int lane, bool on) {
  Mask k;
  const unsigned h = on ? ((lane & 1) ? 0xFFFF0000u : 0x0000FFFFu) : 0u;
#pragma unroll
  for (int r = 0; r < 4; ++r) k.m[r] = (((lane & 7) >> 1) == r) ? h : 0u;
  return k;
}
DEV v4u afrag(unsigned pp, const Mask& k) { return v4u{pp & k.m[0], pp & k.m[1], pp & k.m[2], pp & k.m[3]}; }

DEV unsigned ln_apply_pk(float x, float mean_b, float rden, float c1, float shift) {
  const float ln = rbf(rbf(x - mean_b) * rden);
  return pkr(rbf(ln * c1) + shift);
}
DEV float ln_rden(float var) { return rcp_f(rbf(__builtin_amdgcn_sqrtf(rbf(rbf(var) + 1e-5f)))); }

DEV void red_store(LAS float* area, f32x4 d, int lane) {
  const int u = (lane >> 3) & 1;
  if ((lane >> 5) == u) *(LAS f32x4*)(area + u * 64 + 8 * (lane & 7) + 4 * ((lane >> 4) & 1)) = d;
}
DEV unsigned red_off(int lane, int dump) {
  const int u = (lane >> 3) & 1;
  return 4 * ((lane >> 5) == u ? u * 64 + 8 * (lane & 7) + 4 * ((lane >> 4) & 1) : dump);
}
DEV unsigned silu_pk(unsigned craw) { const float v = bf2f(craw); return pkr(v * rcp_f(1.f + exp_f(-v))); }

struct WoAddr { unsigned tro, tri, obo, wro; gu64* dst; };
DEV WoAddr wo_prep(u64* ws, int pidx, int wave, int lane) {
  WoAddr a;
  a.tro = L_OL + wave * 640 + red_off(lane, 128); a.tri = L_OL + wave * 640 + lane * 4; a.obo = L_OB + (lane >> 3) * 16; a.wro = L_WO + wave * 4096 + lane * 16;
  a.dst = (gu64*)ws + G_WO + pidx * 576 + wave * 64 + lane;
  PIN(a.tro); PIN(a.tri); PIN(a.obo); PIN(a.wro); PIN(a.dst);
  return a;
}
DEV void wo_phase(LAS unsigned char* L, const WoAddr& a, const Tag& tag, const Mask& mk) {
  bar();
  v4u ob = *(const LAS v4u*)(L + a.obo);
  const LAS v4u* wr = (const LAS v4u*)(L + a.wro);
  v4u w0 = wr[0], w1 = wr[64], w2 = wr[128], w3 = wr[192];
  PIN(ob); PIN(w0); PIN(w1); PIN(w2); PIN(w3);
  __builtin_amdgcn_sched_barrier(0);
  f32x4 d0 = {0.f, 0.f, 0.f, 0.f}, d1 = {0.f, 0.f, 0.f, 0.f};
  d0 = mfma(afrag(ob[0], mk), w0, d0); d1 = mfma(afrag(ob[1], mk), w1, d1);
  d0 = mfma(afrag(ob[2], mk), w2, d0); d1 = mfma(afrag(ob[3], mk), w3, d1);
  *(LAS f32x4*)(L + a.tro) = d0 + d1;
  lds_wait();
  pubp(a.dst, fadd(*(const LAS float*)(L + a.tri), *(const LAS float*)(L + a.tri + 256)), tag);
}

struct ModA { v4u w[18]; unsigned c[3]; };
DEV void moda_load(const Params& p, int mt, int lane, ModA& m) {
  const rsrc_t rm = mk_rsrc(p.wmod, D * NMOD * 2), rc = mk_rsrc(p.c, D * 2);
  const int r0 = (mt & 3) * 144;
#pragma unroll
  for (int k = 0; k < 3; ++k) m.c[k] = ldb16(rc, (r0 + (k < 2 || lane < 16 ? lane + 64 * k : 143)) * 2);
  const unsigned v = (r0 + (lane >> 3)) * (NMOD * 2) + (lane & 7) * 16;
#pragma unroll
  for (int t = 0; t < 18; ++t) m.w[t] = ldb(rm, v, (mt >> 2) * 128 + t * 8 * (NMOD * 2));
}
DEV void moda_finish(LAS unsigned char* L, u64* ws, int mt, int lane, ModA& m, const Mask& mk, LAS float* tr, const Tag& tag) {
  unsigned sp[18];
  {
    const unsigned s0 = silu_pk(m.c[0]), s1 = silu_pk(m.c[1]), s2 = silu_pk(m.c[2]);
#pragma unroll
    for (int t = 0; t < 18; ++t) sp[t] = __builtin_amdgcn_ds_bpermute(((8 * t + (lane >> 3)) & 63) * 4, t < 8 ? s0 : t < 16 ? s1 : s2);
  }
  gu64* dst = (gu64*)ws + G_MOD + (mt & 3) * NMOD + (mt >> 2) * 64 + lane;
  unsigned tro = red_off(lane, 128);
  PIN(dst); PIN(tro);
#pragma unroll
  for (int t = 0; t < 18; ++t) PIN(sp[t]);
  __builtin_amdgcn_sched_barrier(0);
  f32x4 d0 = {0.f, 0.f, 0.f, 0.f}, d1 = {0.f, 0.f, 0.f, 0.f};
#pragma unroll
  for (int t = 0; t < 18; t += 2) {
    d0 = mfma(afrag(sp[t], mk), m.w[t], d0);
    __builtin_amdgcn_sched_barrier(0);
    d1 = mfma(afrag(sp[t + 1], mk), m.w[t + 1], d1);
    __builtin_amdgcn_sched_barrier(0);
  }
  *(LAS f32x4*)((LAS unsigned char*)tr + tro) = d0 + d1;
  lds_wait();
  pubp(dst, fadd(tr[lane], tr[64 + lane]), tag);
}

DEV void role_att(const Params& p, LAS unsigned char* L, int x, int i, int wave, int lane, u64 t0) {
  const int g = 2 * x + (i >= NSPLIT ? 1 : 0), j = i >= NSPLIT ? i - NSPLIT : i;
  const bool odd = g & 1;
  const int nm = odd ? 4 : 5;
  const bool moda = j < nm;
  const int ntile = !moda ? 24 : (!odd && j >= 3) ? 23 : 22;
  const int tile0 = !moda ? (odd ? 88 : 112) + 24 * (j - nm) : (!odd && j == 4) ? 89 : 22 * j;
  const bool owner = j < 8;
  int redc = -1;
  if (j >= 8) redc = 9 * x + (g & 1) * 3 + (j - 8);
  else if (j == 0) redc = 9 * x + 6 + (g & 1);
  else if (j == 1 && (g & 1) == 0) redc = 9 * x + 8;
  u64* ws = p.ws;
  const Mask mk = make_mask(lane, true);
  const int kap = lane >> 4, cc = lane & 15;

  if (wave < 8) {
    const size_t hb = ((size_t)g * SK + (SK - WIN)) * DH;
    const rsrc_t rk = mk_rsrc(p.kc + hb, WIN * DH * 2), rv = mk_rsrc(p.vc + hb, WIN * DH * 2);
    const bool three = wave + 16 < ntile;
    const unsigned tso = (tile0 + wave) * 4096;
    const unsigned ldsk = (unsigned)(uintptr_t)(L + L_K) + wave * 3 * 4096;
    unsigned vk[4];
#pragma unroll
    for (int q = 0; q < 4; ++q) vk[q] = (4 * q + kap) * 256 + ((cc ^ (4 * q + kap)) << 4);
#define KDMA(s, q) asm volatile("s_mov_b32 m0, %0\n\ts_nop 0\n\tbuffer_load_dwordx4 %1, %2, %3 offen nt lds" \
                     :: "s"(ldsk + (s) * 4096 + (q) * 1024), "v"(vk[q]), "s"(rk), "s"(tso + (s) * 32768) : "m0", "memory")
#define THR(n) do { if ((n) < ATT_N1) { if ((n) >= ATT_D1 && moda) asm volatile("s_waitcnt vmcnt(%0)" :: "n"(ATT_D1 - 1) : "memory"); } \
                     else if ((n) >= ATT_D2) asm volatile("s_waitcnt vmcnt(%0)" :: "n"(ATT_D2 - 1) : "memory"); } while (0)
#define THR_DEPTH(n) ((n) < ATT_N1 ? ATT_D1 : ATT_D2)
#define THRV(t) asm volatile("s_waitcnt vmcnt(%0)" :: "n"(((t) < V_N1 ? V_D1 : V_D2) - 1) : "memory")
    if (moda) __builtin_amdgcn_s_barrier();
    __builtin_amdgcn_s_setprio(2);
    KDMA(0, 0);
    if (GATE_ATT) gate_wait(t0, GATE_ATT);
    if (MODA_HOLD && moda) __builtin_amdgcn_s_barrier();
    THR(1); KDMA(0, 1); THR(2); KDMA(0, 2); THR(3); KDMA(0, 3);
    THR(4); KDMA(1, 0); THR(5); KDMA(1, 1); THR(6); KDMA(1, 2); THR(7); KDMA(1, 3);
    if (three) { THR(8); KDMA(2, 0); THR(9); KDMA(2, 1); THR(10); KDMA(2, 2); THR(11); KDMA(2, 3); }
#undef KDMA
    __builtin_amdgcn_s_setprio(0);
    v4u vr[12];
    const unsigned vv = kap * 1024 + cc * 16;
#pragma unroll
    for (int t = 0; t < 8; ++t) { THRV(t); vr[t] = ldb(rv, vv + (t & 3) * 256, tso + (t >> 2) * 32768); }
    if (three) {
#pragma unroll
      for (int t = 8; t < 12; ++t) { THRV(t); vr[t] = ldb(rv, vv + (t & 3) * 256, tso + 65536); }
    } else {
#pragma unroll
      for (int t = 8; t < 12; ++t) vr[t] = v4u{0u, 0u, 0u, 0u};
    }
#undef THR
#undef THRV
#undef THR_DEPTH
    __builtin_amdgcn_sched_barrier(0);
    STAMP(1);
    if (three) asm volatile("s_waitcnt vmcnt(12)" ::: "memory");
    else asm volatile("s_waitcnt vmcnt(8)" ::: "memory");
    v4u kf[12];
#pragma unroll
    for (int s = 0; s < 3; ++s)
#pragma unroll
      for (int ks = 0; ks < 4; ++ks)
        kf[s * 4 + ks] = *(const LAS v4u*)(L + L_K + (wave * 3 + (s < 2 || three ? s : 0)) * 4096 + cc * 256 + (((4 * ks + kap) ^ cc) << 4));
#pragma unroll
    for (int t = 0; t < 12; ++t) PIN(kf[t]);
    const int head = (lane >> 3) & 1;
    const int me = (wave & 3) * 64 + lane;
    unsigned qsrc = L_Q + head * 256 + kap * 16, mxw = L_MX + (head * 8 + wave) * 4, mxr = L_MX + head * 32, olw = L_OL + wave * 1024 + lane * 16;
    unsigned msrc = L_OL + ((16 * (((me >> 7) * 8 + (me & 7)) >> 2) + ((me & 127) >> 3)) * 4 + (me & 3)) * 4;
    gu64* mdst = (gu64*)ws + G_O + ((g * 8 + (me >> 5)) * NSPLIT + j) * 64 + (me & 31);
    bool lead = (lane & 0x37) == 0;
    PIN(qsrc); PIN(mxw); PIN(mxr); PIN(olw); PIN(msrc); PIN(mdst);
    STAMP(3);
    bar();
    STAMP(2);
    v4u qf[4];
#pragma unroll
    for (int ks = 0; ks < 4; ++ks) qf[ks] = *(const LAS v4u*)(L + qsrc + ks * 64);
#pragma unroll
    for (int ks = 0; ks < 4; ++ks) PIN(qf[ks]);
    __builtin_amdgcn_sched_barrier(0);
    f32x4 acc[3] = {{0.f, 0.f, 0.f, 0.f}, {0.f, 0.f, 0.f, 0.f}, {0.f, 0.f, 0.f, 0.f}};
#pragma unroll
    for (int ks = 0; ks < 4; ++ks)
#pragma unroll
      for (int s = 0; s < 3; ++s) acc[s] = mfma(kf[s * 4 + ks], qf[ks], acc[s]);
    __builtin_amdgcn_sched_barrier(0);
    float sc[12];
#pragma unroll
    for (int t = 0; t < 12; t += 2) {
      const unsigned u = cvt2(acc[t >> 2][t & 3], acc[t >> 2][(t & 3) + 1]);
      const unsigned w = cvt2(__uint_as_float(u << 16) * 0.08838834764831845f, __uint_as_float(u & 0xFFFF0000u) * 0.08838834764831845f);
      sc[t] = __uint_as_float(w << 16);
      sc[t + 1] = __uint_as_float(w & 0xFFFF0000u);
    }
    if (!three) {
#pragma unroll
      for (int t = 8; t < 12; ++t) sc[t] = -INFINITY;
    }
    float mx = fmx3n(fmx3(fmx3(sc[0], sc[1], sc[2]), fmx3(sc[3], sc[4], sc[5]), fmx3(sc[6], sc[7], sc[8])), fmx3(sc[9], sc[10], sc[11]), sc[0]);
    mx = x16_max(mx);
    if (lead) *(LAS float*)(L + mxw) = mx;
    lds_wait();
    if (lane == 0) __hip_atomic_fetch_add((LAS unsigned*)(L + L_CNT), 1u, __ATOMIC_RELAXED, __HIP_MEMORY_SCOPE_WORKGROUP);
    while (*(volatile LAS unsigned*)(L + L_CNT) < 8u) {}
    asm volatile("" ::: "memory");
    float m;
    {
      const f32x4 a = *(const LAS f32x4*)(L + mxr), b = *(const LAS f32x4*)(L + mxr + 16);
      m = fmx3(fmx3(a[0], a[1], a[2]), fmx3(a[3], b[0], b[1]), fmx(b[2], b[3]));
    }
    v4u af[12];
    float ls0 = 0.f, ls1 = 0.f;
    const float mneg = -m * LOG2E;
#pragma unroll
    for (int t = 0; t < 12; t += 2) {
      const float e0 = __builtin_amdgcn_exp2f(fmaf(sc[t], LOG2E, mneg)), e1 = __builtin_amdgcn_exp2f(fmaf(sc[t + 1], LOG2E, mneg));
      ls0 += e0; ls1 += e1;
      af[t] = afrag(pkr(e0), mk); af[t + 1] = afrag(pkr(e1), mk);
    }
    const float ls = x16_sum(ls0 + ls1);
    if (lead) *(LAS float*)(L + mxw + 64) = ls;
    const Tag tag = tag_get(L);
    __builtin_amdgcn_sched_barrier(0);
    STAMP(4);
    f32x4 o0 = {0.f, 0.f, 0.f, 0.f}, o1 = {0.f, 0.f, 0.f, 0.f};
#pragma unroll
    for (int t = 0; t < 12; t += 2) { o0 = mfma(af[t], vr[t], o0); o1 = mfma(af[t + 1], vr[t + 1], o1); }
    *(LAS f32x4*)(L + olw) = o0 + o1;
    STAMP(5);
    bar();
    if (wave < 4) {
      const LAS float* src = (const LAS float*)(L + msrc);
      float s[8];
#pragma unroll
      for (int w = 0; w < 8; ++w) s[w] = src[w * 256];
      pubp(mdst, fadd(fadd(fadd(s[0], s[1]), fadd(s[2], s[3])), fadd(fadd(s[4], s[5]), fadd(s[6], s[7]))), tag);
    } else if (wave == 4 && lane < 16) {
      const int u = lane >> 1, hh = u >> 2;
      const f32x4 a = *(const LAS f32x4*)(L + ((lane & 1) ? L_LS : L_MX) + hh * 32), b = *(const LAS f32x4*)(L + ((lane & 1) ? L_LS : L_MX) + hh * 32 + 16);
      const float vs = ((a[0] + a[1]) + (a[2] + a[3])) + ((b[0] + b[1]) + (b[2] + b[3]));
      const float vm = fmaxf(fmaxf(fmaxf(a[0], a[1]), fmaxf(a[2], a[3])), fmaxf(fmaxf(b[0], b[1]), fmaxf(b[2], b[3])));
      pub(ws, G_O + ((g * 8 + u) * NSPLIT + j) * 64 + 32 + (lane & 1), (lane & 1) ? vs : vm, tag);
    }
    STAMP(6);
    if (owner) {
      wo_phase(L, wo_prep(ws, g * 8 + j, wave, lane), tag, mk);
      STAMP(8);
    }
  } else {
    __builtin_amdgcn_s_setprio(3);
    Tag tag;
    {
      if (moda) {
        u64 st = load_state(ws);
        ModA ma;
        moda_load(p, 9 * x + (odd ? 5 : 0) + j, lane, ma);
        __builtin_amdgcn_sched_barrier(0);
        __builtin_amdgcn_s_barrier();
        asm volatile("" : "+v"(st));
        tag = tag_put(L, st, lane);
        moda_finish(L, ws, 9 * x + (odd ? 5 : 0) + j, lane, ma, mk, (LAS float*)(L + L_MA), tag);
        STAMP(9);
        if (MODA_HOLD) __builtin_amdgcn_s_barrier();
      } else {
        tag = tag_put(L, load_state(ws), lane);
      }
    }
    float xr = 0.f;
    if (redc >= 0) xr = bf2f(p.x[redc * 8 + (lane & 7)]);
    if (GATE_Q) gate_wait(t0, GATE_Q);
    const rsrc_t rw = mk_rsrc(ws, G_END * 8);
    {
      G2 gq[4];
      gather2<4>(rw, tag, gq, [&](int k) { return G_Q + ((2 * g + (k >> 1)) * 2 + (k & 1)) * 128 + 2 * lane; });
      ((LAS unsigned*)(L + L_Q))[lane] = cvt2(fadd(gq[0].lo(), gq[1].lo()), fadd(gq[0].hi(), gq[1].hi()));
      ((LAS unsigned*)(L + L_Q))[64 + lane] = cvt2(fadd(gq[2].lo(), gq[3].lo()), fadd(gq[2].hi(), gq[3].hi()));
    }
    STAMP(1);
    if (owner) {
      const int ob = G_O + (g * 8 + j) * NSPLIT * 64;
      pretouch<1>(L, ws, [&](int) { const int q_ = lane < 33 ? lane : 32; return ob + (q_ / 3) * 64 + (q_ % 3) * 16; });
    }
    if (redc >= 0) {
      pretouch<2>(L, ws, [&](int s) { return G_WO + (s * 64 + lane) * 576 + redc * 8; });
      pretouch<2>(L, ws, [&](int s) { const int f_ = s * 64 + lane; return G_DN + (f_ < 72 ? f_ : 71) * 576 + redc * 8; });
    }
    if (lane == 0) *(volatile LAS unsigned*)(L + L_CNT) = 0u;
    bar();
    if (owner) {
      const rsrc_t rwo = mk_rsrc(p.wo, 4096 * D * 2);
      const unsigned wo_v = (lane >> 3) * (D * 2) + (lane & 7) * 16;
      const unsigned wo_s = (g * 256 + j * 32) * (D * 2), ldsw = (unsigned)(uintptr_t)(L + L_WO);
#pragma unroll
      for (int w = 0; w < 9; ++w)
#pragma unroll
        for (int t = 0; t < 4; ++t)
          asm volatile("s_mov_b32 m0, %0\n\ts_nop 0\n\tbuffer_load_dwordx4 %1, %2, %3 offen nt lds"
                       :: "s"(ldsw + (w * 4 + t) * 1024), "v"(wo_v), "s"(rwo), "s"(wo_s + w * 128 + t * 8 * (D * 2)) : "m0", "memory");
    }
    bar();
    STAMP(2);
    if (owner) {
      const int ob = G_O + (g * 8 + j) * NSPLIT * 64;
      const int k2 = kap < 3 ? 8 + kap : 10;
      unsigned obw = L_OB + (((2 * cc) & 7) * 4 + (cc >> 2)) * 4;
      float live = kap < 3 ? 1.f : 0.f;
      PIN(obw); PIN(live);
      const WoAddr wa = wo_prep(ws, g * 8 + j, 8, lane);
      G2 go[6];
      gather2<6>(rw, tag, go, [&](int k) { const int sp = k % 3 == 0 ? kap : k % 3 == 1 ? 4 + kap : k2; return ob + sp * 64 + (k < 3 ? 2 * cc : 32); });
      STAMP(3);
      const float M = x16_max(fmaxf(fmaxf(go[3].lo(), go[4].lo()), go[5].lo()));
      const float Mn = -M * LOG2E;
      const float w0 = __builtin_amdgcn_exp2f(fmaf(go[3].lo(), LOG2E, Mn)), w1 = __builtin_amdgcn_exp2f(fmaf(go[4].lo(), LOG2E, Mn));
      const float w2 = __builtin_amdgcn_exp2f(fmaf(go[5].lo(), LOG2E, Mn)) * live;
      const float Ls = x16_sum(fmaf(w2, go[5].hi(), fmaf(w1, go[4].hi(), w0 * go[3].hi())));
      const float o0 = x16_sum(fmaf(w2, go[2].lo(), fmaf(w1, go[1].lo(), w0 * go[0].lo())));
      const float o1 = x16_sum(fmaf(w2, go[2].hi(), fmaf(w1, go[1].hi(), w0 * go[0].hi())));
      const float rl = rcp_f(Ls);
      *(LAS unsigned*)(L + obw) = pkr(o0 * rl);
      *(LAS unsigned*)(L + obw + 16) = pkr(o1 * rl);
      asm volatile("s_waitcnt vmcnt(0)" ::: "memory");
      wo_phase(L, wa, tag, mk);
      STAMP(4);
    }
    if (redc >= 0) {
      u64 gg[8];
      gather<8>(ws, tag, gg, [&](int k) { return G_MOD + (k & 3) * NMOD + (k < 4 ? 2 : 5) * D + redc * 8 + (lane & 7); });
      float ga = rbf(fadd(fadd(gval(gg[0]), gval(gg[1])), fadd(gval(gg[2]), gval(gg[3]))));
      float gb = rbf(fadd(fadd(gval(gg[4]), gval(gg[5])), fadd(gval(gg[6]), gval(gg[7]))));
      gu64* xdst = (gu64*)ws + G_X2 + redc * 16 + (lane & 7);
      GAS u16* odst = (GAS u16*)p.out + redc * 8 + (lane & 7);
      PIN(ga); PIN(gb); PIN(xr); PIN(xdst); PIN(odst);
      float x2;
      {
        G2 gw[8];
        gather2<8>(rw, tag, gw, [&](int s) { return G_WO + (s * 16 + (lane >> 2)) * 576 + redc * 8 + 2 * (lane & 3); });
        STAMP(5);
        float t0_ = faddn(fadd(fadd(gw[0].lo(), gw[1].lo()), fadd(gw[2].lo(), gw[3].lo())), fadd(fadd(gw[4].lo(), gw[5].lo()), fadd(gw[6].lo(), gw[7].lo())));
        float t1_ = faddn(fadd(fadd(gw[0].hi(), gw[1].hi()), fadd(gw[2].hi(), gw[3].hi())), fadd(fadd(gw[4].hi(), gw[5].hi()), fadd(gw[6].hi(), gw[7].hi())));
        t0_ += dpp<ROR8>(t0_); t1_ += dpp<ROR8>(t1_); t0_ += dpp<ROR4>(t0_); t1_ += dpp<ROR4>(t1_);
        t0_ = x16_sum(t0_); t1_ = x16_sum(t1_);
        const float ts = pair_pick(t0_, t1_, lane);
        x2 = rbf(xr + rbf(ga * rbf(ts)));
        if (lane < 8) pubp(xdst, x2, tag);
      }
      STAMP(6);
      {
        G2 gd[5];
        float w4 = lane < 32 ? 1.f : 0.f;
        PIN(w4);
        gather2<5>(rw, tag, gd, [&](int s) { const int f_ = s * 16 + (lane >> 2); return G_DN + (f_ < 72 ? f_ : 71) * 576 + redc * 8 + 2 * (lane & 3); });
        STAMP(7);
        float t0_ = fmaf(w4, gd[4].lo(), fadd(fadd(gd[0].lo(), gd[1].lo()), fadd(gd[2].lo(), gd[3].lo())));
        float t1_ = fmaf(w4, gd[4].hi(), fadd(fadd(gd[0].hi(), gd[1].hi()), fadd(gd[2].hi(), gd[3].hi())));
        t0_ += dpp<ROR8>(t0_); t1_ += dpp<ROR8>(t1_); t0_ += dpp<ROR4>(t0_); t1_ += dpp<ROR4>(t1_);
        t0_ = x16_sum(t0_); t1_ = x16_sum(t1_);
        const float ts = pair_pick(t0_, t1_, lane);
        const unsigned o = pkr(x2 + rbf(gb * rbf(ts)));
        if (lane < 8) *odst = (u16)o;
        if (redc == 0 && lane == 0) __hip_atomic_store((gu64*)ws + G_EPOCH, ((u64)tag.hi << 32) | (u64)tag.rest, RLX_AGENT);
      }
      STAMP(8);
    }
  }
}

DEV void role_ffn(const Params& p, LAS unsigned char* L, int x, int i, int wave, int lane, u64 t0) {
  const int f = 9 * x + i;
  u64* ws = p.ws;
  const Mask mk = make_mask(lane, true);
  LAS float* red = (LAS float*)(L + F_RED);
  const rsrc_t rm = mk_rsrc(p.wmod, D * NMOD * 2), rc = mk_rsrc(p.c, D * 2), rx = mk_rsrc(p.x, D * 2);
  const int hq = 4 * x + (i >> 1), qr = i & 1;
  const rsrc_t rw = mk_rsrc(ws, G_END * 8);
  Tag tag;
  if (i < 8) {
    v4u wa[12];
    if (wave >= 3) {
      const rsrc_t rq = mk_rsrc(p.wq, D * 4096 * 2);
      const unsigned v = (lane >> 4) * 8192 + (lane & 15) * 16, s = (qr * 288 + (wave - 3) * 48) * 8192 + hq * 256;
#pragma unroll
      for (int t = 0; t < 12; ++t) wa[t] = ldb(rq, v, s + t * 4 * 8192);
      __builtin_amdgcn_sched_barrier(0);
      STAMP(1);
    } else {
      __builtin_amdgcn_s_setprio(3);
      const u64 st = load_state(ws);
      float xv[9];
#pragma unroll
      for (int k = 0; k < 9; ++k) xv[k] = bf2f(ldb16(rx, (lane + 64 * k) * 2));
      const int p0 = wave * 64 + lane, pr = p0 < 144 ? p0 : 143;
      const unsigned xo = __builtin_amdgcn_raw_buffer_load_b32(rx, (qr * 288 + 2 * pr) * 2, 0, 0);
      float s = 0.f;
#pragma unroll
      for (int k = 0; k < 9; ++k) s += xv[k];
      const float mf = wave_sum(s) * (1.f / D);
      float v = 0.f;
#pragma unroll
      for (int k = 0; k < 9; ++k) { const float d = xv[k] - mf; v = fmaf(d, d, v); }
      const float rden = ln_rden(wave_sum(v) * (1.f / D)), mean = rbf(mf);
      tag = wave == 0 ? tag_put(L, st, lane) : make_tag(st);
      const int hw = pr / 24, hr = 2 * pr - hw * 48;
      unsigned hdst = F_H + ((hw * 4 + (hr & 3)) * 12 + (hr >> 2)) * 4;
      float ln0 = rbf(rbf(__uint_as_float(xo << 16) - mean) * rden), ln1 = rbf(rbf(__uint_as_float(xo & 0xFFFF0000u) - mean) * rden);
      PIN(hdst); PIN(ln0); PIN(ln1);
      STAMP(2);
      if (GATE_H1) gate_wait(t0, GATE_H1);
      G2 ga[8];
      gather2<8>(rw, tag, ga, [&](int k) { return G_MOD + (k & 3) * NMOD + (k >> 2) * D + qr * 288 + 2 * pr; });
      STAMP(3);
      const float sa0 = rbf(fadd(fadd(ga[0].lo(), ga[1].lo()), fadd(ga[2].lo(), ga[3].lo()))), sa1 = rbf(fadd(fadd(ga[0].hi(), ga[1].hi()), fadd(ga[2].hi(), ga[3].hi())));
      const float ca0 = rbf(fadd(fadd(ga[4].lo(), ga[5].lo()), fadd(ga[6].lo(), ga[7].lo()))), ca1 = rbf(fadd(fadd(ga[4].hi(), ga[5].hi()), fadd(ga[6].hi(), ga[7].hi())));
      *(LAS unsigned*)(L + hdst) = pkr(rbf(ln0 * rbf(1.f + ca0)) + sa0);
      *(LAS unsigned*)(L + hdst + 48) = pkr(rbf(ln1 * rbf(1.f + ca1)) + sa1);
      __builtin_amdgcn_s_setprio(0);
    }
    Mask mq = make_mask(lane, (lane & 8) == 0);
    const int qe = (wave & 1) * 64 + lane;
    unsigned hsrc = F_H + ((wave - 3) * 4 + (lane >> 4)) * 48, qst = F_RED + 4608 + (wave - 3) * 1024 + lane * 16;
    unsigned qsrc = F_RED + 4608 + ((16 * ((qe & 7) >> 2) + (qe >> 3)) * 4 + (qe & 3)) * 4;
    gu64* qdst = (gu64*)ws + G_Q + (hq * 2 + qr) * 128 + qe;
    PIN(mq.m[0]); PIN(mq.m[1]); PIN(mq.m[2]); PIN(mq.m[3]); PIN(hsrc); PIN(qst); PIN(qsrc); PIN(qdst);
    bar();
    if (wave >= 3) {
      const LAS v4u* hp = (const LAS v4u*)(L + hsrc);
      const v4u h0 = hp[0], h1 = hp[1], h2 = hp[2];
      f32x4 d0 = {0.f, 0.f, 0.f, 0.f}, d1 = {0.f, 0.f, 0.f, 0.f};
#pragma unroll
      for (int t = 0; t < 4; ++t) { d0 = mfma(afrag(h0[t], mq), wa[t], d0); d1 = mfma(afrag(h1[t], mq), wa[4 + t], d1); }
#pragma unroll
      for (int t = 0; t < 4; t += 2) { d0 = mfma(afrag(h2[t], mq), wa[8 + t], d0); d1 = mfma(afrag(h2[t + 1], mq), wa[9 + t], d1); }
      *(LAS f32x4*)(L + qst) = d0 + d1;
    }
    bar();
    if (wave < 2) {
      const LAS float* src = (const LAS float*)(L + qsrc);
      pubp(qdst, fadd(fadd(fadd(src[0], src[256]), fadd(src[512], src[768])), fadd(src[1024], src[1280])), tag);
    }
  } else {
    if (wave == 0) tag_put(L, load_state(ws), lane);
    if (GATE_MODB2) gate_wait(t0, GATE_MODB2);
    ModA ma;
    if (wave < 6) moda_load(p, 72 + 6 * x + wave, lane, ma);
    __builtin_amdgcn_sched_barrier(0);
    bar();
    if (wave < 6) {
      tag = tag_get(L);
      moda_finish(L, ws, 72 + 6 * x + wave, lane, ma, mk, (LAS float*)(L + F_RED + wave * 1024), tag);
    }
  }
  tag = tag_get(L);
  STAMP(4);
  pretouch<1>(L, ws, [&](int) { const int e = wave * 64 + lane; return G_X2 + (e >> 3) * 16 + (e & 7); });
  const bool slow_tile = ((((unsigned)(uintptr_t)p.wg >> 7) + f) & 7) == 3;
  v4u wg[9], wu[9], wd[8];
#define THRF(n) do { if ((n) < F_N1 && !slow_tile && __builtin_amdgcn_s_memrealtime() - t0 < (u64)GATE_FFN2) asm volatile("s_waitcnt vmcnt(%0)" :: "n"(F_D1 - 1) : "memory"); } while (0)
  if (GATE_DN) {
    gate_wait(t0, GATE_DN);
    const rsrc_t rd = mk_rsrc(p.wd, FF * D * 2);
    const unsigned v = (lane >> 3) * (D * 2) + (lane & 7) * 16, s = f * 64 * (D * 2) + wave * 128;
#pragma unroll
    for (int t = 0; t < DN_FIRST; ++t) wd[t] = ldb(rd, v, s + t * 8 * (D * 2));
    __builtin_amdgcn_sched_barrier(0);
  }
  if (!slow_tile) gate_wait(t0, GATE_FFN);
  if (wave < 8) {
    const rsrc_t rg = mk_rsrc(p.wg, D * FF * 2), ru = mk_rsrc(p.wu, D * FF * 2);
    const unsigned v = (lane >> 3) * (FF * 2) + (lane & 7) * 16, s = wave * 72 * (FF * 2) + f * 128;
#pragma unroll
    for (int t = 0; t < 9; ++t) { THRF(2 * t); wg[t] = ldb(rg, v, s + t * 8 * (FF * 2)); THRF(2 * t + 1); wu[t] = ldb(ru, v, s + t * 8 * (FF * 2)); }
  }
  if (!GATE_DN) {
    const rsrc_t rd = mk_rsrc(p.wd, FF * D * 2);
    const unsigned v = (lane >> 3) * (D * 2) + (lane & 7) * 16, s = f * 64 * (D * 2) + wave * 128;
#pragma unroll
    for (int t = 0; t < DN_FIRST; ++t) { THRF(18 + t); wd[t] = ldb(rd, v, s + t * 8 * (D * 2)); }
  }
  __builtin_amdgcn_sched_barrier(0);
  STAMP(5);
  unsigned h2w, h2r, stw, str_, gur, guw0, actw, actr, trw, trr;
  gu64* ddst;
  {
    const int e = wave * 64 + lane, hw = e / 72, rem = e - hw * 72, c = (wave & 3) * 16 + (lane >> 2);
    h2w = F_H2 + ((hw * 8 + (rem & 7)) * 12 + (rem >> 3)) * 4;
    h2r = F_H2 + ((wave & 7) * 8 + (lane >> 3)) * 48;
    stw = F_ST + wave * 8;
    str_ = F_ST + ((lane & 15) < 9 ? (lane & 15) : 9) * 8;
    guw0 = F_RED + (wave & 7) * 1040 + red_off(lane, 256);
    gur = F_RED + ((lane & 3) * 520 + c) * 4;
    actw = F_ACT + ((lane & 3) == 0 ? (c & 7) * 8 + (c >> 3) : 64) * 4;
    actr = F_ACT + (lane >> 3) * 32;
    trw = F_TR + wave * 640 + red_off(lane, 128);
    trr = F_TR + wave * 640 + lane * 4;
    ddst = (gu64*)ws + G_DN + f * 576 + wave * 64 + lane;
    if (wave == 0 && lane == 0) *(LAS f32x2*)(L + F_ST + 72) = f32x2{0.f, 0.f};
  }
  PIN(h2w); PIN(h2r); PIN(stw); PIN(str_); PIN(guw0); PIN(gur); PIN(actw); PIN(actr); PIN(trw); PIN(trr); PIN(ddst);
  if (GATE_X2) gate_wait(t0, GATE_X2);
  {
    const int e = wave * 64 + lane;
    u64 gs[9];
    bool have_x2 = false;
    for (int spin = 0; spin < SPIN_CAP; ++spin) {
#pragma unroll
      for (int k = 0; k < 8; ++k) gs[k] = peek(ws, G_MOD + (k & 3) * NMOD + (k < 4 ? 3 : 4) * D + e);
      gs[8] = peek(ws, G_X2 + (e >> 3) * 16 + (e & 7));
      Chk c, cx;
#pragma unroll
      for (int k = 0; k < 8; ++k) c.add(gs[k], tag);
      cx.add(gs[8], tag);
      have_x2 = cx.all_ok();
      if (c.all_ok()) break;
    }
    float sb = rbf(fadd(fadd(gval(gs[0]), gval(gs[1])), fadd(gval(gs[2]), gval(gs[3]))));
    float c1b = rbf(1.f + rbf(fadd(fadd(gval(gs[4]), gval(gs[5])), fadd(gval(gs[6]), gval(gs[7])))));
    float on = (lane & 15) < 9 ? 64.f : 0.f;
    PIN(sb); PIN(c1b); PIN(on);
    STAMP(6);
    u64 gx[1] = {gs[8]};
    if (!have_x2) gather<1>(ws, tag, gx, [&](int) { return G_X2 + (e >> 3) * 16 + (e & 7); });
    STAMP(7);
    const float xv = gval(gx[0]);
    const float sh = __uint_as_float(__builtin_amdgcn_readfirstlane(__float_as_uint(xv)));
    const float d = xv - sh;
    float s1 = d, s2 = d * d;
    s1 += dpp<ROR8>(s1); s2 += dpp<ROR8>(s2); s1 += dpp<ROR4>(s1); s2 += dpp<ROR4>(s2);
    s1 += dpp<ROR2>(s1); s2 += dpp<ROR2>(s2); s1 += dpp<ROR1>(s1); s2 += dpp<ROR1>(s2);
    s1 = x16_sum(s1); s2 = x16_sum(s2);
    const float dm = s1 * (1.f / 64.f);
    if (lane == 0) *(LAS f32x2*)(L + stw) = f32x2{sh + dm, fmaf(-dm, s1, s2)};
    bar();
    const f32x2 st = *(const LAS f32x2*)(L + str_);
    __builtin_amdgcn_sched_barrier(0);
    {
      const rsrc_t rd = mk_rsrc(p.wd, FF * D * 2);
      const unsigned v = (lane >> 3) * (D * 2) + (lane & 7) * 16, s = f * 64 * (D * 2) + wave * 128;
#pragma unroll
      for (int t = DN_FIRST; t < DN_SECOND; ++t) wd[t] = ldb(rd, v, s + t * 8 * (D * 2));
      __builtin_amdgcn_sched_barrier(0);
    }
    const float mf = row_sum(st[0]) * (1.f / 9.f);
    const float dv = st[0] - mf;
    const float m2 = row_sum(fmaf(on * dv, dv, st[1]));
    *(LAS unsigned*)(L + h2w) = ln_apply_pk(xv, rbf(mf), ln_rden(m2 * (1.f / D)), c1b, sb);
  }
  bar();
  STAMP(8);
  v4u h0, h1;
  unsigned h8;
  {
    const LAS v4u* hp = (const LAS v4u*)(L + h2r);
    h0 = hp[0]; h1 = hp[1]; h8 = ((const LAS unsigned*)hp)[8];
  }
  __builtin_amdgcn_sched_barrier(0);
  if (DN_SECOND < 8) {
    const rsrc_t rd = mk_rsrc(p.wd, FF * D * 2);
    const unsigned v = (lane >> 3) * (D * 2) + (lane & 7) * 16, s = f * 64 * (D * 2) + wave * 128;
#pragma unroll
    for (int t = DN_SECOND; t < 8; ++t) wd[t] = ldb(rd, v, s + t * 8 * (D * 2));
    __builtin_amdgcn_sched_barrier(0);
  }
  if (wave < 8) {
    PIN(h0); PIN(h1); PIN(h8);
    f32x4 dg = {0.f, 0.f, 0.f, 0.f}, du = {0.f, 0.f, 0.f, 0.f};
#pragma unroll
    for (int t = 0; t < 9; ++t) {
      const v4u a = afrag(t < 4 ? h0[t & 3] : t < 8 ? h1[t & 3] : h8, mk);
      dg = mfma(a, wg[t], dg);
      du = mfma(a, wu[t], du);
    }
    *(LAS f32x4*)(L + guw0) = dg;
    *(LAS f32x4*)(L + guw0 + ((lane >> 5) == ((lane >> 3) & 1) ? 512 : 0)) = du;
  }
  bar();
  if (wave < 4) {
    const LAS float* r0 = (const LAS float*)(L + gur);
    float g = faddn(fadd(r0[0], r0[64]), fadd(r0[260], r0[324])), u = faddn(fadd(r0[128], r0[192]), fadd(r0[388], r0[452]));
    g += dpp<XOR1>(g); u += dpp<XOR1>(u);
    g += dpp<XOR2>(g); u += dpp<XOR2>(u);
    g = rbf(g); u = rbf(u);
    const float y = g * fmaf(g * g, 0.044715f * 0.7978845608028654f * 2.f * LOG2E, 0.7978845608028654f * 2.f * LOG2E);
    const float th = fmaf(-2.f, rcp_f(__builtin_amdgcn_exp2f(y) + 1.f), 1.f);
    const float act = rbf(0.5f * g * (1.f + th)) * u;
    *(LAS unsigned*)(L + actw) = pkr(act);
  }
  bar();
  STAMP(9);
  {
    const LAS v4u* ap = (const LAS v4u*)(L + actr);
    const v4u a0 = ap[0], a1 = ap[1];
    f32x4 d0 = {0.f, 0.f, 0.f, 0.f}, d1 = {0.f, 0.f, 0.f, 0.f};
#pragma unroll
    for (int t = 0; t < 4; ++t) { d0 = mfma(afrag(a0[t], mk), wd[t], d0); d1 = mfma(afrag(a1[t], mk), wd[4 + t], d1); }
    *(LAS f32x4*)(L + trw) = d0 + d1;
    lds_wait();
    pubp(ddst, fadd(*(const LAS float*)(L + trr), *(const LAS float*)(L + trr + 256)), tag);
  }
  STAMP(10);
}

DEV void role_modb(const Params& p, LAS unsigned char* L, int x, int wave, int lane, u64 t0, u64 code0) {
  if (CODE_PF && wave == 8) {
    const unsigned dump = (unsigned)(uintptr_t)(L + L_PF);
#pragma unroll
    for (int k = 0; k < (CODE_PF + 8191) / 8192; ++k) {
      const unsigned o = k * 8192 + lane * 128;
      asm volatile("s_mov_b32 m0, %0\n\ts_nop 0\n\tglobal_load_lds_dword %1, off" :: "s"(dump), "v"(code0 + (o < CODE_PF - 128 ? o : CODE_PF - 128)) : "m0", "memory");
    }
  }
  const Mask mk = make_mask(lane, true);
  if (wave == 0) tag_put(L, load_state(p.ws), lane);
  bar();
  const Tag tag = tag_get(L);
  gate_wait(t0, GATE_MODB);
  for (int k = wave; k < 12; k += 9) {
    ModA ma;
    moda_load(p, 120 + x + 8 * k, lane, ma);
    __builtin_amdgcn_sched_barrier(0);
    moda_finish(L, p.ws, 120 + x + 8 * k, lane, ma, mk, (LAS float*)(L + wave * 1024), tag);
  }
  STAMP(2);
}

__global__ void __launch_bounds__(NTHR) adaln_block_kernel(Params p) {
  __shared__ __attribute__((aligned(16))) unsigned char smem[L_END + L_EXTRA];
  LAS unsigned char* L = (LAS unsigned char*)smem;
  const int lane = threadIdx.x & 63, wave = __builtin_amdgcn_readfirstlane(threadIdx.x >> 6);
  u64 code0;
  asm volatile("s_getpc_b64 %0" : "=s"(code0));
  code0 &= ~0xFFull;
  const u64 t0 = __builtin_amdgcn_s_memrealtime();
  STAMP_INIT();
  STAMP(0);
  const int x = blockIdx.x & 7, idx = blockIdx.x >> 3;
  if (idx < 22) role_att(p, L, x, idx, wave, lane, t0);
  else if (idx < 31) role_ffn(p, L, x, idx - 22, wave, lane, t0);
  else role_modb(p, L, x, wave, lane, t0, code0);
  STAMP_DUMP();
}

extern "C" void kernel_launch(void* const* d_in, const int* in_sizes, int n_in, void* d_out, int out_size, void* d_ws,
                              size_t ws_size, hipStream_t stream) {
  Params p;
  p.x = (const u16*)d_in[0];
  p.kc = (const u16*)d_in[1];
  p.vc = (const u16*)d_in[2];
  p.wq = (const u16*)d_in[3];
  p.wo = (const u16*)d_in[4];
  p.wg = (const u16*)d_in[6];
  p.wu = (const u16*)d_in[7];
  p.wd = (const u16*)d_in[8];
  p.c = (const u16*)d_in[9];
  p.wmod = (const u16*)d_in[10];
  p.out = (u16*)d_out;
  p.ws = (u64*)d_ws;
  hipLaunchKernelGGL(adaln_block_kernel, dim3(256), dim3(NTHR), 0, stream, p);
}
```

```cpp
#include <hip/hip_runtime.h>
#include <stdint.h>

typedef unsigned short u16;
typedef unsigned long long u64;
typedef unsigned v4u __attribute__((ext_vector_type(4)));
typedef float f32x4 __attribute__((ext_vector_type(4)));
typedef float f32x2 __attribute__((ext_vector_type(2)));
typedef __bf16 bf16x2 __attribute__((ext_vector_type(2)));
typedef short bf16x8 __attribute__((ext_vector_type(8)));
typedef __amdgpu_buffer_rsrc_t rsrc_t;
#define GAS __attribute__((address_space(1)))
#define LAS __attribute__((address_space(3)))
#define DEV __device__ __forceinline__
typedef GAS u64 gu64;
#define RLX_AGENT __ATOMIC_RELAXED, __HIP_MEMORY_SCOPE_AGENT

constexpr int D = 576, SK = 65536, WIN = 4096, DH = 128, FF = 4608, NMOD = 6 * D;
constexpr int NSPLIT = 11;
constexpr int NTHR = 576;
constexpr int SPIN_CAP = 1 << 16;
#define GATE_FFN 790
#define GATE_MODB 200
#define GATE_ATT 140
#define GATE_Q 500
#define GATE_FFN2 0
#define F_N1 0
#define F_D1 2
#define GATE_X2 0
#define POLL_SLEEP 0
#define Q_STAGGER 16
#define GATE_H1 260
#define DN_FIRST 0
#define MODA_HOLD 0
#define GATE_MODB2 120
#define CODE_PF 21504
#define GATE_DN 0
#define DN_SECOND 4
#define PAGE_TOUCH 0
#define V_N1 0
#define V_D1 2
#define V_D2 24
#define ATT_N1 5
#define ATT_D1 2
#define ATT_D2 24

constexpr int G_EPOCH = 0;
constexpr int G_MOD = 64;
constexpr int G_Q = G_MOD + 4 * NMOD;
constexpr int G_O = G_Q + 32 * 2 * 128;
constexpr int G_WO = G_O + 16 * 8 * NSPLIT * 64;
constexpr int G_X2 = G_WO + 128 * 576;
constexpr int G_DN = G_X2 + 72 * 16;
constexpr int G_END = G_DN + 72 * 576;
constexpr size_t G_STAMP = 1 << 20;

constexpr int L_K = 0;
constexpr int L_OL = 98304;
constexpr int L_Q = L_OL + 9216;
constexpr int L_MX = L_Q + 512;
constexpr int L_LS = L_MX + 64;
constexpr int L_OB = L_LS + 64;
constexpr int L_PF = L_OB + 128;
constexpr int L_WO = L_PF + 256;
constexpr int L_MA = L_WO + 36864;
constexpr int L_CNT = L_MA + 1536;
constexpr int L_TAG = L_MA + 1536 + 16;
constexpr int L_END = L_TAG + 16;
constexpr int F_RED = 0;
constexpr int F_H = 16384;
constexpr int F_H2 = 17664;
constexpr int F_ACT = 20736;
constexpr int F_TR = 21248;
constexpr int F_ST = 27136;

struct Params {
  const u16 *x, *kc, *vc, *wq, *wo, *wg, *wu, *wd, *c, *wmod;
  u16* out;
  u64* ws;
};

#define STAMP(i) do {} while (0)
#define STAMP_INIT() do {} while (0)
#define STAMP_DUMP() do {} while (0)
#define L_EXTRA 0

DEV float bf2f(unsigned b) { return __uint_as_float(b << 16); }
DEV unsigned cvt2(float lo, float hi) {
  const f32x2 v = {lo, hi};
  return __builtin_bit_cast(unsigned, __builtin_convertvector(v, bf16x2));
}
DEV unsigned pkr(float f) { return cvt2(f, f); }
DEV float rbf(float f) { return __uint_as_float(cvt2(f, f) & 0xFFFF0000u); }
#define TAG_LO_BITS 4
constexpr unsigned TAGM = (1u << TAG_LO_BITS) - 1u;
struct Tag { unsigned hi, lo, rest; };
DEV Tag make_tag(u64 st) {
  const u64 nx = st * 6364136223846793005ull + 1442695040888963407ull;
  Tag t;
  t.hi = __builtin_amdgcn_readfirstlane((unsigned)(nx >> 32));
  t.rest = __builtin_amdgcn_readfirstlane((unsigned)nx);
  t.lo = t.rest >> (32 - TAG_LO_BITS);
  return t;
}
DEV u64 enc(float v, unsigned hi, unsigned lo) {
  const unsigned u = (__float_as_uint(v) + (TAGM + 1) / 2) & ~TAGM;
  return ((u64)hi << 32) | (u64)(u | lo);
}
DEV Tag tag_put(LAS unsigned char* L, u64 st, int lane) {
  const Tag t = make_tag(st);
  if (lane == 0) *(LAS v4u*)(L + L_TAG) = v4u{t.hi, t.lo, t.rest, 0u};
  return t;
}
DEV Tag tag_get(const LAS unsigned char* L) {
  const v4u v = *(const LAS v4u*)(L + L_TAG);
  Tag t; t.hi = __builtin_amdgcn_readfirstlane(v[0]); t.lo = __builtin_amdgcn_readfirstlane(v[1]); t.rest = __builtin_amdgcn_readfirstlane(v[2]);
  return t;
}
DEV unsigned or_xor(unsigned a, unsigned b, unsigned c) { return __builtin_amdgcn_bitop3_b32(a, b, c, 0xF6); }
struct Chk {
  unsigned a = 0, b = 0;
  DEV void add(u64 g, const Tag& t) { a = or_xor(a, (unsigned)(g >> 32), t.hi); b = or_xor(b, (unsigned)g, t.lo); }
  DEV bool all_ok() const { return __builtin_amdgcn_ballot_w64((a | (b & TAGM)) != 0u) == 0ull; }
};
DEV float gval(u64 g) { return __uint_as_float((unsigned)g & ~TAGM); }
DEV void pub(u64* ws, int idx, float v, const Tag& t) { __hip_atomic_store((gu64*)ws + idx, enc(v, t.hi, t.lo), RLX_AGENT); }
DEV void pubp(gu64* dst, float v, const Tag& t) { __hip_atomic_store(dst, enc(v, t.hi, t.lo), RLX_AGENT); }
DEV u64 peek(u64* ws, int idx) { return __hip_atomic_load((gu64*)ws + idx, RLX_AGENT); }
template <int N, class F> DEV void gather(u64* ws, const Tag& t, u64 (&g)[N], F idx) {
  for (int spin = 0; spin < SPIN_CAP; ++spin) {
#pragma unroll
    for (int k = 0; k < N; ++k) g[k] = peek(ws, idx(k));
    Chk c;
#pragma unroll
    for (int k = 0; k < N; ++k) c.add(g[k], t);
    if (c.all_ok()) break;
    if (POLL_SLEEP) __builtin_amdgcn_s_sleep(POLL_SLEEP);
  }
}

struct G2 { v4u v; DEV float lo() const { return __uint_as_float(v[0] & ~TAGM); } DEV float hi() const { return __uint_as_float(v[2] & ~TAGM); } };
template <int N, class F> DEV void gather2(rsrc_t rw, const Tag& t, G2 (&g)[N], F idx) {
  for (int spin = 0; spin < SPIN_CAP; ++spin) {
    asm volatile("" ::: "memory");
#pragma unroll
    for (int k = 0; k < N; ++k) g[k].v = __builtin_amdgcn_raw_buffer_load_b128(rw, idx(k) * 8, 0, 16);
    unsigned a = 0, b = 0;
#pragma unroll
    for (int k = 0; k < N; ++k) { a = or_xor(or_xor(a, g[k].v[1], t.hi), g[k].v[3], t.hi); b = or_xor(or_xor(b, g[k].v[0], t.lo), g[k].v[2], t.lo); }
    if (__builtin_amdgcn_ballot_w64((a | (b & TAGM)) != 0u) == 0ull) break;
    if (POLL_SLEEP) __builtin_amdgcn_s_sleep(POLL_SLEEP);
  }
}
template <int N, class F> DEV void pretouch(LAS unsigned char* L, u64* ws, F idx) {
  const unsigned dump = (unsigned)(uintptr_t)(L + L_PF);
#pragma unroll
  for (int k = 0; k < N; ++k)
    asm volatile("s_mov_b32 m0, %0\n\ts_nop 0\n\tglobal_load_lds_dword %1, off sc1" :: "s"(dump), "v"((gu64*)ws + idx(k)) : "m0", "memory");
}
DEV void page_touch(LAS unsigned char* L, const void* a) {
  asm volatile("s_mov_b32 m0, %0\n\ts_nop 0\n\tglobal_load_lds_dword %1, off nt" :: "s"((unsigned)(uintptr_t)(L + L_PF)), "v"((const GAS char*)a) : "m0", "memory");
}
DEV f32x4 mfma(v4u a, v4u b, f32x4 c) {
  return __builtin_amdgcn_mfma_f32_16x16x32_bf16(__builtin_bit_cast(bf16x8, a), __builtin_bit_cast(bf16x8, b), c, 0, 0, 0);
}
DEV rsrc_t mk_rsrc(const void* p, unsigned bytes) { return __builtin_amdgcn_make_buffer_rsrc((void*)p, 0, (int)bytes, 0x00020000); }
DEV v4u ldb(rsrc_t r, unsigned voff, unsigned soff) { return __builtin_amdgcn_raw_buffer_load_b128(r, (int)voff, (int)soff, 2); }
#define SMALL_AUX 0
DEV unsigned ldb16(rsrc_t r, unsigned voff) { return __builtin_amdgcn_raw_buffer_load_b16(r, (int)voff, 0, SMALL_AUX); }
#define E_AUX 16
DEV u64 load_state(u64* ws) {
  int zoff = G_EPOCH * 8;
  asm volatile("" : "+v"(zoff));
  typedef unsigned v2u_ __attribute__((ext_vector_type(2)));
  const v2u_ v = __builtin_amdgcn_raw_buffer_load_b64(mk_rsrc(ws, 64), zoff, 0, E_AUX);
  return ((u64)v[1] << 32) | v[0];
}
DEV void bar() {
  asm volatile("s_waitcnt lgkmcnt(0)" ::: "memory");
  __builtin_amdgcn_s_barrier();
  asm volatile("" ::: "memory");
}
#define PIN(x) asm volatile("" : "+v"(x))
#define PINS(x) asm volatile("" : "+s"(x))
DEV void lds_wait() { asm volatile("s_waitcnt lgkmcnt(0)" ::: "memory"); }
template <int CTRL> DEV float dpp(float x) {
  return __builtin_bit_cast(float, __builtin_amdgcn_mov_dpp(__builtin_bit_cast(int, x), CTRL, 0xf, 0xf, true));
}
constexpr int ROR8 = 0x128, ROR4 = 0x124, ROR2 = 0x122, ROR1 = 0x121, XOR1 = 0xB1, XOR2 = 0x4E;
DEV float fmx(float a, float b) { float r; asm("v_max_f32 %0, %1, %2\n\ts_nop 1" : "=v"(r) : "v"(a), "v"(b)); return r; }
DEV float fmx3(float a, float b, float c) { float r; asm("v_max3_f32 %0, %1, %2, %3" : "=v"(r) : "v"(a), "v"(b), "v"(c)); return r; }
DEV float fmx3n(float a, float b, float c) { float r; asm("v_max3_f32 %0, %1, %2, %3\n\ts_nop 1" : "=v"(r) : "v"(a), "v"(b), "v"(c)); return r; }
DEV float x16_sum(float x) {
  auto s = __builtin_amdgcn_permlane16_swap(__float_as_uint(x), __float_as_uint(x), false, false);
  x = __uint_as_float(s[0]) + __uint_as_float(s[1]);
  auto t = __builtin_amdgcn_permlane32_swap(__float_as_uint(x), __float_as_uint(x), false, false);
  return __uint_as_float(t[0]) + __uint_as_float(t[1]);
}
DEV float x16_max(float x) {
  auto s = __builtin_amdgcn_permlane16_swap(__float_as_uint(x), __float_as_uint(x), false, false);
  x = fmaxf(__uint_as_float(s[0]), __uint_as_float(s[1]));
  auto t = __builtin_amdgcn_permlane32_swap(__float_as_uint(x), __float_as_uint(x), false, false);
  return fmaxf(__uint_as_float(t[0]), __uint_as_float(t[1]));
}
DEV float row_sum(float x) { x += dpp<ROR8>(x); x += dpp<ROR4>(x); x += dpp<ROR2>(x); x += dpp<ROR1>(x); return x; }
DEV float row_max(float x) { x = fmaxf(x, dpp<ROR8>(x)); x = fmaxf(x, dpp<ROR4>(x)); x = fmaxf(x, dpp<ROR2>(x)); x = fmaxf(x, dpp<ROR1>(x)); return x; }
DEV float wave_sum(float x) { return x16_sum(row_sum(x)); }
constexpr float LOG2E = 1.4426950408889634f;
DEV float fadd(float a, float b) { float r; asm("v_add_f32 %0, %1, %2" : "=v"(r) : "v"(a), "v"(b)); return r; }
DEV float faddn(float a, float b) { float r; asm("v_add_f32 %0, %1, %2\n\ts_nop 1" : "=v"(r) : "v"(a), "v"(b)); return r; }
DEV float exp_f(float x) { return __builtin_amdgcn_exp2f(x * LOG2E); }
DEV float rcp_f(float x) { return __builtin_amdgcn_rcpf(x); }
DEV float pair_pick(float a, float b, int lane) {
  const float a2 = dpp<0x50>(a), b2 = dpp<0x50>(b);
  const float a3 = dpp<0xFA>(a), b3 = dpp<0xFA>(b);
  const float lo = (lane & 1) ? b2 : a2, hi = (lane & 1) ? b3 : a3;
  return (lane & 4) ? hi : lo;
}
DEV float rdlane(float x, int l) { return __uint_as_float(__builtin_amdgcn_readlane(__float_as_uint(x), l)); }
DEV void gate_wait(u64 t0, unsigned ticks) { while (__builtin_amdgcn_s_memrealtime() - t0 < (u64)ticks) __builtin_amdgcn_s_sleep(2); }

struct Mask { unsigned m[4]; };
DEV Mask make_mask(int lane, bool on) {
  Mask k;
  const unsigned h = on ? ((lane & 1) ? 0xFFFF0000u : 0x0000FFFFu) : 0u;
#pragma unroll
  for (int r = 0; r < 4; ++r) k.m[r] = (((lane & 7) >> 1) == r) ? h : 0u;
  return k;
}
DEV v4u afrag(unsigned pp, const Mask& k) { return v4u{pp & k.m[0], pp & k.m[1], pp & k.m[2], pp & k.m[3]}; }

DEV unsigned ln_apply_pk(float x, float mean_b, float rden, float c1, float shift) {
  const float ln = rbf(rbf(x - mean_b) * rden);
  return pkr(rbf(ln * c1) + shift);
}
DEV float ln_rden(float var) { return rcp_f(rbf(__builtin_amdgcn_sqrtf(rbf(rbf(var) + 1e-5f)))); }

DEV void red_store(LAS float* area, f32x4 d, int lane) {
  const int u = (lane >> 3) & 1;
  if ((lane >> 5) == u) *(LAS f32x4*)(area + u * 64 + 8 * (lane & 7) + 4 * ((lane >> 4) & 1)) = d;
}
DEV unsigned red_off(int lane, int dump) {
  const int u = (lane >> 3) & 1;
  return 4 * ((lane >> 5) == u ? u * 64 + 8 * (lane & 7) + 4 * ((lane >> 4) & 1) : dump);
}
DEV unsigned silu_pk(unsigned craw) { const float v = bf2f(craw); return pkr(v * rcp_f(1.f + exp_f(-v))); }

struct WoAddr { unsigned tro, tri, obo, wro; gu64* dst; };
DEV WoAddr wo_prep(u64* ws, int pidx, int wave, int lane) {
  WoAddr a;
  a.tro = L_OL + wave * 640 + red_off(lane, 128); a.tri = L_OL + wave * 640 + lane * 4; a.obo = L_OB + (lane >> 3) * 16; a.wro = L_WO + wave * 4096 + lane * 16;
  a.dst = (gu64*)ws + G_WO + pidx * 576 + wave * 64 + lane;
  PIN(a.tro); PIN(a.tri); PIN(a.obo); PIN(a.wro); PIN(a.dst);
  return a;
}
DEV void wo_phase(LAS unsigned char* L, const WoAddr& a, const Tag& tag, const Mask& mk) {
  bar();
  v4u ob = *(const LAS v4u*)(L + a.obo);
  const LAS v4u* wr = (const LAS v4u*)(L + a.wro);
  v4u w0 = wr[0], w1 = wr[64], w2 = wr[128], w3 = wr[192];
  PIN(ob); PIN(w0); PIN(w1); PIN(w2); PIN(w3);
  __builtin_amdgcn_sched_barrier(0);
  f32x4 d0 = {0.f, 0.f, 0.f, 0.f}, d1 = {0.f, 0.f, 0.f, 0.f};
  d0 = mfma(afrag(ob[0], mk), w0, d0); d1 = mfma(afrag(ob[1], mk), w1, d1);
  d0 = mfma(afrag(ob[2], mk), w2, d0); d1 = mfma(afrag(ob[3], mk), w3, d1);
  *(LAS f32x4*)(L + a.tro) = d0 + d1;
  lds_wait();
  pubp(a.dst, fadd(*(const LAS float*)(L + a.tri), *(const LAS float*)(L + a.tri + 256)), tag);
}

struct ModA { v4u w[18]; unsigned c[3]; };
DEV void moda_load(const Params& p, int mt, int lane, ModA& m) {
  const rsrc_t rm = mk_rsrc(p.wmod, D * NMOD * 2), rc = mk_rsrc(p.c, D * 2);
  const int r0 = (mt & 3) * 144;
#pragma unroll
  for (int k = 0; k < 3; ++k) m.c[k] = ldb16(rc, (r0 + (k < 2 || lane < 16 ? lane + 64 * k : 143)) * 2);
  const unsigned v = (r0 + (lane >> 3)) * (NMOD * 2) + (lane & 7) * 16;
#pragma unroll
  for (int t = 0; t < 18; ++t) m.w[t] = ldb(rm, v, (mt >> 2) * 128 + t * 8 * (NMOD * 2));
}
DEV void moda_finish(LAS unsigned char* L, u64* ws, int mt, int lane, ModA& m, const Mask& mk, LAS float* tr, const Tag& tag) {
  unsigned sp[18];
  {
    const unsigned s0 = silu_pk(m.c[0]), s1 = silu_pk(m.c[1]), s2 = silu_pk(m.c[2]);
#pragma unroll
    for (int t = 0; t < 18; ++t) sp[t] = __builtin_amdgcn_ds_bpermute(((8 * t + (lane >> 3)) & 63) * 4, t < 8 ? s0 : t < 16 ? s1 : s2);
  }
  gu64* dst = (gu64*)ws + G_MOD + (mt & 3) * NMOD + (mt >> 2) * 64 + lane;
  unsigned tro = red_off(lane, 128);
  PIN(dst); PIN(tro);
#pragma unroll
  for (int t = 0; t < 18; ++t) PIN(sp[t]);
  __builtin_amdgcn_sched_barrier(0);
  f32x4 d0 = {0.f, 0.f, 0.f, 0.f}, d1 = {0.f, 0.f, 0.f, 0.f};
#pragma unroll
  for (int t = 0; t < 18; t += 2) {
    d0 = mfma(afrag(sp[t], mk), m.w[t], d0);
    __builtin_amdgcn_sched_barrier(0);
    d1 = mfma(afrag(sp[t + 1], mk), m.w[t + 1], d1);
    __builtin_amdgcn_sched_barrier(0);
  }
  *(LAS f32x4*)((LAS unsigned char*)tr + tro) = d0 + d1;
  lds_wait();
  pubp(dst, fadd(tr[lane], tr[64 + lane]), tag);
}

DEV void role_att(const Params& p, LAS unsigned char* L, int x, int i, int wave, int lane, u64 t0) {
  const int g = 2 * x + (i >= NSPLIT ? 1 : 0), j = i >= NSPLIT ? i - NSPLIT : i;
  const bool odd = g & 1;
  const int nm = odd ? 4 : 5;
  const bool moda = j < nm;
  const int ntile = !moda ? 24 : (!odd && j >= 3) ? 23 : 22;
  const int tile0 = !moda ? (odd ? 88 : 112) + 24 * (j - nm) : (!odd && j == 4) ? 89 : 22 * j;
  const bool owner = j < 8;
  int redc = -1;
  if (j >= 8) redc = 9 * x + (g & 1) * 3 + (j - 8);
  else if (j == 0) redc = 9 * x + 6 + (g & 1);
  else if (j == 1 && (g & 1) == 0) redc = 9 * x + 8;
  u64* ws = p.ws;
  const Mask mk = make_mask(lane, true);
  const int kap = lane >> 4, cc = lane & 15;

  if (wave < 8) {
    const size_t hb = ((size_t)g * SK + (SK - WIN)) * DH;
    const rsrc_t rk = mk_rsrc(p.kc + hb, WIN * DH * 2), rv = mk_rsrc(p.vc + hb, WIN * DH * 2);
    const bool three = wave + 16 < ntile;
    const unsigned tso = (tile0 + wave) * 4096;
    const unsigned ldsk = (unsigned)(uintptr_t)(L + L_K) + wave * 3 * 4096;
    unsigned vk[4];
#pragma unroll
    for (int q = 0; q < 4; ++q) vk[q] = (4 * q + kap) * 256 + ((cc ^ (4 * q + kap)) << 4);
#define KDMA(s, q) asm volatile("s_mov_b32 m0, %0\n\ts_nop 0\n\tbuffer_load_dwordx4 %1, %2, %3 offen nt lds" \
                     :: "s"(ldsk + (s) * 4096 + (q) * 1024), "v"(vk[q]), "s"(rk), "s"(tso + (s) * 32768) : "m0", "memory")
#define THR(n) do { if ((n) < ATT_N1) { if ((n) >= ATT_D1 && moda) asm volatile("s_waitcnt vmcnt(%0)" :: "n"(ATT_D1 - 1) : "memory"); } \
                     else if ((n) >= ATT_D2) asm volatile("s_waitcnt vmcnt(%0)" :: "n"(ATT_D2 - 1) : "memory"); } while (0)
#define THR_DEPTH(n) ((n) < ATT_N1 ? ATT_D1 : ATT_D2)
#define THRV(t) asm volatile("s_waitcnt vmcnt(%0)" :: "n"(((t) < V_N1 ? V_D1 : V_D2) - 1) : "memory")
    if (moda) __builtin_amdgcn_s_barrier();
    __builtin_amdgcn_s_setprio(2);
    KDMA(0, 0);
    if (GATE_ATT) gate_wait(t0, GATE_ATT);
    if (MODA_HOLD && moda) __builtin_amdgcn_s_barrier();
    THR(1); KDMA(0, 1); THR(2); KDMA(0, 2); THR(3); KDMA(0, 3);
    THR(4); KDMA(1, 0); THR(5); KDMA(1, 1); THR(6); KDMA(1, 2); THR(7); KDMA(1, 3);
    if (three) { THR(8); KDMA(2, 0); THR(9); KDMA(2, 1); THR(10); KDMA(2, 2); THR(11); KDMA(2, 3); }
#undef KDMA
    __builtin_amdgcn_s_setprio(0);
    v4u vr[12];
    const unsigned vv = kap * 1024 + cc * 16;
#pragma unroll
    for (int t = 0; t < 8; ++t) { THRV(t); vr[t] = ldb(rv, vv + (t & 3) * 256, tso + (t >> 2) * 32768); }
    if (three) {
#pragma unroll
      for (int t = 8; t < 12; ++t) { THRV(t); vr[t] = ldb(rv, vv + (t & 3) * 256, tso + 65536); }
    } else {
#pragma unroll
      for (int t = 8; t < 12; ++t) vr[t] = v4u{0u, 0u, 0u, 0u};
    }
#undef THR
#undef THRV
#undef THR_DEPTH
    __builtin_amdgcn_sched_barrier(0);
    STAMP(1);
    if (three) asm volatile("s_waitcnt vmcnt(12)" ::: "memory");
    else asm volatile("s_waitcnt vmcnt(8)" ::: "memory");
    v4u kf[12];
#pragma unroll
    for (int s = 0; s < 3; ++s)
#pragma unroll
      for (int ks = 0; ks < 4; ++ks)
        kf[s * 4 + ks] = *(const LAS v4u*)(L + L_K + (wave * 3 + (s < 2 || three ? s : 0)) * 4096 + cc * 256 + (((4 * ks + kap) ^ cc) << 4));
#pragma unroll
    for (int t = 0; t < 12; ++t) PIN(kf[t]);
    const int head = (lane >> 3) & 1;
    const int me = (wave & 3) * 64 + lane;
    unsigned qsrc = L_Q + head * 256 + kap * 16, mxw = L_MX + (head * 8 + wave) * 4, mxr = L_MX + head * 32, olw = L_OL + wave * 1024 + lane * 16;
    unsigned msrc = L_OL + ((16 * (((me >> 7) * 8 + (me & 7)) >> 2) + ((me & 127) >> 3)) * 4 + (me & 3)) * 4;
    gu64* mdst = (gu64*)ws + G_O + ((g * 8 + (me >> 5)) * NSPLIT + j) * 64 + (me & 31);
    bool lead = (lane & 0x37) == 0;
    PIN(qsrc); PIN(mxw); PIN(mxr); PIN(olw); PIN(msrc); PIN(mdst);
    STAMP(3);
    bar();
    STAMP(2);
    v4u qf[4];
#pragma unroll
    for (int ks = 0; ks < 4; ++ks) qf[ks] = *(const LAS v4u*)(L + qsrc + ks * 64);
#pragma unroll
    for (int ks = 0; ks < 4; ++ks) PIN(qf[ks]);
    __builtin_amdgcn_sched_barrier(0);
    f32x4 acc[3] = {{0.f, 0.f, 0.f, 0.f}, {0.f, 0.f, 0.f, 0.f}, {0.f, 0.f, 0.f, 0.f}};
#pragma unroll
    for (int ks = 0; ks < 4; ++ks)
#pragma unroll
      for (int s = 0; s < 3; ++s) acc[s] = mfma(kf[s * 4 + ks], qf[ks], acc[s]);
    __builtin_amdgcn_sched_barrier(0);
    float sc[12];
#pragma unroll
    for (int t = 0; t < 12; t += 2) {
      const unsigned u = cvt2(acc[t >> 2][t & 3], acc[t >> 2][(t & 3) + 1]);
      const unsigned w = cvt2(__uint_as_float(u << 16) * 0.08838834764831845f, __uint_as_float(u & 0xFFFF0000u) * 0.08838834764831845f);
      sc[t] = __uint_as_float(w << 16);
      sc[t + 1] = __uint_as_float(w & 0xFFFF0000u);
    }
    if (!three) {
#pragma unroll
      for (int t = 8; t < 12; ++t) sc[t] = -INFINITY;
    }
    float mx = fmx3n(fmx3(fmx3(sc[0], sc[1], sc[2]), fmx3(sc[3], sc[4], sc[5]), fmx3(sc[6], sc[7], sc[8])), fmx3(sc[9], sc[10], sc[11]), sc[0]);
    mx = x16_max(mx);
    if (lead) *(LAS float*)(L + mxw) = mx;
    lds_wait();
    if (lane == 0) __hip_atomic_fetch_add((LAS unsigned*)(L + L_CNT), 1u, __ATOMIC_RELAXED, __HIP_MEMORY_SCOPE_WORKGROUP);
    while (*(volatile LAS unsigned*)(L + L_CNT) < 8u) {}
    asm volatile("" ::: "memory");
    float m;
    {
      const f32x4 a = *(const LAS f32x4*)(L + mxr), b = *(const LAS f32x4*)(L + mxr + 16);
      m = fmx3(fmx3(a[0], a[1], a[2]), fmx3(a[3], b[0], b[1]), fmx(b[2], b[3]));
    }
    v4u af[12];
    float ls0 = 0.f, ls1 = 0.f;
    const float mneg = -m * LOG2E;
#pragma unroll
    for (int t = 0; t < 12; t += 2) {
      const float e0 = __builtin_amdgcn_exp2f(fmaf(sc[t], LOG2E, mneg)), e1 = __builtin_amdgcn_exp2f(fmaf(sc[t + 1], LOG2E, mneg));
      ls0 += e0; ls1 += e1;
      af[t] = afrag(pkr(e0), mk); af[t + 1] = afrag(pkr(e1), mk);
    }
    const float ls = x16_sum(ls0 + ls1);
    if (lead) *(LAS float*)(L + mxw + 64) = ls;
    const Tag tag = tag_get(L);
    __builtin_amdgcn_sched_barrier(0);
    STAMP(4);
    f32x4 o0 = {0.f, 0.f, 0.f, 0.f}, o1 = {0.f, 0.f, 0.f, 0.f};
#pragma unroll
    for (int t = 0; t < 12; t += 2) { o0 = mfma(af[t], vr[t], o0); o1 = mfma(af[t + 1], vr[t + 1], o1); }
    *(LAS f32x4*)(L + olw) = o0 + o1;
    STAMP(5);
    bar();
    if (wave < 4) {
      const LAS float* src = (const LAS float*)(L + msrc);
      float s[8];
#pragma unroll
      for (int w = 0; w < 8; ++w) s[w] = src[w * 256];
      pubp(mdst, fadd(fadd(fadd(s[0], s[1]), fadd(s[2], s[3])), fadd(fadd(s[4], s[5]), fadd(s[6], s[7]))), tag);
    } else if (wave == 4 && lane < 16) {
      const int u = lane >> 1, hh = u >> 2;
      const f32x4 a = *(const LAS f32x4*)(L + ((lane & 1) ? L_LS : L_MX) + hh * 32), b = *(const LAS f32x4*)(L + ((lane & 1) ? L_LS : L_MX) + hh * 32 + 16);
      const float vs = ((a[0] + a[1]) + (a[2] + a[3])) + ((b[0] + b[1]) + (b[2] + b[3]));
      const float vm = fmaxf(fmaxf(fmaxf(a[0], a[1]), fmaxf(a[2], a[3])), fmaxf(fmaxf(b[0], b[1]), fmaxf(b[2], b[3])));
      pub(ws, G_O + ((g * 8 + u) * NSPLIT + j) * 64 + 32 + (lane & 1), (lane & 1) ? vs : vm, tag);
    }
    STAMP(6);
    if (owner) {
      wo_phase(L, wo_prep(ws, g * 8 + j, wave, lane), tag, mk);
      STAMP(8);
    }
  } else {
    __builtin_amdgcn_s_setprio(3);
    Tag tag;
    {
      if (moda) {
        u64 st = load_state(ws);
        ModA ma;
        moda_load(p, 9 * x + (odd ? 5 : 0) + j, lane, ma);
        __builtin_amdgcn_sched_barrier(0);
        __builtin_amdgcn_s_barrier();
        asm volatile("" : "+v"(st));
        tag = tag_put(L, st, lane);
        moda_finish(L, ws, 9 * x + (odd ? 5 : 0) + j, lane, ma, mk, (LAS float*)(L + L_MA), tag);
        STAMP(9);
        if (MODA_HOLD) __builtin_amdgcn_s_barrier();
      } else {
        tag = tag_put(L, load_state(ws), lane);
      }
    }
    float xr = 0.f;
    if (redc >= 0) xr = bf2f(p.x[redc * 8 + (lane & 7)]);
    if (GATE_Q) gate_wait(t0, GATE_Q);
    const rsrc_t rw = mk_rsrc(ws, G_END * 8);
    {
      G2 gq[4];
      gather2<4>(rw, tag, gq, [&](int k) { return G_Q + ((2 * g + (k >> 1)) * 2 + (k & 1)) * 128 + 2 * lane; });
      ((LAS unsigned*)(L + L_Q))[lane] = cvt2(fadd(gq[0].lo(), gq[1].lo()), fadd(gq[0].hi(), gq[1].hi()));
      ((LAS unsigned*)(L + L_Q))[64 + lane] = cvt2(fadd(gq[2].lo(), gq[3].lo()), fadd(gq[2].hi(), gq[3].hi()));
    }
    STAMP(1);
    if (owner) {
      const int ob = G_O + (g * 8 + j) * NSPLIT * 64;
      pretouch<1>(L, ws, [&](int) { const int q_ = lane < 33 ? lane : 32; return ob + (q_ / 3) * 64 + (q_ % 3) * 16; });
    }
    if (redc >= 0) {
      pretouch<2>(L, ws, [&](int s) { return G_WO + (s * 64 + lane) * 576 + redc * 8; });
      pretouch<2>(L, ws, [&](int s) { const int f_ = s * 64 + lane; return G_DN + (f_ < 72 ? f_ : 71) * 576 + redc * 8; });
    }
    if (lane == 0) *(volatile LAS unsigned*)(L + L_CNT) = 0u;
    bar();
    if (owner) {
      const rsrc_t rwo = mk_rsrc(p.wo, 4096 * D * 2);
      const unsigned wo_v = (lane >> 3) * (D * 2) + (lane & 7) * 16;
      const unsigned wo_s = (g * 256 + j * 32) * (D * 2), ldsw = (unsigned)(uintptr_t)(L + L_WO);
#pragma unroll
      for (int w = 0; w < 9; ++w)
#pragma unroll
        for (int t = 0; t < 4; ++t)
          asm volatile("s_mov_b32 m0, %0\n\ts_nop 0\n\tbuffer_load_dwordx4 %1, %2, %3 offen nt lds"
                       :: "s"(ldsw + (w * 4 + t) * 1024), "v"(wo_v), "s"(rwo), "s"(wo_s + w * 128 + t * 8 * (D * 2)) : "m0", "memory");
    }
    bar();
    STAMP(2);
    if (owner) {
      const int ob = G_O + (g * 8 + j) * NSPLIT * 64;
      const int k2 = kap < 3 ? 8 + kap : 10;
      unsigned obw = L_OB + (((2 * cc) & 7) * 4 + (cc >> 2)) * 4;
      float live = kap < 3 ? 1.f : 0.f;
      PIN(obw); PIN(live);
      const WoAddr wa = wo_prep(ws, g * 8 + j, 8, lane);
      G2 go[6];
      gather2<6>(rw, tag, go, [&](int k) { const int sp = k % 3 == 0 ? kap : k % 3 == 1 ? 4 + kap : k2; return ob + sp * 64 + (k < 3 ? 2 * cc : 32); });
      STAMP(3);
      const float M = x16_max(fmaxf(fmaxf(go[3].lo(), go[4].lo()), go[5].lo()));
      const float Mn = -M * LOG2E;
      const float w0 = __builtin_amdgcn_exp2f(fmaf(go[3].lo(), LOG2E, Mn)), w1 = __builtin_amdgcn_exp2f(fmaf(go[4].lo(), LOG2E, Mn));
      const float w2 = __builtin_amdgcn_exp2f(fmaf(go[5].lo(), LOG2E, Mn)) * live;
      const float Ls = x16_sum(fmaf(w2, go[5].hi(), fmaf(w1, go[4].hi(), w0 * go[3].hi())));
      const float o0 = x16_sum(fmaf(w2, go[2].lo(), fmaf(w1, go[1].lo(), w0 * go[0].lo())));
      const float o1 = x16_sum(fmaf(w2, go[2].hi(), fmaf(w1, go[1].hi(), w0 * go[0].hi())));
      const float rl = rcp_f(Ls);
      *(LAS unsigned*)(L + obw) = pkr(o0 * rl);
      *(LAS unsigned*)(L + obw + 16) = pkr(o1 * rl);
      asm volatile("s_waitcnt vmcnt(0)" ::: "memory");
      wo_phase(L, wa, tag, mk);
      STAMP(4);
    }
    if (redc >= 0) {
      u64 gg[8];
      gather<8>(ws, tag, gg, [&](int k) { return G_MOD + (k & 3) * NMOD + (k < 4 ? 2 : 5) * D + redc * 8 + (lane & 7); });
      float ga = rbf(fadd(fadd(gval(gg[0]), gval(gg[1])), fadd(gval(gg[2]), gval(gg[3]))));
      float gb = rbf(fadd(fadd(gval(gg[4]), gval(gg[5])), fadd(gval(gg[6]), gval(gg[7]))));
      gu64* xdst = (gu64*)ws + G_X2 + redc * 16 + (lane & 7);
      GAS u16* odst = (GAS u16*)p.out + redc * 8 + (lane & 7);
      PIN(ga); PIN(gb); PIN(xr); PIN(xdst); PIN(odst);
      float x2;
      {
        G2 gw[8];
        gather2<8>(rw, tag, gw, [&](int s) { return G_WO + (s * 16 + (lane >> 2)) * 576 + redc * 8 + 2 * (lane & 3); });
        STAMP(5);
        float t0_ = faddn(fadd(fadd(gw[0].lo(), gw[1].lo()), fadd(gw[2].lo(), gw[3].lo())), fadd(fadd(gw[4].lo(), gw[5].lo()), fadd(gw[6].lo(), gw[7].lo())));
        float t1_ = faddn(fadd(fadd(gw[0].hi(), gw[1].hi()), fadd(gw[2].hi(), gw[3].hi())), fadd(fadd(gw[4].hi(), gw[5].hi()), fadd(gw[6].hi(), gw[7].hi())));
        t0_ += dpp<ROR8>(t0_); t1_ += dpp<ROR8>(t1_); t0_ += dpp<ROR4>(t0_); t1_ += dpp<ROR4>(t1_);
        t0_ = x16_sum(t0_); t1_ = x16_sum(t1_);
        const float ts = pair_pick(t0_, t1_, lane);
        x2 = rbf(xr + rbf(ga * rbf(ts)));
        if (lane < 8) pubp(xdst, x2, tag);
      }
      STAMP(6);
      if (PAGE_TOUCH) page_touch(L, p.out);
      {
        G2 gd[5];
        float w4 = lane < 32 ? 1.f : 0.f;
        PIN(w4);
        gather2<5>(rw, tag, gd, [&](int s) { const int f_ = s * 16 + (lane >> 2); return G_DN + (f_ < 72 ? f_ : 71) * 576 + redc * 8 + 2 * (lane & 3); });
        STAMP(7);
        float t0_ = fmaf(w4, gd[4].lo(), fadd(fadd(gd[0].lo(), gd[1].lo()), fadd(gd[2].lo(), gd[3].lo())));
        float t1_ = fmaf(w4, gd[4].hi(), fadd(fadd(gd[0].hi(), gd[1].hi()), fadd(gd[2].hi(), gd[3].hi())));
        t0_ += dpp<ROR8>(t0_); t1_ += dpp<ROR8>(t1_); t0_ += dpp<ROR4>(t0_); t1_ += dpp<ROR4>(t1_);
        t0_ = x16_sum(t0_); t1_ = x16_sum(t1_);
        const float ts = pair_pick(t0_, t1_, lane);
        const unsigned o = pkr(x2 + rbf(gb * rbf(ts)));
        if (lane < 8) *odst = (u16)o;
        if (redc == 0 && lane == 0) __hip_atomic_store((gu64*)ws + G_EPOCH, ((u64)tag.hi << 32) | (u64)tag.rest, RLX_AGENT);
      }
      STAMP(8);
    }
  }
}

DEV void role_ffn(const Params& p, LAS unsigned char* L, int x, int i, int wave, int lane, u64 t0) {
  const int f = 9 * x + i;
  u64* ws = p.ws;
  const Mask mk = make_mask(lane, true);
  LAS float* red = (LAS float*)(L + F_RED);
  const rsrc_t rm = mk_rsrc(p.wmod, D * NMOD * 2), rc = mk_rsrc(p.c, D * 2), rx = mk_rsrc(p.x, D * 2);
  const int hq = 4 * x + (i >> 1), qr = i & 1;
  const rsrc_t rw = mk_rsrc(ws, G_END * 8);
  Tag tag;
  if (i < 8) {
    v4u wa[12];
    if (wave >= 3) {
      const rsrc_t rq = mk_rsrc(p.wq, D * 4096 * 2);
      const unsigned v = (lane >> 4) * 8192 + (lane & 15) * 16, s = (qr * 288 + (wave - 3) * 48) * 8192 + hq * 256;
#pragma unroll
      for (int t = 0; t < 12; ++t) wa[t] = ldb(rq, v, s + t * 4 * 8192);
      __builtin_amdgcn_sched_barrier(0);
      STAMP(1);
    } else {
      __builtin_amdgcn_s_setprio(3);
      const u64 st = load_state(ws);
      float xv[9];
#pragma unroll
      for (int k = 0; k < 9; ++k) xv[k] = bf2f(ldb16(rx, (lane + 64 * k) * 2));
      const int p0 = wave * 64 + lane, pr = p0 < 144 ? p0 : 143;
      const unsigned xo = __builtin_amdgcn_raw_buffer_load_b32(rx, (qr * 288 + 2 * pr) * 2, 0, 0);
      float s = 0.f;
#pragma unroll
      for (int k = 0; k < 9; ++k) s += xv[k];
      const float mf = wave_sum(s) * (1.f / D);
      float v = 0.f;
#pragma unroll
      for (int k = 0; k < 9; ++k) { const float d = xv[k] - mf; v = fmaf(d, d, v); }
      const float rden = ln_rden(wave_sum(v) * (1.f / D)), mean = rbf(mf);
      tag = wave == 0 ? tag_put(L, st, lane) : make_tag(st);
      const int hw = pr / 24, hr = 2 * pr - hw * 48;
      unsigned hdst = F_H + ((hw * 4 + (hr & 3)) * 12 + (hr >> 2)) * 4;
      float ln0 = rbf(rbf(__uint_as_float(xo << 16) - mean) * rden), ln1 = rbf(rbf(__uint_as_float(xo & 0xFFFF0000u) - mean) * rden);
      PIN(hdst); PIN(ln0); PIN(ln1);
      STAMP(2);
      if (GATE_H1) gate_wait(t0, GATE_H1);
      G2 ga[8];
      gather2<8>(rw, tag, ga, [&](int k) { return G_MOD + (k & 3) * NMOD + (k >> 2) * D + qr * 288 + 2 * pr; });
      STAMP(3);
      const float sa0 = rbf(fadd(fadd(ga[0].lo(), ga[1].lo()), fadd(ga[2].lo(), ga[3].lo()))), sa1 = rbf(fadd(fadd(ga[0].hi(), ga[1].hi()), fadd(ga[2].hi(), ga[3].hi())));
      const float ca0 = rbf(fadd(fadd(ga[4].lo(), ga[5].lo()), fadd(ga[6].lo(), ga[7].lo()))), ca1 = rbf(fadd(fadd(ga[4].hi(), ga[5].hi()), fadd(ga[6].hi(), ga[7].hi())));
      *(LAS unsigned*)(L + hdst) = pkr(rbf(ln0 * rbf(1.f + ca0)) + sa0);
      *(LAS unsigned*)(L + hdst + 48) = pkr(rbf(ln1 * rbf(1.f + ca1)) + sa1);
      __builtin_amdgcn_s_setprio(0);
    }
    Mask mq = make_mask(lane, (lane & 8) == 0);
    const int qe = (wave & 1) * 64 + lane;
    unsigned hsrc = F_H + ((wave - 3) * 4 + (lane >> 4)) * 48, qst = F_RED + 4608 + (wave - 3) * 1024 + lane * 16;
    unsigned qsrc = F_RED + 4608 + ((16 * ((qe & 7) >> 2) + (qe >> 3)) * 4 + (qe & 3)) * 4;
    gu64* qdst = (gu64*)ws + G_Q + (hq * 2 + qr) * 128 + qe;
    PIN(mq.m[0]); PIN(mq.m[1]); PIN(mq.m[2]); PIN(mq.m[3]); PIN(hsrc); PIN(qst); PIN(qsrc); PIN(qdst);
    bar();
    if (wave >= 3) {
      const LAS v4u* hp = (const LAS v4u*)(L + hsrc);
      const v4u h0 = hp[0], h1 = hp[1], h2 = hp[2];
      f32x4 d0 = {0.f, 0.f, 0.f, 0.f}, d1 = {0.f, 0.f, 0.f, 0.f};
#pragma unroll
      for (int t = 0; t < 4; ++t) { d0 = mfma(afrag(h0[t], mq), wa[t], d0); d1 = mfma(afrag(h1[t], mq), wa[4 + t], d1); }
#pragma unroll
      for (int t = 0; t < 4; t += 2) { d0 = mfma(afrag(h2[t], mq), wa[8 + t], d0); d1 = mfma(afrag(h2[t + 1], mq), wa[9 + t], d1); }
      *(LAS f32x4*)(L + qst) = d0 + d1;
    }
    bar();
    if (wave < 2) {
      const LAS float* src = (const LAS float*)(L + qsrc);
      pubp(qdst, fadd(fadd(fadd(src[0], src[256]), fadd(src[512], src[768])), fadd(src[1024], src[1280])), tag);
    }
  } else {
    if (wave == 0) tag_put(L, load_state(ws), lane);
    if (GATE_MODB2) gate_wait(t0, GATE_MODB2);
    ModA ma;
    if (wave < 6) moda_load(p, 72 + 6 * x + wave, lane, ma);
    __builtin_amdgcn_sched_barrier(0);
    bar();
    if (wave < 6) {
      tag = tag_get(L);
      moda_finish(L, ws, 72 + 6 * x + wave, lane, ma, mk, (LAS float*)(L + F_RED + wave * 1024), tag);
    }
  }
  tag = tag_get(L);
  STAMP(4);
  pretouch<1>(L, ws, [&](int) { const int e = wave * 64 + lane; return G_X2 + (e >> 3) * 16 + (e & 7); });
  if (PAGE_TOUCH && wave == 8) {
    const int k = lane & 7;
    const char* a = lane < 8 ? (const char*)p.wg + (size_t)(k < 7 ? k : 6) * 786432 + f * 128
                  : lane < 16 ? (const char*)p.wu + (size_t)(k < 7 ? k : 6) * 786432 + f * 128
                              : (const char*)p.wd + (size_t)f * 64 * D * 2 + (lane & 1) * 65536;
    page_touch(L, a);
  }
  const bool slow_tile = ((((unsigned)(uintptr_t)p.wg >> 7) + f) & 7) == 3;
  v4u wg[9], wu[9], wd[8];
#define THRF(n) do { if ((n) < F_N1 && !slow_tile && __builtin_amdgcn_s_memrealtime() - t0 < (u64)GATE_FFN2) asm volatile("s_waitcnt vmcnt(%0)" :: "n"(F_D1 - 1) : "memory"); } while (0)
  if (GATE_DN) {
    gate_wait(t0, GATE_DN);
    const rsrc_t rd = mk_rsrc(p.wd, FF * D * 2);
    const unsigned v = (lane >> 3) * (D * 2) + (lane & 7) * 16, s = f * 64 * (D * 2) + wave * 128;
#pragma unroll
    for (int t = 0; t < DN_FIRST; ++t) wd[t] = ldb(rd, v, s + t * 8 * (D * 2));
    __builtin_amdgcn_sched_barrier(0);
  }
  if (!slow_tile) gate_wait(t0, GATE_FFN);
  if (wave < 8) {
    const rsrc_t rg = mk_rsrc(p.wg, D * FF * 2), ru = mk_rsrc(p.wu, D * FF * 2);
    const unsigned v = (lane >> 3) * (FF * 2) + (lane & 7) * 16, s = wave * 72 * (FF * 2) + f * 128;
#pragma unroll
    for (int t = 0; t < 9; ++t) { THRF(2 * t); wg[t] = ldb(rg, v, s + t * 8 * (FF * 2)); THRF(2 * t + 1); wu[t] = ldb(ru, v, s + t * 8 * (FF * 2)); }
  }
  if (!GATE_DN) {
    const rsrc_t rd = mk_rsrc(p.wd, FF * D * 2);
    const unsigned v = (lane >> 3) * (D * 2) + (lane & 7) * 16, s = f * 64 * (D * 2) + wave * 128;
#pragma unroll
    for (int t = 0; t < DN_FIRST; ++t) { THRF(18 + t); wd[t] = ldb(rd, v, s + t * 8 * (D * 2)); }
  }
  __builtin_amdgcn_sched_barrier(0);
  STAMP(5);
  unsigned h2w, h2r, stw, str_, gur, guw0, actw, actr, trw, trr;
  gu64* ddst;
  {
    const int e = wave * 64 + lane, hw = e / 72, rem = e - hw * 72, c = (wave & 3) * 16 + (lane >> 2);
    h2w = F_H2 + ((hw * 8 + (rem & 7)) * 12 + (rem >> 3)) * 4;
    h2r = F_H2 + ((wave & 7) * 8 + (lane >> 3)) * 48;
    stw = F_ST + wave * 8;
    str_ = F_ST + ((lane & 15) < 9 ? (lane & 15) : 9) * 8;
    guw0 = F_RED + (wave & 7) * 1040 + red_off(lane, 256);
    gur = F_RED + ((lane & 3) * 520 + c) * 4;
    actw = F_ACT + ((lane & 3) == 0 ? (c & 7) * 8 + (c >> 3) : 64) * 4;
    actr = F_ACT + (lane >> 3) * 32;
    trw = F_TR + wave * 640 + red_off(lane, 128);
    trr = F_TR + wave * 640 + lane * 4;
    ddst = (gu64*)ws + G_DN + f * 576 + wave * 64 + lane;
    if (wave == 0 && lane == 0) *(LAS f32x2*)(L + F_ST + 72) = f32x2{0.f, 0.f};
  }
  PIN(h2w); PIN(h2r); PIN(stw); PIN(str_); PIN(guw0); PIN(gur); PIN(actw); PIN(actr); PIN(trw); PIN(trr); PIN(ddst);
  if (GATE_X2) gate_wait(t0, GATE_X2);
  {
    const int e = wave * 64 + lane;
    u64 gs[9];
    bool have_x2 = false;
    for (int spin = 0; spin < SPIN_CAP; ++spin) {
#pragma unroll
      for (int k = 0; k < 8; ++k) gs[k] = peek(ws, G_MOD + (k & 3) * NMOD + (k < 4 ? 3 : 4) * D + e);
      gs[8] = peek(ws, G_X2 + (e >> 3) * 16 + (e & 7));
      Chk c, cx;
#pragma unroll
      for (int k = 0; k < 8; ++k) c.add(gs[k], tag);
      cx.add(gs[8], tag);
      have_x2 = cx.all_ok();
      if (c.all_ok()) break;
    }
    float sb = rbf(fadd(fadd(gval(gs[0]), gval(gs[1])), fadd(gval(gs[2]), gval(gs[3]))));
    float c1b = rbf(1.f + rbf(fadd(fadd(gval(gs[4]), gval(gs[5])), fadd(gval(gs[6]), gval(gs[7])))));
    float on = (lane & 15) < 9 ? 64.f : 0.f;
    PIN(sb); PIN(c1b); PIN(on);
    STAMP(6);
    u64 gx[1] = {gs[8]};
    if (!have_x2) gather<1>(ws, tag, gx, [&](int) { return G_X2 + (e >> 3) * 16 + (e & 7); });
    STAMP(7);
    const float xv = gval(gx[0]);
    const float sh = __uint_as_float(__builtin_amdgcn_readfirstlane(__float_as_uint(xv)));
    const float d = xv - sh;
    float s1 = d, s2 = d * d;
    s1 += dpp<ROR8>(s1); s2 += dpp<ROR8>(s2); s1 += dpp<ROR4>(s1); s2 += dpp<ROR4>(s2);
    s1 += dpp<ROR2>(s1); s2 += dpp<ROR2>(s2); s1 += dpp<ROR1>(s1); s2 += dpp<ROR1>(s2);
    s1 = x16_sum(s1); s2 = x16_sum(s2);
    const float dm = s1 * (1.f / 64.f);
    if (lane == 0) *(LAS f32x2*)(L + stw) = f32x2{sh + dm, fmaf(-dm, s1, s2)};
    bar();
    const f32x2 st = *(const LAS f32x2*)(L + str_);
    __builtin_amdgcn_sched_barrier(0);
    {
      const rsrc_t rd = mk_rsrc(p.wd, FF * D * 2);
      const unsigned v = (lane >> 3) * (D * 2) + (lane & 7) * 16, s = f * 64 * (D * 2) + wave * 128;
#pragma unroll
      for (int t = DN_FIRST; t < DN_SECOND; ++t) wd[t] = ldb(rd, v, s + t * 8 * (D * 2));
      __builtin_amdgcn_sched_barrier(0);
    }
    const float mf = row_sum(st[0]) * (1.f / 9.f);
    const float dv = st[0] - mf;
    const float m2 = row_sum(fmaf(on * dv, dv, st[1]));
    *(LAS unsigned*)(L + h2w) = ln_apply_pk(xv, rbf(mf), ln_rden(m2 * (1.f / D)), c1b, sb);
  }
  bar();
  STAMP(8);
  v4u h0, h1;
  unsigned h8;
  {
    const LAS v4u* hp = (const LAS v4u*)(L + h2r);
    h0 = hp[0]; h1 = hp[1]; h8 = ((const LAS unsigned*)hp)[8];
  }
  __builtin_amdgcn_sched_barrier(0);
  if (DN_SECOND < 8) {
    const rsrc_t rd = mk_rsrc(p.wd, FF * D * 2);
    const unsigned v = (lane >> 3) * (D * 2) + (lane & 7) * 16, s = f * 64 * (D * 2) + wave * 128;
#pragma unroll
    for (int t = DN_SECOND; t < 8; ++t) wd[t] = ldb(rd, v, s + t * 8 * (D * 2));
    __builtin_amdgcn_sched_barrier(0);
  }
  if (wave < 8) {
    PIN(h0); PIN(h1); PIN(h8);
    f32x4 dg = {0.f, 0.f, 0.f, 0.f}, du = {0.f, 0.f, 0.f, 0.f};
#pragma unroll
    for (int t = 0; t < 9; ++t) {
      const v4u a = afrag(t < 4 ? h0[t & 3] : t < 8 ? h1[t & 3] : h8, mk);
      dg = mfma(a, wg[t], dg);
      du = mfma(a, wu[t], du);
    }
    *(LAS f32x4*)(L + guw0) = dg;
    *(LAS f32x4*)(L + guw0 + ((lane >> 5) == ((lane >> 3) & 1) ? 512 : 0)) = du;
  }
  bar();
  if (wave < 4) {
    const LAS float* r0 = (const LAS float*)(L + gur);
    float g = faddn(fadd(r0[0], r0[64]), fadd(r0[260], r0[324])), u = faddn(fadd(r0[128], r0[192]), fadd(r0[388], r0[452]));
    g += dpp<XOR1>(g); u += dpp<XOR1>(u);
    g += dpp<XOR2>(g); u += dpp<XOR2>(u);
    g = rbf(g); u = rbf(u);
    const float y = g * fmaf(g * g, 0.044715f * 0.7978845608028654f * 2.f * LOG2E, 0.7978845608028654f * 2.f * LOG2E);
    const float th = fmaf(-2.f, rcp_f(__builtin_amdgcn_exp2f(y) + 1.f), 1.f);
    const float act = rbf(0.5f * g * (1.f + th)) * u;
    *(LAS unsigned*)(L + actw) = pkr(act);
  }
  bar();
  STAMP(9);
  {
    const LAS v4u* ap = (const LAS v4u*)(L + actr);
    const v4u a0 = ap[0], a1 = ap[1];
    f32x4 d0 = {0.f, 0.f, 0.f, 0.f}, d1 = {0.f, 0.f, 0.f, 0.f};
#pragma unroll
    for (int t = 0; t < 4; ++t) { d0 = mfma(afrag(a0[t], mk), wd[t], d0); d1 = mfma(afrag(a1[t], mk), wd[4 + t], d1); }
    *(LAS f32x4*)(L + trw) = d0 + d1;
    lds_wait();
    pubp(ddst, fadd(*(const LAS float*)(L + trr), *(const LAS float*)(L + trr + 256)), tag);
  }
  STAMP(10);
}

DEV void role_modb(const Params& p, LAS unsigned char* L, int x, int wave, int lane, u64 t0, u64 code0) {
  if (CODE_PF && wave == 8) {
    const unsigned dump = (unsigned)(uintptr_t)(L + L_PF);
#pragma unroll
    for (int k = 0; k < (CODE_PF + 8191) / 8192; ++k) {
      const unsigned o = k * 8192 + lane * 128;
      asm volatile("s_mov_b32 m0, %0\n\ts_nop 0\n\tglobal_load_lds_dword %1, off" :: "s"(dump), "v"(code0 + (o < CODE_PF - 128 ? o : CODE_PF - 128)) : "m0", "memory");
    }
  }
  const Mask mk = make_mask(lane, true);
  if (wave == 0) tag_put(L, load_state(p.ws), lane);
  bar();
  const Tag tag = tag_get(L);
  gate_wait(t0, GATE_MODB);
  for (int k = wave; k < 12; k += 9) {
    ModA ma;
    moda_load(p, 120 + x + 8 * k, lane, ma);
    __builtin_amdgcn_sched_barrier(0);
    moda_finish(L, p.ws, 120 + x + 8 * k, lane, ma, mk, (LAS float*)(L + wave * 1024), tag);
  }
  STAMP(2);
}

__global__ void __launch_bounds__(NTHR) adaln_block_kernel(Params p) {
  __shared__ __attribute__((aligned(16))) unsigned char smem[L_END + L_EXTRA];
  LAS unsigned char* L = (LAS unsigned char*)smem;
  const int lane = threadIdx.x & 63, wave = __builtin_amdgcn_readfirstlane(threadIdx.x >> 6);
  u64 code0;
  asm volatile("s_getpc_b64 %0" : "=s"(code0));
  code0 &= ~0xFFull;
  const u64 t0 = __builtin_amdgcn_s_memrealtime();
  STAMP_INIT();
  STAMP(0);
  const int x = blockIdx.x & 7, idx = blockIdx.x >> 3;
  if (idx < 22) role_att(p, L, x, idx, wave, lane, t0);
  else if (idx < 31) role_ffn(p, L, x, idx - 22, wave, lane, t0);
  else role_modb(p, L, x, wave, lane, t0, code0);
  STAMP_DUMP();
}

extern "C" void kernel_launch(void* const* d_in, const int* in_sizes, int n_in, void* d_out, int out_size, void* d_ws,
                              size_t ws_size, hipStream_t stream) {
  Params p;
  p.x = (const u16*)d_in[0];
  p.kc = (const u16*)d_in[1];
  p.vc = (const u16*)d_in[2];
  p.wq = (const u16*)d_in[3];
  p.wo = (const u16*)d_in[4];
  p.wg = (const u16*)d_in[6];
  p.wu = (const u16*)d_in[7];
  p.wd = (const u16*)d_in[8];
  p.c = (const u16*)d_in[9];
  p.wmod = (const u16*)d_in[10];
  p.out = (u16*)d_out;
  p.ws = (u64*)d_ws;
  hipLaunchKernelGGL(adaln_block_kernel, dim3(256), dim3(NTHR), 0, stream, p);
}
```

```cpp
#include <hip/hip_runtime.h>
#include <stdint.h>

typedef unsigned short u16;
typedef unsigned long long u64;
typedef unsigned v4u __attribute__((ext_vector_type(4)));
typedef float f32x4 __attribute__((ext_vector_type(4)));
typedef float f32x2 __attribute__((ext_vector_type(2)));
typedef __bf16 bf16x2 __attribute__((ext_vector_type(2)));
typedef short bf16x8 __attribute__((ext_vector_type(8)));
typedef __amdgpu_buffer_rsrc_t rsrc_t;
#define GAS __attribute__((address_space(1)))
#define LAS __attribute__((address_space(3)))
#define DEV __device__ __forceinline__
typedef GAS u64 gu64;
#define RLX_AGENT __ATOMIC_RELAXED, __HIP_MEMORY_SCOPE_AGENT

constexpr int D = 576, SK = 65536, WIN = 4096, DH = 128, FF = 4608, NMOD = 6 * D;
constexpr int NSPLIT = 11;
constexpr int NTHR = 576;
constexpr int SPIN_CAP = 1 << 16;
#define GATE_FFN 790
#define GATE_MODB 200
#define GATE_ATT 140
#define GATE_Q 500
#define GATE_FFN2 0
#define F_N1 0
#define F_D1 2
#define GATE_X2 0
#define POLL_SLEEP 0
#define Q_STAGGER 16
#define GATE_H1 260
#define DN_FIRST 0
#define MODA_HOLD 0
#define GATE_MODB2 120
#define CODE_PF 21504
#define GATE_DN 0
#define DN_SECOND 4
#define PAGE_TOUCH 1
#define V_N1 0
#define V_D1 2
#define V_D2 24
#define ATT_N1 5
#define ATT_D1 2
#define ATT_D2 24

constexpr int G_EPOCH = 0;
constexpr int G_MOD = 64;
constexpr int G_Q = G_MOD + 4 * NMOD;
constexpr int G_O = G_Q + 32 * 2 * 128;
constexpr int G_WO = G_O + 16 * 8 * NSPLIT * 64;
constexpr int G_X2 = G_WO + 128 * 576;
constexpr int G_DN = G_X2 + 72 * 16;
constexpr int G_END = G_DN + 72 * 576;
constexpr size_t G_STAMP = 1 << 20;

constexpr int L_K = 0;
constexpr int L_OL = 98304;
constexpr int L_Q = L_OL + 9216;
constexpr int L_MX = L_Q + 512;
constexpr int L_LS = L_MX + 64;
constexpr int L_OB = L_LS + 64;
constexpr int L_PF = L_OB + 128;
constexpr int L_WO = L_PF + 256;
constexpr int L_MA = L_WO + 36864;
constexpr int L_CNT = L_MA + 1536;
constexpr int L_TAG = L_MA + 1536 + 16;
constexpr int L_END = L_TAG + 16;
constexpr int F_RED = 0;
constexpr int F_H = 16384;
constexpr int F_H2 = 17664;
constexpr int F_ACT = 20736;
constexpr int F_TR = 21248;
constexpr int F_ST = 27136;

struct Params {
  const u16 *x, *kc, *vc, *wq, *wo, *wg, *wu, *wd, *c, *wmod;
  u16* out;
  u64* ws;
};

#define STAMP(i) do {} while (0)
#define STAMP_INIT() do {} while (0)
#define STAMP_DUMP() do {} while (0)
#define L_EXTRA 0

DEV float bf2f(unsigned b) { return __uint_as_float(b << 16); }
DEV unsigned cvt2(float lo, float hi) {
  const f32x2 v = {lo, hi};
  return __builtin_bit_cast(unsigned, __builtin_convertvector(v, bf16x2));
}
DEV unsigned pkr(float f) { return cvt2(f, f); }
DEV float rbf(float f) { return __uint_as_float(cvt2(f, f) & 0xFFFF0000u); }
#define TAG_LO_BITS 4
constexpr unsigned TAGM = (1u << TAG_LO_BITS) - 1u;
struct Tag { unsigned hi, lo, rest; };
DEV Tag make_tag(u64 st) {
  const u64 nx = st * 6364136223846793005ull + 1442695040888963407ull;
  Tag t;
  t.hi = __builtin_amdgcn_readfirstlane((unsigned)(nx >> 32));
  t.rest = __builtin_amdgcn_readfirstlane((unsigned)nx);
  t.lo = t.rest >> (32 - TAG_LO_BITS);
  return t;
}
DEV u64 enc(float v, unsigned hi, unsigned lo) {
  const unsigned u = (__float_as_uint(v) + (TAGM + 1) / 2) & ~TAGM;
  return ((u64)hi << 32) | (u64)(u | lo);
}
DEV Tag tag_put(LAS unsigned char* L, u64 st, int lane) {
  const Tag t = make_tag(st);
  if (lane == 0) *(LAS v4u*)(L + L_TAG) = v4u{t.hi, t.lo, t.rest, 0u};
  return t;
}
DEV Tag tag_get(const LAS unsigned char* L) {
  const v4u v = *(const LAS v4u*)(L + L_TAG);
  Tag t; t.hi = __builtin_amdgcn_readfirstlane(v[0]); t.lo = __builtin_amdgcn_readfirstlane(v[1]); t.rest = __builtin_amdgcn_readfirstlane(v[2]);
  return t;
}
DEV unsigned or_xor(unsigned a, unsigned b, unsigned c) { return __builtin_amdgcn_bitop3_b32(a, b, c, 0xF6); }
struct Chk {
  unsigned a = 0, b = 0;
  DEV void add(u64 g, const Tag& t) { a = or_xor(a, (unsigned)(g >> 32), t.hi); b = or_xor(b, (unsigned)g, t.lo); }
  DEV bool all_ok() const { return __builtin_amdgcn_ballot_w64((a | (b & TAGM)) != 0u) == 0ull; }
};
DEV float gval(u64 g) { return __uint_as_float((unsigned)g & ~TAGM); }
DEV void pub(u64* ws, int idx, float v, const Tag& t) { __hip_atomic_store((gu64*)ws + idx, enc(v, t.hi, t.lo), RLX_AGENT); }
DEV void pubp(gu64* dst, float v, const Tag& t) { __hip_atomic_store(dst, enc(v, t.hi, t.lo), RLX_AGENT); }
DEV u64 peek(u64* ws, int idx) { return __hip_atomic_load((gu64*)ws + idx, RLX_AGENT); }
template <int N, class F> DEV void gather(u64* ws, const Tag& t, u64 (&g)[N], F idx) {
  for (int spin = 0; spin < SPIN_CAP; ++spin) {
#pragma unroll
    for (int k = 0; k < N; ++k) g[k] = peek(ws, idx(k));
    Chk c;
#pragma unroll
    for (int k = 0; k < N; ++k) c.add(g[k], t);
    if (c.all_ok()) break;
    if (POLL_SLEEP) __builtin_amdgcn_s_sleep(POLL_SLEEP);
  }
}

struct G2 { v4u v; DEV float lo() const { return __uint_as_float(v[0] & ~TAGM); } DEV float hi() const { return __uint_as_float(v[2] & ~TAGM); } };
template <int N, class F> DEV void gather2(rsrc_t rw, const Tag& t, G2 (&g)[N], F idx) {
  for (int spin = 0; spin < SPIN_CAP; ++spin) {
    asm volatile("" ::: "memory");
#pragma unroll
    for (int k = 0; k < N; ++k) g[k].v = __builtin_amdgcn_raw_buffer_load_b128(rw, idx(k) * 8, 0, 16);
    unsigned a = 0, b = 0;
#pragma unroll
    for (int k = 0; k < N; ++k) { a = or_xor(or_xor(a, g[k].v[1], t.hi), g[k].v[3], t.hi); b = or_xor(or_xor(b, g[k].v[0], t.lo), g[k].v[2], t.lo); }
    if (__builtin_amdgcn_ballot_w64((a | (b & TAGM)) != 0u) == 0ull) break;
    if (POLL_SLEEP) __builtin_amdgcn_s_sleep(POLL_SLEEP);
  }
}
template <int N, class F> DEV void pretouch(LAS unsigned char* L, u64* ws, F idx) {
  const unsigned dump = (unsigned)(uintptr_t)(L + L_PF);
#pragma unroll
  for (int k = 0; k < N; ++k)
    asm volatile("s_mov_b32 m0, %0\n\ts_nop 0\n\tglobal_load_lds_dword %1, off sc1" :: "s"(dump), "v"((gu64*)ws + idx(k)) : "m0", "memory");
}
DEV void page_touch(LAS unsigned char* L, const void* a) {
  asm volatile("s_mov_b32 m0, %0\n\ts_nop 0\n\tglobal_load_lds_dword %1, off nt" :: "s"((unsigned)(uintptr_t)(L + L_PF)), "v"((const GAS char*)a) : "m0", "memory");
}
DEV f32x4 mfma(v4u a, v4u b, f32x4 c) {
  return __builtin_amdgcn_mfma_f32_16x16x32_bf16(__builtin_bit_cast(bf16x8, a), __builtin_bit_cast(bf16x8, b), c, 0, 0, 0);
}
DEV rsrc_t mk_rsrc(const void* p, unsigned bytes) { return __builtin_amdgcn_make_buffer_rsrc((void*)p, 0, (int)bytes, 0x00020000); }
DEV v4u ldb(rsrc_t r, unsigned voff, unsigned soff) { return __builtin_amdgcn_raw_buffer_load_b128(r, (int)voff, (int)soff, 2); }
#define SMALL_AUX 0
DEV unsigned ldb16(rsrc_t r, unsigned voff) { return __builtin_amdgcn_raw_buffer_load_b16(r, (int)voff, 0, SMALL_AUX); }
#define E_AUX 16
DEV u64 load_state(u64* ws) {
  int zoff = G_EPOCH * 8;
  asm volatile("" : "+v"(zoff));
  typedef unsigned v2u_ __attribute__((ext_vector_type(2)));
  const v2u_ v = __builtin_amdgcn_raw_buffer_load_b64(mk_rsrc(ws, 64), zoff, 0, E_AUX);
  return ((u64)v[1] << 32) | v[0];
}
DEV void bar() {
  asm volatile("s_waitcnt lgkmcnt(0)" ::: "memory");
  __builtin_amdgcn_s_barrier();
  asm volatile("" ::: "memory");
}
#define PIN(x) asm volatile("" : "+v"(x))
#define PINS(x) asm volatile("" : "+s"(x))
DEV void lds_wait() { asm volatile("s_waitcnt lgkmcnt(0)" ::: "memory"); }
template <int CTRL> DEV float dpp(float x) {
  return __builtin_bit_cast(float, __builtin_amdgcn_mov_dpp(__builtin_bit_cast(int, x), CTRL, 0xf, 0xf, true));
}
constexpr int ROR8 = 0x128, ROR4 = 0x124, ROR2 = 0x122, ROR1 = 0x121, XOR1 = 0xB1, XOR2 = 0x4E;
DEV float fmx(float a, float b) { float r; asm("v_max_f32 %0, %1, %2\n\ts_nop 1" : "=v"(r) : "v"(a), "v"(b)); return r; }
DEV float fmx3(float a, float b, float c) { float r; asm("v_max3_f32 %0, %1, %2, %3" : "=v"(r) : "v"(a), "v"(b), "v"(c)); return r; }
DEV float fmx3n(float a, float b, float c) { float r; asm("v_max3_f32 %0, %1, %2, %3\n\ts_nop 1" : "=v"(r) : "v"(a), "v"(b), "v"(c)); return r; }
DEV float x16_sum(float x) {
  auto s = __builtin_amdgcn_permlane16_swap(__float_as_uint(x), __float_as_uint(x), false, false);
  x = __uint_as_float(s[0]) + __uint_as_float(s[1]);
  auto t = __builtin_amdgcn_permlane32_swap(__float_as_uint(x), __float_as_uint(x), false, false);
  return __uint_as_float(t[0]) + __uint_as_float(t[1]);
}
DEV float x16_max(float x) {
  auto s = __builtin_amdgcn_permlane16_swap(__float_as_uint(x), __float_as_uint(x), false, false);
  x = fmaxf(__uint_as_float(s[0]), __uint_as_float(s[1]));
  auto t = __builtin_amdgcn_permlane32_swap(__float_as_uint(x), __float_as_uint(x), false, false);
  return fmaxf(__uint_as_float(t[0]), __uint_as_float(t[1]));
}
DEV float row_sum(float x) { x += dpp<ROR8>(x); x += dpp<ROR4>(x); x += dpp<ROR2>(x); x += dpp<ROR1>(x); return x; }
DEV float row_max(float x) { x = fmaxf(x, dpp<ROR8>(x)); x = fmaxf(x, dpp<ROR4>(x)); x = fmaxf(x, dpp<ROR2>(x)); x = fmaxf(x, dpp<ROR1>(x)); return x; }
DEV float wave_sum(float x) { return x16_sum(row_sum(x)); }
constexpr float LOG2E = 1.4426950408889634f;
DEV float fadd(float a, float b) { float r; asm("v_add_f32 %0, %1, %2" : "=v"(r) : "v"(a), "v"(b)); return r; }
DEV float faddn(float a, float b) { float r; asm("v_add_f32 %0, %1, %2\n\ts_nop 1" : "=v"(r) : "v"(a), "v"(b)); return r; }
DEV float exp_f(float x) { return __builtin_amdgcn_exp2f(x * LOG2E); }
DEV float rcp_f(float x) { return __builtin_amdgcn_rcpf(x); }
DEV float pair_pick(float a, float b, int lane) {
  const float a2 = dpp<0x50>(a), b2 = dpp<0x50>(b);
  const float a3 = dpp<0xFA>(a), b3 = dpp<0xFA>(b);
  const float lo = (lane & 1) ? b2 : a2, hi = (lane & 1) ? b3 : a3;
  return (lane & 4) ? hi : lo;
}
DEV float rdlane(float x, int l) { return __uint_as_float(__builtin_amdgcn_readlane(__float_as_uint(x), l)); }
DEV void gate_wait(u64 t0, unsigned ticks) { while (__builtin_amdgcn_s_memrealtime() - t0 < (u64)ticks) __builtin_amdgcn_s_sleep(2); }

struct Mask { unsigned m[4]; };
DEV Mask make_mask(int lane, bool on) {
  Mask k;
  const unsigned h = on ? ((lane & 1) ? 0xFFFF0000u : 0x0000FFFFu) : 0u;
#pragma unroll
  for (int r = 0; r < 4; ++r) k.m[r] = (((lane & 7) >> 1) == r) ? h : 0u;
  return k;
}
DEV v4u afrag(unsigned pp, const Mask& k) { return v4u{pp & k.m[0], pp & k.m[1], pp & k.m[2], pp & k.m[3]}; }

DEV unsigned ln_apply_pk(float x, float mean_b, float rden, float c1, float shift) {
  const float ln = rbf(rbf(x - mean_b) * rden);
  return pkr(rbf(ln * c1) + shift);
}
DEV float ln_rden(float var) { return rcp_f(rbf(__builtin_amdgcn_sqrtf(rbf(rbf(var) + 1e-5f)))); }

DEV void red_store(LAS float* area, f32x4 d, int lane) {
  const int u = (lane >> 3) & 1;
  if ((lane >> 5) == u) *(LAS f32x4*)(area + u * 64 + 8 * (lane & 7) + 4 * ((lane >> 4) & 1)) = d;
}
DEV unsigned red_off(int lane, int dump) {
  const int u = (lane >> 3) & 1;
  return 4 * ((lane >> 5) == u ? u * 64 + 8 * (lane & 7) + 4 * ((lane >> 4) & 1) : dump);
}
DEV unsigned silu_pk(unsigned craw) { const float v = bf2f(craw); return pkr(v * rcp_f(1.f + exp_f(-v))); }

struct WoAddr { unsigned tro, tri, obo, wro; gu64* dst; };
DEV WoAddr wo_prep(u64* ws, int pidx, int wave, int lane) {
  WoAddr a;
  a.tro = L_OL + wave * 640 + red_off(lane, 128); a.tri = L_OL + wave * 640 + lane * 4; a.obo = L_OB + (lane >> 3) * 16; a.wro = L_WO + wave * 4096 + lane * 16;
  a.dst = (gu64*)ws + G_WO + pidx * 576 + wave * 64 + lane;
  PIN(a.tro); PIN(a.tri); PIN(a.obo); PIN(a.wro); PIN(a.dst);
  return a;
}
DEV void wo_phase(LAS unsigned char* L, const WoAddr& a, const Tag& tag, const Mask& mk) {
  bar();
  v4u ob = *(const LAS v4u*)(L + a.obo);
  const LAS v4u* wr = (const LAS v4u*)(L + a.wro);
  v4u w0 = wr[0], w1 = wr[64], w2 = wr[128], w3 = wr[192];
  PIN(ob); PIN(w0); PIN(w1); PIN(w2); PIN(w3);
  __builtin_amdgcn_sched_barrier(0);
  f32x4 d0 = {0.f, 0.f, 0.f, 0.f}, d1 = {0.f, 0.f, 0.f, 0.f};
  d0 = mfma(afrag(ob[0], mk), w0, d0); d1 = mfma(afrag(ob[1], mk), w1, d1);
  d0 = mfma(afrag(ob[2], mk), w2, d0); d1 = mfma(afrag(ob[3], mk), w3, d1);
  *(LAS f32x4*)(L + a.tro) = d0 + d1;
  lds_wait();
  pubp(a.dst, fadd(*(const LAS float*)(L + a.tri), *(const LAS float*)(L + a.tri + 256)), tag);
}

struct ModA { v4u w[18]; unsigned c[3]; };
DEV void moda_load(const Params& p, int mt, int lane, ModA& m) {
  const rsrc_t rm = mk_rsrc(p.wmod, D * NMOD * 2), rc = mk_rsrc(p.c, D * 2);
  const int r0 = (mt & 3) * 144;
#pragma unroll
  for (int k = 0; k < 3; ++k) m.c[k] = ldb16(rc, (r0 + (k < 2 || lane < 16 ? lane + 64 * k : 143)) * 2);
  const unsigned v = (r0 + (lane >> 3)) * (NMOD * 2) + (lane & 7) * 16;
#pragma unroll
  for (int t = 0; t < 18; ++t) m.w[t] = ldb(rm, v, (mt >> 2) * 128 + t * 8 * (NMOD * 2));
}
DEV void moda_finish(LAS unsigned char* L, u64* ws, int mt, int lane, ModA& m, const Mask& mk, LAS float* tr, const Tag& tag) {
  unsigned sp[18];
  {
    const unsigned s0 = silu_pk(m.c[0]), s1 = silu_pk(m.c[1]), s2 = silu_pk(m.c[2]);
#pragma unroll
    for (int t = 0; t < 18; ++t) sp[t] = __builtin_amdgcn_ds_bpermute(((8 * t + (lane >> 3)) & 63) * 4, t < 8 ? s0 : t < 16 ? s1 : s2);
  }
  gu64* dst = (gu64*)ws + G_MOD + (mt & 3) * NMOD + (mt >> 2) * 64 + lane;
  unsigned tro = red_off(lane, 128);
  PIN(dst); PIN(tro);
#pragma unroll
  for (int t = 0; t < 18; ++t) PIN(sp[t]);
  __builtin_amdgcn_sched_barrier(0);
  f32x4 d0 = {0.f, 0.f, 0.f, 0.f}, d1 = {0.f, 0.f, 0.f, 0.f};
#pragma unroll
  for (int t = 0; t < 18; t += 2) {
    d0 = mfma(afrag(sp[t], mk), m.w[t], d0);
    __builtin_amdgcn_sched_barrier(0);
    d1 = mfma(afrag(sp[t + 1], mk), m.w[t + 1], d1);
    __builtin_amdgcn_sched_barrier(0);
  }
  *(LAS f32x4*)((LAS unsigned char*)tr + tro) = d0 + d1;
  lds_wait();
  pubp(dst, fadd(tr[lane], tr[64 + lane]), tag);
}

DEV void role_att(const Params& p, LAS unsigned char* L, int x, int i, int wave, int lane, u64 t0) {
  const int g = 2 * x + (i >= NSPLIT ? 1 : 0), j = i >= NSPLIT ? i - NSPLIT : i;
  const bool odd = g & 1;
  const int nm = odd ? 4 : 5;
  const bool moda = j < nm;
  const int ntile = !moda ? 24 : (!odd && j >= 3) ? 23 : 22;
  const int tile0 = !moda ? (odd ? 88 : 112) + 24 * (j - nm) : (!odd && j == 4) ? 89 : 22 * j;
  const bool owner = j < 8;
  int redc = -1;
  if (j >= 8) redc = 9 * x + (g & 1) * 3 + (j - 8);
  else if (j == 0) redc = 9 * x + 6 + (g & 1);
  else if (j == 1 && (g & 1) == 0) redc = 9 * x + 8;
  u64* ws = p.ws;
  const Mask mk = make_mask(lane, true);
  const int kap = lane >> 4, cc = lane & 15;

  if (wave < 8) {
    const size_t hb = ((size_t)g * SK + (SK - WIN)) * DH;
    const rsrc_t rk = mk_rsrc(p.kc + hb, WIN * DH * 2), rv = mk_rsrc(p.vc + hb, WIN * DH * 2);
    const bool three = wave + 16 < ntile;
    const unsigned tso = (tile0 + wave) * 4096;
    const unsigned ldsk = (unsigned)(uintptr_t)(L + L_K) + wave * 3 * 4096;
    unsigned vk[4];
#pragma unroll
    for (int q = 0; q < 4; ++q) vk[q] = (4 * q + kap) * 256 + ((cc ^ (4 * q + kap)) << 4);
#define KDMA(s, q) asm volatile("s_mov_b32 m0, %0\n\ts_nop 0\n\tbuffer_load_dwordx4 %1, %2, %3 offen nt lds" \
                     :: "s"(ldsk + (s) * 4096 + (q) * 1024), "v"(vk[q]), "s"(rk), "s"(tso + (s) * 32768) : "m0", "memory")
#define THR(n) do { if ((n) < ATT_N1) { if ((n) >= ATT_D1 && moda) asm volatile("s_waitcnt vmcnt(%0)" :: "n"(ATT_D1 - 1) : "memory"); } \
                     else if ((n) >= ATT_D2) asm volatile("s_waitcnt vmcnt(%0)" :: "n"(ATT_D2 - 1) : "memory"); } while (0)
#define THR_DEPTH(n) ((n) < ATT_N1 ? ATT_D1 : ATT_D2)
#define THRV(t) asm volatile("s_waitcnt vmcnt(%0)" :: "n"(((t) < V_N1 ? V_D1 : V_D2) - 1) : "memory")
    if (moda) __builtin_amdgcn_s_barrier();
    __builtin_amdgcn_s_setprio(2);
    KDMA(0, 0);
    if (GATE_ATT) gate_wait(t0, GATE_ATT);
    if (MODA_HOLD && moda) __builtin_amdgcn_s_barrier();
    THR(1); KDMA(0, 1); THR(2); KDMA(0, 2); THR(3); KDMA(0, 3);
    THR(4); KDMA(1, 0); THR(5); KDMA(1, 1); THR(6); KDMA(1, 2); THR(7); KDMA(1, 3);
    if (three) { THR(8); KDMA(2, 0); THR(9); KDMA(2, 1); THR(10); KDMA(2, 2); THR(11); KDMA(2, 3); }
#undef KDMA
    __builtin_amdgcn_s_setprio(0);
    v4u vr[12];
    const unsigned vv = kap * 1024 + cc * 16;
#pragma unroll
    for (int t = 0; t < 8; ++t) { THRV(t); vr[t] = ldb(rv, vv + (t & 3) * 256, tso + (t >> 2) * 32768); }
    if (three) {
#pragma unroll
      for (int t = 8; t < 12; ++t) { THRV(t); vr[t] = ldb(rv, vv + (t & 3) * 256, tso + 65536); }
    } else {
#pragma unroll
      for (int t = 8; t < 12; ++t) vr[t] = v4u{0u, 0u, 0u, 0u};
    }
#undef THR
#undef THRV
#undef THR_DEPTH
    __builtin_amdgcn_sched_barrier(0);
    STAMP(1);
    if (three) asm volatile("s_waitcnt vmcnt(12)" ::: "memory");
    else asm volatile("s_waitcnt vmcnt(8)" ::: "memory");
    v4u kf[12];
#pragma unroll
    for (int s = 0; s < 3; ++s)
#pragma unroll
      for (int ks = 0; ks < 4; ++ks)
        kf[s * 4 + ks] = *(const LAS v4u*)(L + L_K + (wave * 3 + (s < 2 || three ? s : 0)) * 4096 + cc * 256 + (((4 * ks + kap) ^ cc) << 4));
#pragma unroll
    for (int t = 0; t < 12; ++t) PIN(kf[t]);
    const int head = (lane >> 3) & 1;
    const int me = (wave & 3) * 64 + lane;
    unsigned qsrc = L_Q + head * 256 + kap * 16, mxw = L_MX + (head * 8 + wave) * 4, mxr = L_MX + head * 32, olw = L_OL + wave * 1024 + lane * 16;
    unsigned msrc = L_OL + ((16 * (((me >> 7) * 8 + (me & 7)) >> 2) + ((me & 127) >> 3)) * 4 + (me & 3)) * 4;
    gu64* mdst = (gu64*)ws + G_O + ((g * 8 + (me >> 5)) * NSPLIT + j) * 64 + (me & 31);
    bool lead = (lane & 0x37) == 0;
    PIN(qsrc); PIN(mxw); PIN(mxr); PIN(olw); PIN(msrc); PIN(mdst);
    STAMP(3);
    bar();
    STAMP(2);
    v4u qf[4];
#pragma unroll
    for (int ks = 0; ks < 4; ++ks) qf[ks] = *(const LAS v4u*)(L + qsrc + ks * 64);
#pragma unroll
    for (int ks = 0; ks < 4; ++ks) PIN(qf[ks]);
    __builtin_amdgcn_sched_barrier(0);
    f32x4 acc[3] = {{0.f, 0.f, 0.f, 0.f}, {0.f, 0.f, 0.f, 0.f}, {0.f, 0.f, 0.f, 0.f}};
#pragma unroll
    for (int ks = 0; ks < 4; ++ks)
#pragma unroll
      for (int s = 0; s < 3; ++s) acc[s] = mfma(kf[s * 4 + ks], qf[ks], acc[s]);
    __builtin_amdgcn_sched_barrier(0);
    float sc[12];
#pragma unroll
    for (int t = 0; t < 12; t += 2) {
      const unsigned u = cvt2(acc[t >> 2][t & 3], acc[t >> 2][(t & 3) + 1]);
      const unsigned w = cvt2(__uint_as_float(u << 16) * 0.08838834764831845f, __uint_as_float(u & 0xFFFF0000u) * 0.08838834764831845f);
      sc[t] = __uint_as_float(w << 16);
      sc[t + 1] = __uint_as_float(w & 0xFFFF0000u);
    }
    if (!three) {
#pragma unroll
      for (int t = 8; t < 12; ++t) sc[t] = -INFINITY;
    }
    float mx = fmx3n(fmx3(fmx3(sc[0], sc[1], sc[2]), fmx3(sc[3], sc[4], sc[5]), fmx3(sc[6], sc[7], sc[8])), fmx3(sc[9], sc[10], sc[11]), sc[0]);
    mx = x16_max(mx);
    if (lead) *(LAS float*)(L + mxw) = mx;
    lds_wait();
    if (lane == 0) __hip_atomic_fetch_add((LAS unsigned*)(L + L_CNT), 1u, __ATOMIC_RELAXED, __HIP_MEMORY_SCOPE_WORKGROUP);
    while (*(volatile LAS unsigned*)(L + L_CNT) < 8u) {}
    asm volatile("" ::: "memory");
    float m;
    {
      const f32x4 a = *(const LAS f32x4*)(L + mxr), b = *(const LAS f32x4*)(L + mxr + 16);
      m = fmx3(fmx3(a[0], a[1], a[2]), fmx3(a[3], b[0], b[1]), fmx(b[2], b[3]));
    }
    v4u af[12];
    float ls0 = 0.f, ls1 = 0.f;
    const float mneg = -m * LOG2E;
#pragma unroll
    for (int t = 0; t < 12; t += 2) {
      const float e0 = __builtin_amdgcn_exp2f(fmaf(sc[t], LOG2E, mneg)), e1 = __builtin_amdgcn_exp2f(fmaf(sc[t + 1], LOG2E, mneg));
      ls0 += e0; ls1 += e1;
      af[t] = afrag(pkr(e0), mk); af[t + 1] = afrag(pkr(e1), mk);
    }
    const float ls = x16_sum(ls0 + ls1);
    if (lead) *(LAS float*)(L + mxw + 64) = ls;
    const Tag tag = tag_get(L);
    __builtin_amdgcn_sched_barrier(0);
    STAMP(4);
    f32x4 o0 = {0.f, 0.f, 0.f, 0.f}, o1 = {0.f, 0.f, 0.f, 0.f};
#pragma unroll
    for (int t = 0; t < 12; t += 2) { o0 = mfma(af[t], vr[t], o0); o1 = mfma(af[t + 1], vr[t + 1], o1); }
    *(LAS f32x4*)(L + olw) = o0 + o1;
    STAMP(5);
    bar();
    if (wave < 4) {
      const LAS float* src = (const LAS float*)(L + msrc);
      float s[8];
#pragma unroll
      for (int w = 0; w < 8; ++w) s[w] = src[w * 256];
      pubp(mdst, fadd(fadd(fadd(s[0], s[1]), fadd(s[2], s[3])), fadd(fadd(s[4], s[5]), fadd(s[6], s[7]))), tag);
    } else if (wave == 4 && lane < 16) {
      const int u = lane >> 1, hh = u >> 2;
      const f32x4 a = *(const LAS f32x4*)(L + ((lane & 1) ? L_LS : L_MX) + hh * 32), b = *(const LAS f32x4*)(L + ((lane & 1) ? L_LS : L_MX) + hh * 32 + 16);
      const float vs = ((a[0] + a[1]) + (a[2] + a[3])) + ((b[0] + b[1]) + (b[2] + b[3]));
      const float vm = fmaxf(fmaxf(fmaxf(a[0], a[1]), fmaxf(a[2], a[3])), fmaxf(fmaxf(b[0], b[1]), fmaxf(b[2], b[3])));
      pub(ws, G_O + ((g * 8 + u) * NSPLIT + j) * 64 + 32 + (lane & 1), (lane & 1) ? vs : vm, tag);
    }
    STAMP(6);
    if (owner) {
      wo_phase(L, wo_prep(ws, g * 8 + j, wave, lane), tag, mk);
      STAMP(8);
    }
  } else {
    __builtin_amdgcn_s_setprio(3);
    Tag tag;
    {
      if (moda) {
        u64 st = load_state(ws);
        ModA ma;
        moda_load(p, 9 * x + (odd ? 5 : 0) + j, lane, ma);
        __builtin_amdgcn_sched_barrier(0);
        __builtin_amdgcn_s_barrier();
        asm volatile("" : "+v"(st));
        tag = tag_put(L, st, lane);
        moda_finish(L, ws, 9 * x + (odd ? 5 : 0) + j, lane, ma, mk, (LAS float*)(L + L_MA), tag);
        STAMP(9);
        if (MODA_HOLD) __builtin_amdgcn_s_barrier();
      } else {
        tag = tag_put(L, load_state(ws), lane);
      }
    }
    float xr = 0.f;
    if (redc >= 0) xr = bf2f(p.x[redc * 8 + (lane & 7)]);
    if (GATE_Q) gate_wait(t0, GATE_Q);
    const rsrc_t rw = mk_rsrc(ws, G_END * 8);
    {
      G2 gq[4];
      gather2<4>(rw, tag, gq, [&](int k) { return G_Q + ((2 * g + (k >> 1)) * 2 + (k & 1)) * 128 + 2 * lane; });
      ((LAS unsigned*)(L + L_Q))[lane] = cvt2(fadd(gq[0].lo(), gq[1].lo()), fadd(gq[0].hi(), gq[1].hi()));
      ((LAS unsigned*)(L + L_Q))[64 + lane] = cvt2(fadd(gq[2].lo(), gq[3].lo()), fadd(gq[2].hi(), gq[3].hi()));
    }
    STAMP(1);
    if (owner) {
      const int ob = G_O + (g * 8 + j) * NSPLIT * 64;
      pretouch<1>(L, ws, [&](int) { const int q_ = lane < 33 ? lane : 32; return ob + (q_ / 3) * 64 + (q_ % 3) * 16; });
    }
    if (redc >= 0) {
      pretouch<2>(L, ws, [&](int s) { return G_WO + (s * 64 + lane) * 576 + redc * 8; });
      pretouch<2>(L, ws, [&](int s) { const int f_ = s * 64 + lane; return G_DN + (f_ < 72 ? f_ : 71) * 576 + redc * 8; });
    }
    if (lane == 0) *(volatile LAS unsigned*)(L + L_CNT) = 0u;
    bar();
    if (owner) {
      const rsrc_t rwo = mk_rsrc(p.wo, 4096 * D * 2);
      const unsigned wo_v = (lane >> 3) * (D * 2) + (lane & 7) * 16;
      const unsigned wo_s = (g * 256 + j * 32) * (D * 2), ldsw = (unsigned)(uintptr_t)(L + L_WO);
#pragma unroll
      for (int w = 0; w < 9; ++w)
#pragma unroll
        for (int t = 0; t < 4; ++t)
          asm volatile("s_mov_b32 m0, %0\n\ts_nop 0\n\tbuffer_load_dwordx4 %1, %2, %3 offen nt lds"
                       :: "s"(ldsw + (w * 4 + t) * 1024), "v"(wo_v), "s"(rwo), "s"(wo_s + w * 128 + t * 8 * (D * 2)) : "m0", "memory");
    }
    bar();
    STAMP(2);
    if (owner) {
      const int ob = G_O + (g * 8 + j) * NSPLIT * 64;
      const int k2 = kap < 3 ? 8 + kap : 10;
      unsigned obw = L_OB + (((2 * cc) & 7) * 4 + (cc >> 2)) * 4;
      float live = kap < 3 ? 1.f : 0.f;
      PIN(obw); PIN(live);
      const WoAddr wa = wo_prep(ws, g * 8 + j, 8, lane);
      G2 go[6];
      gather2<6>(rw, tag, go, [&](int k) { const int sp = k % 3 == 0 ? kap : k % 3 == 1 ? 4 + kap : k2; return ob + sp * 64 + (k < 3 ? 2 * cc : 32); });
      STAMP(3);
      const float M = x16_max(fmaxf(fmaxf(go[3].lo(), go[4].lo()), go[5].lo()));
      const float Mn = -M * LOG2E;
      const float w0 = __builtin_amdgcn_exp2f(fmaf(go[3].lo(), LOG2E, Mn)), w1 = __builtin_amdgcn_exp2f(fmaf(go[4].lo(), LOG2E, Mn));
      const float w2 = __builtin_amdgcn_exp2f(fmaf(go[5].lo(), LOG2E, Mn)) * live;
      const float Ls = x16_sum(fmaf(w2, go[5].hi(), fmaf(w1, go[4].hi(), w0 * go[3].hi())));
      const float o0 = x16_sum(fmaf(w2, go[2].lo(), fmaf(w1, go[1].lo(), w0 * go[0].lo())));
      const float o1 = x16_sum(fmaf(w2, go[2].hi(), fmaf(w1, go[1].hi(), w0 * go[0].hi())));
      const float rl = rcp_f(Ls);
      *(LAS unsigned*)(L + obw) = pkr(o0 * rl);
      *(LAS unsigned*)(L + obw + 16) = pkr(o1 * rl);
      asm volatile("s_waitcnt vmcnt(0)" ::: "memory");
      wo_phase(L, wa, tag, mk);
      STAMP(4);
    }
    if (redc >= 0) {
      u64 gg[8];
      gather<8>(ws, tag, gg, [&](int k) { return G_MOD + (k & 3) * NMOD + (k < 4 ? 2 : 5) * D + redc * 8 + (lane & 7); });
      float ga = rbf(fadd(fadd(gval(gg[0]), gval(gg[1])), fadd(gval(gg[2]), gval(gg[3]))));
      float gb = rbf(fadd(fadd(gval(gg[4]), gval(gg[5])), fadd(gval(gg[6]), gval(gg[7]))));
      gu64* xdst = (gu64*)ws + G_X2 + redc * 16 + (lane & 7);
      GAS u16* odst = (GAS u16*)p.out + redc * 8 + (lane & 7);
      PIN(ga); PIN(gb); PIN(xr); PIN(xdst); PIN(odst);
      float x2;
      {
        G2 gw[8];
        gather2<8>(rw, tag, gw, [&](int s) { return G_WO + (s * 16 + (lane >> 2)) * 576 + redc * 8 + 2 * (lane & 3); });
        STAMP(5);
        float t0_ = faddn(fadd(fadd(gw[0].lo(), gw[1].lo()), fadd(gw[2].lo(), gw[3].lo())), fadd(fadd(gw[4].lo(), gw[5].lo()), fadd(gw[6].lo(), gw[7].lo())));
        float t1_ = faddn(fadd(fadd(gw[0].hi(), gw[1].hi()), fadd(gw[2].hi(), gw[3].hi())), fadd(fadd(gw[4].hi(), gw[5].hi()), fadd(gw[6].hi(), gw[7].hi())));
        t0_ += dpp<ROR8>(t0_); t1_ += dpp<ROR8>(t1_); t0_ += dpp<ROR4>(t0_); t1_ += dpp<ROR4>(t1_);
        t0_ = x16_sum(t0_); t1_ = x16_sum(t1_);
        const float ts = pair_pick(t0_, t1_, lane);
        x2 = rbf(xr + rbf(ga * rbf(ts)));
        if (lane < 8) pubp(xdst, x2, tag);
      }
      STAMP(6);
      if (PAGE_TOUCH & 2) page_touch(L, p.out);
      {
        G2 gd[5];
        float w4 = lane < 32 ? 1.f : 0.f;
        PIN(w4);
        gather2<5>(rw, tag, gd, [&](int s) { const int f_ = s * 16 + (lane >> 2); return G_DN + (f_ < 72 ? f_ : 71) * 576 + redc * 8 + 2 * (lane & 3); });
        STAMP(7);
        float t0_ = fmaf(w4, gd[4].lo(), fadd(fadd(gd[0].lo(), gd[1].lo()), fadd(gd[2].lo(), gd[3].lo())));
        float t1_ = fmaf(w4, gd[4].hi(), fadd(fadd(gd[0].hi(), gd[1].hi()), fadd(gd[2].hi(), gd[3].hi())));
        t0_ += dpp<ROR8>(t0_); t1_ += dpp<ROR8>(t1_); t0_ += dpp<ROR4>(t0_); t1_ += dpp<ROR4>(t1_);
        t0_ = x16_sum(t0_); t1_ = x16_sum(t1_);
        const float ts = pair_pick(t0_, t1_, lane);
        const unsigned o = pkr(x2 + rbf(gb * rbf(ts)));
        if (lane < 8) *odst = (u16)o;
        if (redc == 0 && lane == 0) __hip_atomic_store((gu64*)ws + G_EPOCH, ((u64)tag.hi << 32) | (u64)tag.rest, RLX_AGENT);
      }
      STAMP(8);
    }
  }
}

DEV void role_ffn(const Params& p, LAS unsigned char* L, int x, int i, int wave, int lane, u64 t0) {
  const int f = 9 * x + i;
  u64* ws = p.ws;
  const Mask mk = make_mask(lane, true);
  LAS float* red = (LAS float*)(L + F_RED);
  const rsrc_t rm = mk_rsrc(p.wmod, D * NMOD * 2), rc = mk_rsrc(p.c, D * 2), rx = mk_rsrc(p.x, D * 2);
  const int hq = 4 * x + (i >> 1), qr = i & 1;
  const rsrc_t rw = mk_rsrc(ws, G_END * 8);
  Tag tag;
  if (i < 8) {
    v4u wa[12];
    if (wave >= 3) {
      const rsrc_t rq = mk_rsrc(p.wq, D * 4096 * 2);
      const unsigned v = (lane >> 4) * 8192 + (lane & 15) * 16, s = (qr * 288 + (wave - 3) * 48) * 8192 + hq * 256;
#pragma unroll
      for (int t = 0; t < 12; ++t) wa[t] = ldb(rq, v, s + t * 4 * 8192);
      __builtin_amdgcn_sched_barrier(0);
      STAMP(1);
    } else {
      __builtin_amdgcn_s_setprio(3);
      const u64 st = load_state(ws);
      float xv[9];
#pragma unroll
      for (int k = 0; k < 9; ++k) xv[k] = bf2f(ldb16(rx, (lane + 64 * k) * 2));
      const int p0 = wave * 64 + lane, pr = p0 < 144 ? p0 : 143;
      const unsigned xo = __builtin_amdgcn_raw_buffer_load_b32(rx, (qr * 288 + 2 * pr) * 2, 0, 0);
      float s = 0.f;
#pragma unroll
      for (int k = 0; k < 9; ++k) s += xv[k];
      const float mf = wave_sum(s) * (1.f / D);
      float v = 0.f;
#pragma unroll
      for (int k = 0; k < 9; ++k) { const float d = xv[k] - mf; v = fmaf(d, d, v); }
      const float rden = ln_rden(wave_sum(v) * (1.f / D)), mean = rbf(mf);
      tag = wave == 0 ? tag_put(L, st, lane) : make_tag(st);
      const int hw = pr / 24, hr = 2 * pr - hw * 48;
      unsigned hdst = F_H + ((hw * 4 + (hr & 3)) * 12 + (hr >> 2)) * 4;
      float ln0 = rbf(rbf(__uint_as_float(xo << 16) - mean) * rden), ln1 = rbf(rbf(__uint_as_float(xo & 0xFFFF0000u) - mean) * rden);
      PIN(hdst); PIN(ln0); PIN(ln1);
      STAMP(2);
      if (GATE_H1) gate_wait(t0, GATE_H1);
      G2 ga[8];
      gather2<8>(rw, tag, ga, [&](int k) { return G_MOD + (k & 3) * NMOD + (k >> 2) * D + qr * 288 + 2 * pr; });
      STAMP(3);
      const float sa0 = rbf(fadd(fadd(ga[0].lo(), ga[1].lo()), fadd(ga[2].lo(), ga[3].lo()))), sa1 = rbf(fadd(fadd(ga[0].hi(), ga[1].hi()), fadd(ga[2].hi(), ga[3].hi())));
      const float ca0 = rbf(fadd(fadd(ga[4].lo(), ga[5].lo()), fadd(ga[6].lo(), ga[7].lo()))), ca1 = rbf(fadd(fadd(ga[4].hi(), ga[5].hi()), fadd(ga[6].hi(), ga[7].hi())));
      *(LAS unsigned*)(L + hdst) = pkr(rbf(ln0 * rbf(1.f + ca0)) + sa0);
      *(LAS unsigned*)(L + hdst + 48) = pkr(rbf(ln1 * rbf(1.f + ca1)) + sa1);
      __builtin_amdgcn_s_setprio(0);
    }
    Mask mq = make_mask(lane, (lane & 8) == 0);
    const int qe = (wave & 1) * 64 + lane;
    unsigned hsrc = F_H + ((wave - 3) * 4 + (lane >> 4)) * 48, qst = F_RED + 4608 + (wave - 3) * 1024 + lane * 16;
    unsigned qsrc = F_RED + 4608 + ((16 * ((qe & 7) >> 2) + (qe >> 3)) * 4 + (qe & 3)) * 4;
    gu64* qdst = (gu64*)ws + G_Q + (hq * 2 + qr) * 128 + qe;
    PIN(mq.m[0]); PIN(mq.m[1]); PIN(mq.m[2]); PIN(mq.m[3]); PIN(hsrc); PIN(qst); PIN(qsrc); PIN(qdst);
    bar();
    if (wave >= 3) {
      const LAS v4u* hp = (const LAS v4u*)(L + hsrc);
      const v4u h0 = hp[0], h1 = hp[1], h2 = hp[2];
      f32x4 d0 = {0.f, 0.f, 0.f, 0.f}, d1 = {0.f, 0.f, 0.f, 0.f};
#pragma unroll
      for (int t = 0; t < 4; ++t) { d0 = mfma(afrag(h0[t], mq), wa[t], d0); d1 = mfma(afrag(h1[t], mq), wa[4 + t], d1); }
#pragma unroll
      for (int t = 0; t < 4; t += 2) { d0 = mfma(afrag(h2[t], mq), wa[8 + t], d0); d1 = mfma(afrag(h2[t + 1], mq), wa[9 + t], d1); }
      *(LAS f32x4*)(L + qst) = d0 + d1;
    }
    bar();
    if (wave < 2) {
      const LAS float* src = (const LAS float*)(L + qsrc);
      pubp(qdst, fadd(fadd(fadd(src[0], src[256]), fadd(src[512], src[768])), fadd(src[1024], src[1280])), tag);
    }
  } else {
    if (wave == 0) tag_put(L, load_state(ws), lane);
    if (GATE_MODB2) gate_wait(t0, GATE_MODB2);
    ModA ma;
    if (wave < 6) moda_load(p, 72 + 6 * x + wave, lane, ma);
    __builtin_amdgcn_sched_barrier(0);
    bar();
    if (wave < 6) {
      tag = tag_get(L);
      moda_finish(L, ws, 72 + 6 * x + wave, lane, ma, mk, (LAS float*)(L + F_RED + wave * 1024), tag);
    }
  }
  tag = tag_get(L);
  STAMP(4);
  pretouch<1>(L, ws, [&](int) { const int e = wave * 64 + lane; return G_X2 + (e >> 3) * 16 + (e & 7); });
  if ((PAGE_TOUCH & 1) && wave == 8) {
    const int k = lane & 7;
    const char* a = lane < 8 ? (const char*)p.wg + (size_t)(k < 7 ? k : 6) * 786432 + f * 128
                  : lane < 16 ? (const char*)p.wu + (size_t)(k < 7 ? k : 6) * 786432 + f * 128
                              : (const char*)p.wd + (size_t)f * 64 * D * 2 + (lane & 1) * 65536;
    page_touch(L, a);
  }
  const bool slow_tile = ((((unsigned)(uintptr_t)p.wg >> 7) + f) & 7) == 3;
  v4u wg[9], wu[9], wd[8];
#define THRF(n) do { if ((n) < F_N1 && !slow_tile && __builtin_amdgcn_s_memrealtime() - t0 < (u64)GATE_FFN2) asm volatile("s_waitcnt vmcnt(%0)" :: "n"(F_D1 - 1) : "memory"); } while (0)
  if (GATE_DN) {
    gate_wait(t0, GATE_DN);
    const rsrc_t rd = mk_rsrc(p.wd, FF * D * 2);
    const unsigned v = (lane >> 3) * (D * 2) + (lane & 7) * 16, s = f * 64 * (D * 2) + wave * 128;
#pragma unroll
    for (int t = 0; t < DN_FIRST; ++t) wd[t] = ldb(rd, v, s + t * 8 * (D * 2));
    __builtin_amdgcn_sched_barrier(0);
  }
  if (!slow_tile) gate_wait(t0, GATE_FFN);
  if (wave < 8) {
    const rsrc_t rg = mk_rsrc(p.wg, D * FF * 2), ru = mk_rsrc(p.wu, D * FF * 2);
    const unsigned v = (lane >> 3) * (FF * 2) + (lane & 7) * 16, s = wave * 72 * (FF * 2) + f * 128;
#pragma unroll
    for (int t = 0; t < 9; ++t) { THRF(2 * t); wg[t] = ldb(rg, v, s + t * 8 * (FF * 2)); THRF(2 * t + 1); wu[t] = ldb(ru, v, s + t * 8 * (FF * 2)); }
  }
  if (!GATE_DN) {
    const rsrc_t rd = mk_rsrc(p.wd, FF * D * 2);
    const unsigned v = (lane >> 3) * (D * 2) + (lane & 7) * 16, s = f * 64 * (D * 2) + wave * 128;
#pragma unroll
    for (int t = 0; t < DN_FIRST; ++t) { THRF(18 + t); wd[t] = ldb(rd, v, s + t * 8 * (D * 2)); }
  }
  __builtin_amdgcn_sched_barrier(0);
  STAMP(5);
  unsigned h2w, h2r, stw, str_, gur, guw0, actw, actr, trw, trr;
  gu64* ddst;
  {
    const int e = wave * 64 + lane, hw = e / 72, rem = e - hw * 72, c = (wave & 3) * 16 + (lane >> 2);
    h2w = F_H2 + ((hw * 8 + (rem & 7)) * 12 + (rem >> 3)) * 4;
    h2r = F_H2 + ((wave & 7) * 8 + (lane >> 3)) * 48;
    stw = F_ST + wave * 8;
    str_ = F_ST + ((lane & 15) < 9 ? (lane & 15) : 9) * 8;
    guw0 = F_RED + (wave & 7) * 1040 + red_off(lane, 256);
    gur = F_RED + ((lane & 3) * 520 + c) * 4;
    actw = F_ACT + ((lane & 3) == 0 ? (c & 7) * 8 + (c >> 3) : 64) * 4;
    actr = F_ACT + (lane >> 3) * 32;
    trw = F_TR + wave * 640 + red_off(lane, 128);
    trr = F_TR + wave * 640 + lane * 4;
    ddst = (gu64*)ws + G_DN + f * 576 + wave * 64 + lane;
    if (wave == 0 && lane == 0) *(LAS f32x2*)(L + F_ST + 72) = f32x2{0.f, 0.f};
  }
  PIN(h2w); PIN(h2r); PIN(stw); PIN(str_); PIN(guw0); PIN(gur); PIN(actw); PIN(actr); PIN(trw); PIN(trr); PIN(ddst);
  if (GATE_X2) gate_wait(t0, GATE_X2);
  {
    const int e = wave * 64 + lane;
    u64 gs[9];
    bool have_x2 = false;
    for (int spin = 0; spin < SPIN_CAP; ++spin) {
#pragma unroll
      for (int k = 0; k < 8; ++k) gs[k] = peek(ws, G_MOD + (k & 3) * NMOD + (k < 4 ? 3 : 4) * D + e);
      gs[8] = peek(ws, G_X2 + (e >> 3) * 16 + (e & 7));
      Chk c, cx;
#pragma unroll
      for (int k = 0; k < 8; ++k) c.add(gs[k], tag);
      cx.add(gs[8], tag);
      have_x2 = cx.all_ok();
      if (c.all_ok()) break;
    }
    float sb = rbf(fadd(fadd(gval(gs[0]), gval(gs[1])), fadd(gval(gs[2]), gval(gs[3]))));
    float c1b = rbf(1.f + rbf(fadd(fadd(gval(gs[4]), gval(gs[5])), fadd(gval(gs[6]), gval(gs[7])))));
    float on = (lane & 15) < 9 ? 64.f : 0.f;
    PIN(sb); PIN(c1b); PIN(on);
    STAMP(6);
    u64 gx[1] = {gs[8]};
    if (!have_x2) gather<1>(ws, tag, gx, [&](int) { return G_X2 + (e >> 3) * 16 + (e & 7); });
    STAMP(7);
    const float xv = gval(gx[0]);
    const float sh = __uint_as_float(__builtin_amdgcn_readfirstlane(__float_as_uint(xv)));
    const float d = xv - sh;
    float s1 = d, s2 = d * d;
    s1 += dpp<ROR8>(s1); s2 += dpp<ROR8>(s2); s1 += dpp<ROR4>(s1); s2 += dpp<ROR4>(s2);
    s1 += dpp<ROR2>(s1); s2 += dpp<ROR2>(s2); s1 += dpp<ROR1>(s1); s2 += dpp<ROR1>(s2);
    s1 = x16_sum(s1); s2 = x16_sum(s2);
    const float dm = s1 * (1.f / 64.f);
    if (lane == 0) *(LAS f32x2*)(L + stw) = f32x2{sh + dm, fmaf(-dm, s1, s2)};
    bar();
    const f32x2 st = *(const LAS f32x2*)(L + str_);
    __builtin_amdgcn_sched_barrier(0);
    {
      const rsrc_t rd = mk_rsrc(p.wd, FF * D * 2);
      const unsigned v = (lane >> 3) * (D * 2) + (lane & 7) * 16, s = f * 64 * (D * 2) + wave * 128;
#pragma unroll
      for (int t = DN_FIRST; t < DN_SECOND; ++t) wd[t] = ldb(rd, v, s + t * 8 * (D * 2));
      __builtin_amdgcn_sched_barrier(0);
    }
    const float mf = row_sum(st[0]) * (1.f / 9.f);
    const float dv = st[0] - mf;
    const float m2 = row_sum(fmaf(on * dv, dv, st[1]));
    *(LAS unsigned*)(L + h2w) = ln_apply_pk(xv, rbf(mf), ln_rden(m2 * (1.f / D)), c1b, sb);
  }
  bar();
  STAMP(8);
  v4u h0, h1;
  unsigned h8;
  {
    const LAS v4u* hp = (const LAS v4u*)(L + h2r);
    h0 = hp[0]; h1 = hp[1]; h8 = ((const LAS unsigned*)hp)[8];
  }
  __builtin_amdgcn_sched_barrier(0);
  if (DN_SECOND < 8) {
    const rsrc_t rd = mk_rsrc(p.wd, FF * D * 2);
    const unsigned v = (lane >> 3) * (D * 2) + (lane & 7) * 16, s = f * 64 * (D * 2) + wave * 128;
#pragma unroll
    for (int t = DN_SECOND; t < 8; ++t) wd[t] = ldb(rd, v, s + t * 8 * (D * 2));
    __builtin_amdgcn_sched_barrier(0);
  }
  if (wave < 8) {
    PIN(h0); PIN(h1); PIN(h8);
    f32x4 dg = {0.f, 0.f, 0.f, 0.f}, du = {0.f, 0.f, 0.f, 0.f};
#pragma unroll
    for (int t = 0; t < 9; ++t) {
      const v4u a = afrag(t < 4 ? h0[t & 3] : t < 8 ? h1[t & 3] : h8, mk);
      dg = mfma(a, wg[t], dg);
      du = mfma(a, wu[t], du);
    }
    *(LAS f32x4*)(L + guw0) = dg;
    *(LAS f32x4*)(L + guw0 + ((lane >> 5) == ((lane >> 3) & 1) ? 512 : 0)) = du;
  }
  bar();
  if (wave < 4) {
    const LAS float* r0 = (const LAS float*)(L + gur);
    float g = faddn(fadd(r0[0], r0[64]), fadd(r0[260], r0[324])), u = faddn(fadd(r0[128], r0[192]), fadd(r0[388], r0[452]));
    g += dpp<XOR1>(g); u += dpp<XOR1>(u);
    g += dpp<XOR2>(g); u += dpp<XOR2>(u);
    g = rbf(g); u = rbf(u);
    const float y = g * fmaf(g * g, 0.044715f * 0.7978845608028654f * 2.f * LOG2E, 0.7978845608028654f * 2.f * LOG2E);
    const float th = fmaf(-2.f, rcp_f(__builtin_amdgcn_exp2f(y) + 1.f), 1.f);
    const float act = rbf(0.5f * g * (1.f + th)) * u;
    *(LAS unsigned*)(L + actw) = pkr(act);
  }
  bar();
  STAMP(9);
  {
    const LAS v4u* ap = (const LAS v4u*)(L + actr);
    const v4u a0 = ap[0], a1 = ap[1];
    f32x4 d0 = {0.f, 0.f, 0.f, 0.f}, d1 = {0.f, 0.f, 0.f, 0.f};
#pragma unroll
    for (int t = 0; t < 4; ++t) { d0 = mfma(afrag(a0[t], mk), wd[t], d0); d1 = mfma(afrag(a1[t], mk), wd[4 + t], d1); }
    *(LAS f32x4*)(L + trw) = d0 + d1;
    lds_wait();
    pubp(ddst, fadd(*(const LAS float*)(L + trr), *(const LAS float*)(L + trr + 256)), tag);
  }
  STAMP(10);
}

DEV void role_modb(const Params& p, LAS unsigned char* L, int x, int wave, int lane, u64 t0, u64 code0) {
  if (CODE_PF && wave == 8) {
    const unsigned dump = (unsigned)(uintptr_t)(L + L_PF);
#pragma unroll
    for (int k = 0; k < (CODE_PF + 8191) / 8192; ++k) {
      const unsigned o = k * 8192 + lane * 128;
      asm volatile("s_mov_b32 m0, %0\n\ts_nop 0\n\tglobal_load_lds_dword %1, off" :: "s"(dump), "v"(code0 + (o < CODE_PF - 128 ? o : CODE_PF - 128)) : "m0", "memory");
    }
  }
  if ((PAGE_TOUCH & 4) && wave == 7) {
    const int k = lane & 7, m = lane >> 3;
    const char* base = m == 0 ? (const char*)p.wo : m == 1 ? (const char*)p.wg : m == 2 ? (const char*)p.wu : (const char*)p.wd;
    const size_t off = (size_t)(k < 6 ? k : 5) * 917504;
    page_touch(L, m < 4 ? base + off : (const char*)p.out);
  }
  const Mask mk = make_mask(lane, true);
  if (wave == 0) tag_put(L, load_state(p.ws), lane);
  bar();
  const Tag tag = tag_get(L);
  gate_wait(t0, GATE_MODB);
  for (int k = wave; k < 12; k += 9) {
    ModA ma;
    moda_load(p, 120 + x + 8 * k, lane, ma);
    __builtin_amdgcn_sched_barrier(0);
    moda_finish(L, p.ws, 120 + x + 8 * k, lane, ma, mk, (LAS float*)(L + wave * 1024), tag);
  }
  STAMP(2);
}

__global__ void __launch_bounds__(NTHR) adaln_block_kernel(Params p) {
  __shared__ __attribute__((aligned(16))) unsigned char smem[L_END + L_EXTRA];
  LAS unsigned char* L = (LAS unsigned char*)smem;
  const int lane = threadIdx.x & 63, wave = __builtin_amdgcn_readfirstlane(threadIdx.x >> 6);
  u64 code0;
  asm volatile("s_getpc_b64 %0" : "=s"(code0));
  code0 &= ~0xFFull;
  const u64 t0 = __builtin_amdgcn_s_memrealtime();
  STAMP_INIT();
  STAMP(0);
  const int x = blockIdx.x & 7, idx = blockIdx.x >> 3;
  if (idx < 22) role_att(p, L, x, idx, wave, lane, t0);
  else if (idx < 31) role_ffn(p, L, x, idx - 22, wave, lane, t0);
  else role_modb(p, L, x, wave, lane, t0, code0);
  STAMP_DUMP();
}

extern "C" void kernel_launch(void* const* d_in, const int* in_sizes, int n_in, void* d_out, int out_size, void* d_ws,
                              size_t ws_size, hipStream_t stream) {
  Params p;
  p.x = (const u16*)d_in[0];
  p.kc = (const u16*)d_in[1];
  p.vc = (const u16*)d_in[2];
  p.wq = (const u16*)d_in[3];
  p.wo = (const u16*)d_in[4];
  p.wg = (const u16*)d_in[6];
  p.wu = (const u16*)d_in[7];
  p.wd = (const u16*)d_in[8];
  p.c = (const u16*)d_in[9];
  p.wmod = (const u16*)d_in[10];
  p.out = (u16*)d_out;
  p.ws = (u64*)d_ws;
  hipLaunchKernelGGL(adaln_block_kernel, dim3(256), dim3(NTHR), 0, stream, p);
}
```

```cpp
#include <hip/hip_runtime.h>
#include <stdint.h>

typedef unsigned short u16;
typedef unsigned long long u64;
typedef unsigned v4u __attribute__((ext_vector_type(4)));
typedef float f32x4 __attribute__((ext_vector_type(4)));
typedef float f32x2 __attribute__((ext_vector_type(2)));
typedef __bf16 bf16x2 __attribute__((ext_vector_type(2)));
typedef short bf16x8 __attribute__((ext_vector_type(8)));
typedef __amdgpu_buffer_rsrc_t rsrc_t;
#define GAS __attribute__((address_space(1)))
#define LAS __attribute__((address_space(3)))
#define DEV __device__ __forceinline__
typedef GAS u64 gu64;
#define RLX_AGENT __ATOMIC_RELAXED, __HIP_MEMORY_SCOPE_AGENT

constexpr int D = 576, SK = 65536, WIN = 4096, DH = 128, FF = 4608, NMOD = 6 * D;
constexpr int NSPLIT = 11;
constexpr int NTHR = 576;
constexpr int SPIN_CAP = 1 << 16;
#define GATE_FFN 790
#define GATE_MODB 200
#define GATE_ATT 140
#define GATE_Q 450
#define GATE_FFN2 0
#define F_N1 0
#define F_D1 2
#define GATE_X2 0
#define POLL_SLEEP 0
#define Q_STAGGER 16
#define GATE_H1 260
#define DN_FIRST 0
#define MODA_HOLD 0
#define GATE_MODB2 120
#define CODE_PF 21504
#define GATE_DN 0
#define DN_SECOND 4
#define PAGE_TOUCH 0
#define V_N1 0
#define V_D1 2
#define V_D2 24
#define ATT_N1 5
#define ATT_D1 2
#define ATT_D2 24

constexpr int G_EPOCH = 0;
constexpr int G_MOD = 64;
constexpr int G_Q = G_MOD + 4 * NMOD;
constexpr int G_O = G_Q + 32 * 2 * 128;
constexpr int G_WO = G_O + 16 * 8 * NSPLIT * 64;
constexpr int G_X2 = G_WO + 128 * 576;
constexpr int G_DN = G_X2 + 72 * 16;
constexpr int G_END = G_DN + 72 * 576;
constexpr size_t G_STAMP = 1 << 20;

constexpr int L_K = 0;
constexpr int L_OL = 98304;
constexpr int L_Q = L_OL + 9216;
constexpr int L_MX = L_Q + 512;
constexpr int L_LS = L_MX + 64;
constexpr int L_OB = L_LS + 64;
constexpr int L_PF = L_OB + 128;
constexpr int L_WO = L_PF + 256;
constexpr int L_MA = L_WO + 36864;
constexpr int L_CNT = L_MA + 1536;
constexpr int L_TAG = L_MA + 1536 + 16;
constexpr int L_END = L_TAG + 16;
constexpr int F_RED = 0;
constexpr int F_H = 16384;
constexpr int F_H2 = 17664;
constexpr int F_ACT = 20736;
constexpr int F_TR = 21248;
constexpr int F_ST = 27136;

struct Params {
  const u16 *x, *kc, *vc, *wq, *wo, *wg, *wu, *wd, *c, *wmod;
  u16* out;
  u64* ws;
};

#define STAMP(i) do {} while (0)
#define STAMP_INIT() do {} while (0)
#define STAMP_DUMP() do {} while (0)
#define L_EXTRA 0

DEV float bf2f(unsigned b) { return __uint_as_float(b << 16); }
DEV unsigned cvt2(float lo, float hi) {
  const f32x2 v = {lo, hi};
  return __builtin_bit_cast(unsigned, __builtin_convertvector(v, bf16x2));
}
DEV unsigned pkr(float f) { return cvt2(f, f); }
DEV float rbf(float f) { return __uint_as_float(cvt2(f, f) & 0xFFFF0000u); }
#define TAG_LO_BITS 4
constexpr unsigned TAGM = (1u << TAG_LO_BITS) - 1u;
struct Tag { unsigned hi, lo, rest; };
DEV Tag make_tag(u64 st) {
  const u64 nx = st * 6364136223846793005ull + 1442695040888963407ull;
  Tag t;
  t.hi = __builtin_amdgcn_readfirstlane((unsigned)(nx >> 32));
  t.rest = __builtin_amdgcn_readfirstlane((unsigned)nx);
  t.lo = t.rest >> (32 - TAG_LO_BITS);
  return t;
}
DEV u64 enc(float v, unsigned hi, unsigned lo) {
  const unsigned u = (__float_as_uint(v) + (TAGM + 1) / 2) & ~TAGM;
  return ((u64)hi << 32) | (u64)(u | lo);
}
DEV Tag tag_put(LAS unsigned char* L, u64 st, int lane) {
  const Tag t = make_tag(st);
  if (lane == 0) *(LAS v4u*)(L + L_TAG) = v4u{t.hi, t.lo, t.rest, 0u};
  return t;
}
DEV Tag tag_get(const LAS unsigned char* L) {
  const v4u v = *(const LAS v4u*)(L + L_TAG);
  Tag t; t.hi = __builtin_amdgcn_readfirstlane(v[0]); t.lo = __builtin_amdgcn_readfirstlane(v[1]); t.rest = __builtin_amdgcn_readfirstlane(v[2]);
  return t;
}
DEV unsigned or_xor(unsigned a, unsigned b, unsigned c) { return __builtin_amdgcn_bitop3_b32(a, b, c, 0xF6); }
struct Chk {
  unsigned a = 0, b = 0;
  DEV void add(u64 g, const Tag& t) { a = or_xor(a, (unsigned)(g >> 32), t.hi); b = or_xor(b, (unsigned)g, t.lo); }
  DEV bool all_ok() const { return __builtin_amdgcn_ballot_w64((a | (b & TAGM)) != 0u) == 0ull; }
};
DEV float gval(u64 g) { return __uint_as_float((unsigned)g & ~TAGM); }
DEV void pub(u64* ws, int idx, float v, const Tag& t) { __hip_atomic_store((gu64*)ws + idx, enc(v, t.hi, t.lo), RLX_AGENT); }
DEV void pubp(gu64* dst, float v, const Tag& t) { __hip_atomic_store(dst, enc(v, t.hi, t.lo), RLX_AGENT); }
DEV u64 peek(u64* ws, int idx) { return __hip_atomic_load((gu64*)ws + idx, RLX_AGENT); }
template <int N, class F> DEV void gather(u64* ws, const Tag& t, u64 (&g)[N], F idx) {
  for (int spin = 0; spin < SPIN_CAP; ++spin) {
#pragma unroll
    for (int k = 0; k < N; ++k) g[k] = peek(ws, idx(k));
    Chk c;
#pragma unroll
    for (int k = 0; k < N; ++k) c.add(g[k], t);
    if (c.all_ok()) break;
    if (POLL_SLEEP) __builtin_amdgcn_s_sleep(POLL_SLEEP);
  }
}

struct G2 { v4u v; DEV float lo() const { return __uint_as_float(v[0] & ~TAGM); } DEV float hi() const { return __uint_as_float(v[2] & ~TAGM); } };
template <int N, class F> DEV void gather2(rsrc_t rw, const Tag& t, G2 (&g)[N], F idx) {
  for (int spin = 0; spin < SPIN_CAP; ++spin) {
    asm volatile("" ::: "memory");
#pragma unroll
    for (int k = 0; k < N; ++k) g[k].v = __builtin_amdgcn_raw_buffer_load_b128(rw, idx(k) * 8, 0, 16);
    unsigned a = 0, b = 0;
#pragma unroll
    for (int k = 0; k < N; ++k) { a = or_xor(or_xor(a, g[k].v[1], t.hi), g[k].v[3], t.hi); b = or_xor(or_xor(b, g[k].v[0], t.lo), g[k].v[2], t.lo); }
    if (__builtin_amdgcn_ballot_w64((a | (b & TAGM)) != 0u) == 0ull) break;
    if (POLL_SLEEP) __builtin_amdgcn_s_sleep(POLL_SLEEP);
  }
}
DEV void q_handoff(rsrc_t rw, unsigned voff, unsigned qdst, const Tag& t) {
  static_assert(TAG_LO_BITS == 4, "masks below");
  unsigned cnt;
  asm volatile(
      "s_mov_b32 %[cnt], %[cap]\n\t"
      "buffer_load_dwordx4 v[100:103], %[vo], %[rs], 0 offen sc1\n\t"
      "buffer_load_dwordx4 v[104:107], %[vo], %[rs], 0 offen offset:1024 sc1\n\t"
      "buffer_load_dwordx4 v[108:111], %[vo], %[rs], 0 offen offset:2048 sc1\n\t"
      "buffer_load_dwordx4 v[112:115], %[vo], %[rs], 0 offen offset:3072 sc1\n\t"
      "s_sleep %[stag]\n\t"
      "1:\n\t"
      "buffer_load_dwordx4 v[116:119], %[vo], %[rs], 0 offen sc1\n\t"
      "buffer_load_dwordx4 v[120:123], %[vo], %[rs], 0 offen offset:1024 sc1\n\t"
      "buffer_load_dwordx4 v[124:127], %[vo], %[rs], 0 offen offset:2048 sc1\n\t"
      "buffer_load_dwordx4 v[128:131], %[vo], %[rs], 0 offen offset:3072 sc1\n\t"
      "s_waitcnt vmcnt(4)\n\t"
      "v_xor_b32 v132, %[th], v101\n\t"
      "v_bitop3_b32 v132, v132, v103, %[th] bitop3:0xf6\n\t"
      "v_bitop3_b32 v132, v132, v105, %[th] bitop3:0xf6\n\t"
      "v_bitop3_b32 v132, v132, v107, %[th] bitop3:0xf6\n\t"
      "v_bitop3_b32 v132, v132, v109, %[th] bitop3:0xf6\n\t"
      "v_bitop3_b32 v132, v132, v111, %[th] bitop3:0xf6\n\t"
      "v_bitop3_b32 v132, v132, v113, %[th] bitop3:0xf6\n\t"
      "v_bitop3_b32 v132, v132, v115, %[th] bitop3:0xf6\n\t"
      "v_xor_b32 v133, %[tl], v100\n\t"
      "v_bitop3_b32 v133, v133, v102, %[tl] bitop3:0xf6\n\t"
      "v_bitop3_b32 v133, v133, v104, %[tl] bitop3:0xf6\n\t"
      "v_bitop3_b32 v133, v133, v106, %[tl] bitop3:0xf6\n\t"
      "v_bitop3_b32 v133, v133, v108, %[tl] bitop3:0xf6\n\t"
      "v_bitop3_b32 v133, v133, v110, %[tl] bitop3:0xf6\n\t"
      "v_bitop3_b32 v133, v133, v112, %[tl] bitop3:0xf6\n\t"
      "v_bitop3_b32 v133, v133, v114, %[tl] bitop3:0xf6\n\t"
      "v_and_b32 v133, 15, v133\n\t"
      "v_or_b32 v132, v132, v133\n\t"
      "v_cmp_ne_u32 vcc, 0, v132\n\t"
      "s_cbranch_vccz 2f\n\t"
      "buffer_load_dwordx4 v[100:103], %[vo], %[rs], 0 offen sc1\n\t"
      "buffer_load_dwordx4 v[104:107], %[vo], %[rs], 0 offen offset:1024 sc1\n\t"
      "buffer_load_dwordx4 v[108:111], %[vo], %[rs], 0 offen offset:2048 sc1\n\t"
      "buffer_load_dwordx4 v[112:115], %[vo], %[rs], 0 offen offset:3072 sc1\n\t"
      "s_waitcnt vmcnt(4)\n\t"
      "v_xor_b32 v132, %[th], v117\n\t"
      "v_bitop3_b32 v132, v132, v119, %[th] bitop3:0xf6\n\t"
      "v_bitop3_b32 v132, v132, v121, %[th] bitop3:0xf6\n\t"
      "v_bitop3_b32 v132, v132, v123, %[th] bitop3:0xf6\n\t"
      "v_bitop3_b32 v132, v132, v125, %[th] bitop3:0xf6\n\t"
      "v_bitop3_b32 v132, v132, v127, %[th] bitop3:0xf6\n\t"
      "v_bitop3_b32 v132, v132, v129, %[th] bitop3:0xf6\n\t"
      "v_bitop3_b32 v132, v132, v131, %[th] bitop3:0xf6\n\t"
      "v_xor_b32 v133, %[tl], v116\n\t"
      "v_bitop3_b32 v133, v133, v118, %[tl] bitop3:0xf6\n\t"
      "v_bitop3_b32 v133, v133, v120, %[tl] bitop3:0xf6\n\t"
      "v_bitop3_b32 v133, v133, v122, %[tl] bitop3:0xf6\n\t"
      "v_bitop3_b32 v133, v133, v124, %[tl] bitop3:0xf6\n\t"
      "v_bitop3_b32 v133, v133, v126, %[tl] bitop3:0xf6\n\t"
      "v_bitop3_b32 v133, v133, v128, %[tl] bitop3:0xf6\n\t"
      "v_bitop3_b32 v133, v133, v130, %[tl] bitop3:0xf6\n\t"
      "v_and_b32 v133, 15, v133\n\t"
      "v_or_b32 v132, v132, v133\n\t"
      "v_cmp_ne_u32 vcc, 0, v132\n\t"
      "s_cbranch_vccz 3f\n\t"
      "s_sub_u32 %[cnt], %[cnt], 1\n\t"
      "s_cbranch_scc0 1b\n\t"
      "2:\n\t"
      "v_and_b32 v132, 0xfffffff0, v100\n\t"
      "v_and_b32 v133, 0xfffffff0, v104\n\t"
      "v_add_f32 v132, v132, v133\n\t"
      "v_and_b32 v133, 0xfffffff0, v102\n\t"
      "v_and_b32 v136, 0xfffffff0, v106\n\t"
      "v_add_f32 v133, v133, v136\n\t"
      "v_cvt_pk_bf16_f32 v132, v132, v133\n\t"
      "ds_write_b32 %[qd], v132\n\t"
      "v_and_b32 v134, 0xfffffff0, v108\n\t"
      "v_and_b32 v135, 0xfffffff0, v112\n\t"
      "v_add_f32 v134, v134, v135\n\t"
      "v_and_b32 v135, 0xfffffff0, v110\n\t"
      "v_and_b32 v136, 0xfffffff0, v114\n\t"
      "v_add_f32 v135, v135, v136\n\t"
      "v_cvt_pk_bf16_f32 v134, v134, v135\n\t"
      "ds_write_b32 %[qd], v134 offset:256\n\t"
      "s_branch 4f\n\t"
      "3:\n\t"
      "v_and_b32 v132, 0xfffffff0, v116\n\t"
      "v_and_b32 v133, 0xfffffff0, v120\n\t"
      "v_add_f32 v132, v132, v133\n\t"
      "v_and_b32 v133, 0xfffffff0, v118\n\t"
      "v_and_b32 v136, 0xfffffff0, v122\n\t"
      "v_add_f32 v133, v133, v136\n\t"
      "v_cvt_pk_bf16_f32 v132, v132, v133\n\t"
      "ds_write_b32 %[qd], v132\n\t"
      "v_and_b32 v134, 0xfffffff0, v124\n\t"
      "v_and_b32 v135, 0xfffffff0, v128\n\t"
      "v_add_f32 v134, v134, v135\n\t"
      "v_and_b32 v135, 0xfffffff0, v126\n\t"
      "v_and_b32 v136, 0xfffffff0, v130\n\t"
      "v_add_f32 v135, v135, v136\n\t"
      "v_cvt_pk_bf16_f32 v134, v134, v135\n\t"
      "ds_write_b32 %[qd], v134 offset:256\n\t"
      "4:\n\t"
      "s_waitcnt lgkmcnt(0)\n\t"
      "s_barrier\n\t"
      "s_waitcnt vmcnt(0)"
      : [cnt] "=&s"(cnt)
      : [vo] "v"(voff), [rs] "s"(rw), [th] "s"(t.hi), [tl] "s"(t.lo), [qd] "v"(qdst), [cap] "n"(SPIN_CAP / 2), [stag] "n"(Q_STAGGER)
      : "vcc", "scc", "memory", "v100", "v101", "v102", "v103", "v104", "v105", "v106", "v107", "v108", "v109", "v110", "v111", "v112", "v113", "v114", "v115", "v116", "v117", "v118", "v119", "v120", "v121", "v122", "v123", "v124", "v125", "v126", "v127", "v128", "v129", "v130", "v131", "v132", "v133", "v134", "v135", "v136");
}
template <int N, class F> DEV void pretouch(LAS unsigned char* L, u64* ws, F idx) {
  const unsigned dump = (unsigned)(uintptr_t)(L + L_PF);
#pragma unroll
  for (int k = 0; k < N; ++k)
    asm volatile("s_mov_b32 m0, %0\n\ts_nop 0\n\tglobal_load_lds_dword %1, off sc1" :: "s"(dump), "v"((gu64*)ws + idx(k)) : "m0", "memory");
}
DEV void page_touch(LAS unsigned char* L, const void* a) {
  asm volatile("s_mov_b32 m0, %0\n\ts_nop 0\n\tglobal_load_lds_dword %1, off nt" :: "s"((unsigned)(uintptr_t)(L + L_PF)), "v"((const GAS char*)a) : "m0", "memory");
}
DEV f32x4 mfma(v4u a, v4u b, f32x4 c) {
  return __builtin_amdgcn_mfma_f32_16x16x32_bf16(__builtin_bit_cast(bf16x8, a), __builtin_bit_cast(bf16x8, b), c, 0, 0, 0);
}
DEV rsrc_t mk_rsrc(const void* p, unsigned bytes) { return __builtin_amdgcn_make_buffer_rsrc((void*)p, 0, (int)bytes, 0x00020000); }
DEV v4u ldb(rsrc_t r, unsigned voff, unsigned soff) { return __builtin_amdgcn_raw_buffer_load_b128(r, (int)voff, (int)soff, 2); }
#define SMALL_AUX 0
DEV unsigned ldb16(rsrc_t r, unsigned voff) { return __builtin_amdgcn_raw_buffer_load_b16(r, (int)voff, 0, SMALL_AUX); }
#define E_AUX 16
DEV u64 load_state(u64* ws) {
  int zoff = G_EPOCH * 8;
  asm volatile("" : "+v"(zoff));
  typedef unsigned v2u_ __attribute__((ext_vector_type(2)));
  const v2u_ v = __builtin_amdgcn_raw_buffer_load_b64(mk_rsrc(ws, 64), zoff, 0, E_AUX);
  return ((u64)v[1] << 32) | v[0];
}
DEV void bar() {
  asm volatile("s_waitcnt lgkmcnt(0)" ::: "memory");
  __builtin_amdgcn_s_barrier();
  asm volatile("" ::: "memory");
}
#define PIN(x) asm volatile("" : "+v"(x))
#define PINS(x) asm volatile("" : "+s"(x))
DEV void lds_wait() { asm volatile("s_waitcnt lgkmcnt(0)" ::: "memory"); }
template <int CTRL> DEV float dpp(float x) {
  return __builtin_bit_cast(float, __builtin_amdgcn_mov_dpp(__builtin_bit_cast(int, x), CTRL, 0xf, 0xf, true));
}
constexpr int ROR8 = 0x128, ROR4 = 0x124, ROR2 = 0x122, ROR1 = 0x121, XOR1 = 0xB1, XOR2 = 0x4E;
DEV float fmx(float a, float b) { float r; asm("v_max_f32 %0, %1, %2\n\ts_nop 1" : "=v"(r) : "v"(a), "v"(b)); return r; }
DEV float fmx3(float a, float b, float c) { float r; asm("v_max3_f32 %0, %1, %2, %3" : "=v"(r) : "v"(a), "v"(b), "v"(c)); return r; }
DEV float fmx3n(float a, float b, float c) { float r; asm("v_max3_f32 %0, %1, %2, %3\n\ts_nop 1" : "=v"(r) : "v"(a), "v"(b), "v"(c)); return r; }
DEV float x16_sum(float x) {
  auto s = __builtin_amdgcn_permlane16_swap(__float_as_uint(x), __float_as_uint(x), false, false);
  x = __uint_as_float(s[0]) + __uint_as_float(s[1]);
  auto t = __builtin_amdgcn_permlane32_swap(__float_as_uint(x), __float_as_uint(x), false, false);
  return __uint_as_float(t[0]) + __uint_as_float(t[1]);
}
DEV float x16_max(float x) {
  auto s = __builtin_amdgcn_permlane16_swap(__float_as_uint(x), __float_as_uint(x), false, false);
  x = fmaxf(__uint_as_float(s[0]), __uint_as_float(s[1]));
  auto t = __builtin_amdgcn_permlane32_swap(__float_as_uint(x), __float_as_uint(x), false, false);
  return fmaxf(__uint_as_float(t[0]), __uint_as_float(t[1]));
}
DEV float row_sum(float x) { x += dpp<ROR8>(x); x += dpp<ROR4>(x); x += dpp<ROR2>(x); x += dpp<ROR1>(x); return x; }
DEV float row_max(float x) { x = fmaxf(x, dpp<ROR8>(x)); x = fmaxf(x, dpp<ROR4>(x)); x = fmaxf(x, dpp<ROR2>(x)); x = fmaxf(x, dpp<ROR1>(x)); return x; }
DEV float wave_sum(float x) { return x16_sum(row_sum(x)); }
constexpr float LOG2E = 1.4426950408889634f;
DEV float fadd(float a, float b) { float r; asm("v_add_f32 %0, %1, %2" : "=v"(r) : "v"(a), "v"(b)); return r; }
DEV float faddn(float a, float b) { float r; asm("v_add_f32 %0, %1, %2\n\ts_nop 1" : "=v"(r) : "v"(a), "v"(b)); return r; }
DEV float exp_f(float x) { return __builtin_amdgcn_exp2f(x * LOG2E); }
DEV float rcp_f(float x) { return __builtin_amdgcn_rcpf(x); }
DEV float pair_pick(float a, float b, int lane) {
  const float a2 = dpp<0x50>(a), b2 = dpp<0x50>(b);
  const float a3 = dpp<0xFA>(a), b3 = dpp<0xFA>(b);
  const float lo = (lane & 1) ? b2 : a2, hi = (lane & 1) ? b3 : a3;
  return (lane & 4) ? hi : lo;
}
DEV float rdlane(float x, int l) { return __uint_as_float(__builtin_amdgcn_readlane(__float_as_uint(x), l)); }
DEV void gate_wait(u64 t0, unsigned ticks) { while (__builtin_amdgcn_s_memrealtime() - t0 < (u64)ticks) __builtin_amdgcn_s_sleep(2); }

struct Mask { unsigned m[4]; };
DEV Mask make_mask(int lane, bool on) {
  Mask k;
  const unsigned h = on ? ((lane & 1) ? 0xFFFF0000u : 0x0000FFFFu) : 0u;
#pragma unroll
  for (int r = 0; r < 4; ++r) k.m[r] = (((lane & 7) >> 1) == r) ? h : 0u;
  return k;
}
DEV v4u afrag(unsigned pp, const Mask& k) { return v4u{pp & k.m[0], pp & k.m[1], pp & k.m[2], pp & k.m[3]}; }

DEV unsigned ln_apply_pk(float x, float mean_b, float rden, float c1, float shift) {
  const float ln = rbf(rbf(x - mean_b) * rden);
  return pkr(rbf(ln * c1) + shift);
}
DEV float ln_rden(float var) { return rcp_f(rbf(__builtin_amdgcn_sqrtf(rbf(rbf(var) + 1e-5f)))); }

DEV void red_store(LAS float* area, f32x4 d, int lane) {
  const int u = (lane >> 3) & 1;
  if ((lane >> 5) == u) *(LAS f32x4*)(area + u * 64 + 8 * (lane & 7) + 4 * ((lane >> 4) & 1)) = d;
}
DEV unsigned red_off(int lane, int dump) {
  const int u = (lane >> 3) & 1;
  return 4 * ((lane >> 5) == u ? u * 64 + 8 * (lane & 7) + 4 * ((lane >> 4) & 1) : dump);
}
DEV unsigned silu_pk(unsigned craw) { const float v = bf2f(craw); return pkr(v * rcp_f(1.f + exp_f(-v))); }

struct WoAddr { unsigned tro, tri, obo, wro; gu64* dst; };
DEV WoAddr wo_prep(u64* ws, int pidx, int wave, int lane) {
  WoAddr a;
  a.tro = L_OL + wave * 640 + red_off(lane, 128); a.tri = L_OL + wave * 640 + lane * 4; a.obo = L_OB + (lane >> 3) * 16; a.wro = L_WO + wave * 4096 + lane * 16;
  a.dst = (gu64*)ws + G_WO + pidx * 576 + wave * 64 + lane;
  PIN(a.tro); PIN(a.tri); PIN(a.obo); PIN(a.wro); PIN(a.dst);
  return a;
}
DEV void wo_phase(LAS unsigned char* L, const WoAddr& a, const Tag& tag, const Mask& mk) {
  bar();
  v4u ob = *(const LAS v4u*)(L + a.obo);
  const LAS v4u* wr = (const LAS v4u*)(L + a.wro);
  v4u w0 = wr[0], w1 = wr[64], w2 = wr[128], w3 = wr[192];
  PIN(ob); PIN(w0); PIN(w1); PIN(w2); PIN(w3);
  __builtin_amdgcn_sched_barrier(0);
  f32x4 d0 = {0.f, 0.f, 0.f, 0.f}, d1 = {0.f, 0.f, 0.f, 0.f};
  d0 = mfma(afrag(ob[0], mk), w0, d0); d1 = mfma(afrag(ob[1], mk), w1, d1);
  d0 = mfma(afrag(ob[2], mk), w2, d0); d1 = mfma(afrag(ob[3], mk), w3, d1);
  *(LAS f32x4*)(L + a.tro) = d0 + d1;
  lds_wait();
  pubp(a.dst, fadd(*(const LAS float*)(L + a.tri), *(const LAS float*)(L + a.tri + 256)), tag);
}

struct ModA { v4u w[18]; unsigned c[3]; };
DEV void moda_load(const Params& p, int mt, int lane, ModA& m) {
  const rsrc_t rm = mk_rsrc(p.wmod, D * NMOD * 2), rc = mk_rsrc(p.c, D * 2);
  const int r0 = (mt & 3) * 144;
#pragma unroll
  for (int k = 0; k < 3; ++k) m.c[k] = ldb16(rc, (r0 + (k < 2 || lane < 16 ? lane + 64 * k : 143)) * 2);
  const unsigned v = (r0 + (lane >> 3)) * (NMOD * 2) + (lane & 7) * 16;
#pragma unroll
  for (int t = 0; t < 18; ++t) m.w[t] = ldb(rm, v, (mt >> 2) * 128 + t * 8 * (NMOD * 2));
}
DEV void moda_finish(LAS unsigned char* L, u64* ws, int mt, int lane, ModA& m, const Mask& mk, LAS float* tr, const Tag& tag) {
  unsigned sp[18];
  {
    const unsigned s0 = silu_pk(m.c[0]), s1 = silu_pk(m.c[1]), s2 = silu_pk(m.c[2]);
#pragma unroll
    for (int t = 0; t < 18; ++t) sp[t] = __builtin_amdgcn_ds_bpermute(((8 * t + (lane >> 3)) & 63) * 4, t < 8 ? s0 : t < 16 ? s1 : s2);
  }
  gu64* dst = (gu64*)ws + G_MOD + (mt & 3) * NMOD + (mt >> 2) * 64 + lane;
  unsigned tro = red_off(lane, 128);
  PIN(dst); PIN(tro);
#pragma unroll
  for (int t = 0; t < 18; ++t) PIN(sp[t]);
  __builtin_amdgcn_sched_barrier(0);
  f32x4 d0 = {0.f, 0.f, 0.f, 0.f}, d1 = {0.f, 0.f, 0.f, 0.f};
#pragma unroll
  for (int t = 0; t < 18; t += 2) {
    d0 = mfma(afrag(sp[t], mk), m.w[t], d0);
    __builtin_amdgcn_sched_barrier(0);
    d1 = mfma(afrag(sp[t + 1], mk), m.w[t + 1], d1);
    __builtin_amdgcn_sched_barrier(0);
  }
  *(LAS f32x4*)((LAS unsigned char*)tr + tro) = d0 + d1;
  lds_wait();
  pubp(dst, fadd(tr[lane], tr[64 + lane]), tag);
}

DEV void role_att(const Params& p, LAS unsigned char* L, int x, int i, int wave, int lane, u64 t0) {
  const int g = 2 * x + (i >= NSPLIT ? 1 : 0), j = i >= NSPLIT ? i - NSPLIT : i;
  const bool odd = g & 1;
  const int nm = odd ? 4 : 5;
  const bool moda = j < nm;
  const int ntile = !moda ? 24 : (!odd && j >= 3) ? 23 : 22;
  const int tile0 = !moda ? (odd ? 88 : 112) + 24 * (j - nm) : (!odd && j == 4) ? 89 : 22 * j;
  const bool owner = j < 8;
  int redc = -1;
  if (j >= 8) redc = 9 * x + (g & 1) * 3 + (j - 8);
  else if (j == 0) redc = 9 * x + 6 + (g & 1);
  else if (j == 1 && (g & 1) == 0) redc = 9 * x + 8;
  u64* ws = p.ws;
  const Mask mk = make_mask(lane, true);
  const int kap = lane >> 4, cc = lane & 15;

  if (wave < 8) {
    const size_t hb = ((size_t)g * SK + (SK - WIN)) * DH;
    const rsrc_t rk = mk_rsrc(p.kc + hb, WIN * DH * 2), rv = mk_rsrc(p.vc + hb, WIN * DH * 2);
    const bool three = wave + 16 < ntile;
    const unsigned tso = (tile0 + wave) * 4096;
    const unsigned ldsk = (unsigned)(uintptr_t)(L + L_K) + wave * 3 * 4096;
    unsigned vk[4];
#pragma unroll
    for (int q = 0; q < 4; ++q) vk[q] = (4 * q + kap) * 256 + ((cc ^ (4 * q + kap)) << 4);
#define KDMA(s, q) asm volatile("s_mov_b32 m0, %0\n\ts_nop 0\n\tbuffer_load_dwordx4 %1, %2, %3 offen nt lds" \
                     :: "s"(ldsk + (s) * 4096 + (q) * 1024), "v"(vk[q]), "s"(rk), "s"(tso + (s) * 32768) : "m0", "memory")
#define THR(n) do { if ((n) < ATT_N1) { if ((n) >= ATT_D1 && moda) asm volatile("s_waitcnt vmcnt(%0)" :: "n"(ATT_D1 - 1) : "memory"); } \
                     else if ((n) >= ATT_D2) asm volatile("s_waitcnt vmcnt(%0)" :: "n"(ATT_D2 - 1) : "memory"); } while (0)
#define THR_DEPTH(n) ((n) < ATT_N1 ? ATT_D1 : ATT_D2)
#define THRV(t) asm volatile("s_waitcnt vmcnt(%0)" :: "n"(((t) < V_N1 ? V_D1 : V_D2) - 1) : "memory")
    if (moda) __builtin_amdgcn_s_barrier();
    __builtin_amdgcn_s_setprio(2);
    KDMA(0, 0);
    if (GATE_ATT) gate_wait(t0, GATE_ATT);
    if (MODA_HOLD && moda) __builtin_amdgcn_s_barrier();
    THR(1); KDMA(0, 1); THR(2); KDMA(0, 2); THR(3); KDMA(0, 3);
    THR(4); KDMA(1, 0); THR(5); KDMA(1, 1); THR(6); KDMA(1, 2); THR(7); KDMA(1, 3);
    if (three) { THR(8); KDMA(2, 0); THR(9); KDMA(2, 1); THR(10); KDMA(2, 2); THR(11); KDMA(2, 3); }
#undef KDMA
    __builtin_amdgcn_s_setprio(0);
    v4u vr[12];
    const unsigned vv = kap * 1024 + cc * 16;
#pragma unroll
    for (int t = 0; t < 8; ++t) { THRV(t); vr[t] = ldb(rv, vv + (t & 3) * 256, tso + (t >> 2) * 32768); }
    if (three) {
#pragma unroll
      for (int t = 8; t < 12; ++t) { THRV(t); vr[t] = ldb(rv, vv + (t & 3) * 256, tso + 65536); }
    } else {
#pragma unroll
      for (int t = 8; t < 12; ++t) vr[t] = v4u{0u, 0u, 0u, 0u};
    }
#undef THR
#undef THRV
#undef THR_DEPTH
    __builtin_amdgcn_sched_barrier(0);
    STAMP(1);
    if (three) asm volatile("s_waitcnt vmcnt(12)" ::: "memory");
    else asm volatile("s_waitcnt vmcnt(8)" ::: "memory");
    v4u kf[12];
#pragma unroll
    for (int s = 0; s < 3; ++s)
#pragma unroll
      for (int ks = 0; ks < 4; ++ks)
        kf[s * 4 + ks] = *(const LAS v4u*)(L + L_K + (wave * 3 + (s < 2 || three ? s : 0)) * 4096 + cc * 256 + (((4 * ks + kap) ^ cc) << 4));
#pragma unroll
    for (int t = 0; t < 12; ++t) PIN(kf[t]);
    const int head = (lane >> 3) & 1;
    const int me = (wave & 3) * 64 + lane;
    unsigned qsrc = L_Q + head * 256 + kap * 16, mxw = L_MX + (head * 8 + wave) * 4, mxr = L_MX + head * 32, olw = L_OL + wave * 1024 + lane * 16;
    unsigned msrc = L_OL + ((16 * (((me >> 7) * 8 + (me & 7)) >> 2) + ((me & 127) >> 3)) * 4 + (me & 3)) * 4;
    gu64* mdst = (gu64*)ws + G_O + ((g * 8 + (me >> 5)) * NSPLIT + j) * 64 + (me & 31);
    bool lead = (lane & 0x37) == 0;
    PIN(qsrc); PIN(mxw); PIN(mxr); PIN(olw); PIN(msrc); PIN(mdst);
    STAMP(3);
    bar();
    STAMP(2);
    v4u qf[4];
#pragma unroll
    for (int ks = 0; ks < 4; ++ks) qf[ks] = *(const LAS v4u*)(L + qsrc + ks * 64);
#pragma unroll
    for (int ks = 0; ks < 4; ++ks) PIN(qf[ks]);
    __builtin_amdgcn_sched_barrier(0);
    f32x4 acc[3] = {{0.f, 0.f, 0.f, 0.f}, {0.f, 0.f, 0.f, 0.f}, {0.f, 0.f, 0.f, 0.f}};
#pragma unroll
    for (int ks = 0; ks < 4; ++ks)
#pragma unroll
      for (int s = 0; s < 3; ++s) acc[s] = mfma(kf[s * 4 + ks], qf[ks], acc[s]);
    __builtin_amdgcn_sched_barrier(0);
    float sc[12];
#pragma unroll
    for (int t = 0; t < 12; t += 2) {
      const unsigned u = cvt2(acc[t >> 2][t & 3], acc[t >> 2][(t & 3) + 1]);
      const unsigned w = cvt2(__uint_as_float(u << 16) * 0.08838834764831845f, __uint_as_float(u & 0xFFFF0000u) * 0.08838834764831845f);
      sc[t] = __uint_as_float(w << 16);
      sc[t + 1] = __uint_as_float(w & 0xFFFF0000u);
    }
    if (!three) {
#pragma unroll
      for (int t = 8; t < 12; ++t) sc[t] = -INFINITY;
    }
    float mx = fmx3n(fmx3(fmx3(sc[0], sc[1], sc[2]), fmx3(sc[3], sc[4], sc[5]), fmx3(sc[6], sc[7], sc[8])), fmx3(sc[9], sc[10], sc[11]), sc[0]);
    mx = x16_max(mx);
    if (lead) *(LAS float*)(L + mxw) = mx;
    lds_wait();
    if (lane == 0) __hip_atomic_fetch_add((LAS unsigned*)(L + L_CNT), 1u, __ATOMIC_RELAXED, __HIP_MEMORY_SCOPE_WORKGROUP);
    while (*(volatile LAS unsigned*)(L + L_CNT) < 8u) {}
    asm volatile("" ::: "memory");
    float m;
    {
      const f32x4 a = *(const LAS f32x4*)(L + mxr), b = *(const LAS f32x4*)(L + mxr + 16);
      m = fmx3(fmx3(a[0], a[1], a[2]), fmx3(a[3], b[0], b[1]), fmx(b[2], b[3]));
    }
    v4u af[12];
    float ls0 = 0.f, ls1 = 0.f;
    const float mneg = -m * LOG2E;
#pragma unroll
    for (int t = 0; t < 12; t += 2) {
      const float e0 = __builtin_amdgcn_exp2f(fmaf(sc[t], LOG2E, mneg)), e1 = __builtin_amdgcn_exp2f(fmaf(sc[t + 1], LOG2E, mneg));
      ls0 += e0; ls1 += e1;
      af[t] = afrag(pkr(e0), mk); af[t + 1] = afrag(pkr(e1), mk);
    }
    const float ls = x16_sum(ls0 + ls1);
    if (lead) *(LAS float*)(L + mxw + 64) = ls;
    const Tag tag = tag_get(L);
    __builtin_amdgcn_sched_barrier(0);
    STAMP(4);
    f32x4 o0 = {0.f, 0.f, 0.f, 0.f}, o1 = {0.f, 0.f, 0.f, 0.f};
#pragma unroll
    for (int t = 0; t < 12; t += 2) { o0 = mfma(af[t], vr[t], o0); o1 = mfma(af[t + 1], vr[t + 1], o1); }
    *(LAS f32x4*)(L + olw) = o0 + o1;
    STAMP(5);
    bar();
    if (wave < 4) {
      const LAS float* src = (const LAS float*)(L + msrc);
      float s[8];
#pragma unroll
      for (int w = 0; w < 8; ++w) s[w] = src[w * 256];
      pubp(mdst, fadd(fadd(fadd(s[0], s[1]), fadd(s[2], s[3])), fadd(fadd(s[4], s[5]), fadd(s[6], s[7]))), tag);
    } else if (wave == 4 && lane < 16) {
      const int u = lane >> 1, hh = u >> 2;
      const f32x4 a = *(const LAS f32x4*)(L + ((lane & 1) ? L_LS : L_MX) + hh * 32), b = *(const LAS f32x4*)(L + ((lane & 1) ? L_LS : L_MX) + hh * 32 + 16);
      const float vs = ((a[0] + a[1]) + (a[2] + a[3])) + ((b[0] + b[1]) + (b[2] + b[3]));
      const float vm = fmaxf(fmaxf(fmaxf(a[0], a[1]), fmaxf(a[2], a[3])), fmaxf(fmaxf(b[0], b[1]), fmaxf(b[2], b[3])));
      pub(ws, G_O + ((g * 8 + u) * NSPLIT + j) * 64 + 32 + (lane & 1), (lane & 1) ? vs : vm, tag);
    }
    STAMP(6);
    if (owner) {
      wo_phase(L, wo_prep(ws, g * 8 + j, wave, lane), tag, mk);
      STAMP(8);
    }
  } else {
    __builtin_amdgcn_s_setprio(3);
    Tag tag;
    {
      if (moda) {
        u64 st = load_state(ws);
        ModA ma;
        moda_load(p, 9 * x + (odd ? 5 : 0) + j, lane, ma);
        __builtin_amdgcn_sched_barrier(0);
        __builtin_amdgcn_s_barrier();
        asm volatile("" : "+v"(st));
        tag = tag_put(L, st, lane);
        moda_finish(L, ws, 9 * x + (odd ? 5 : 0) + j, lane, ma, mk, (LAS float*)(L + L_MA), tag);
        STAMP(9);
        if (MODA_HOLD) __builtin_amdgcn_s_barrier();
      } else {
        tag = tag_put(L, load_state(ws), lane);
      }
    }
    float xr = 0.f;
    if (redc >= 0) xr = bf2f(p.x[redc * 8 + (lane & 7)]);
    if (GATE_Q) gate_wait(t0, GATE_Q);
    const rsrc_t rw = mk_rsrc(ws, G_END * 8);
    if (lane == 0) *(volatile LAS unsigned*)(L + L_CNT) = 0u;
    q_handoff(rw, (G_Q + 2 * g * 256 + 2 * lane) * 8, (unsigned)(uintptr_t)(L + L_Q) + lane * 4, tag);
    STAMP(1);
    if (owner) {
      const int ob = G_O + (g * 8 + j) * NSPLIT * 64;
      pretouch<1>(L, ws, [&](int) { const int q_ = lane < 33 ? lane : 32; return ob + (q_ / 3) * 64 + (q_ % 3) * 16; });
    }
    if (redc >= 0) {
      pretouch<2>(L, ws, [&](int s) { return G_WO + (s * 64 + lane) * 576 + redc * 8; });
      pretouch<2>(L, ws, [&](int s) { const int f_ = s * 64 + lane; return G_DN + (f_ < 72 ? f_ : 71) * 576 + redc * 8; });
    }
    if (owner) {
      const rsrc_t rwo = mk_rsrc(p.wo, 4096 * D * 2);
      const unsigned wo_v = (lane >> 3) * (D * 2) + (lane & 7) * 16;
      const unsigned wo_s = (g * 256 + j * 32) * (D * 2), ldsw = (unsigned)(uintptr_t)(L + L_WO);
#pragma unroll
      for (int w = 0; w < 9; ++w)
#pragma unroll
        for (int t = 0; t < 4; ++t)
          asm volatile("s_mov_b32 m0, %0\n\ts_nop 0\n\tbuffer_load_dwordx4 %1, %2, %3 offen nt lds"
                       :: "s"(ldsw + (w * 4 + t) * 1024), "v"(wo_v), "s"(rwo), "s"(wo_s + w * 128 + t * 8 * (D * 2)) : "m0", "memory");
    }
    bar();
    STAMP(2);
    if (owner) {
      const int ob = G_O + (g * 8 + j) * NSPLIT * 64;
      const int k2 = kap < 3 ? 8 + kap : 10;
      unsigned obw = L_OB + (((2 * cc) & 7) * 4 + (cc >> 2)) * 4;
      float live = kap < 3 ? 1.f : 0.f;
      PIN(obw); PIN(live);
      const WoAddr wa = wo_prep(ws, g * 8 + j, 8, lane);
      G2 go[6];
      gather2<6>(rw, tag, go, [&](int k) { const int sp = k % 3 == 0 ? kap : k % 3 == 1 ? 4 + kap : k2; return ob + sp * 64 + (k < 3 ? 2 * cc : 32); });
      STAMP(3);
      const float M = x16_max(fmaxf(fmaxf(go[3].lo(), go[4].lo()), go[5].lo()));
      const float Mn = -M * LOG2E;
      const float w0 = __builtin_amdgcn_exp2f(fmaf(go[3].lo(), LOG2E, Mn)), w1 = __builtin_amdgcn_exp2f(fmaf(go[4].lo(), LOG2E, Mn));
      const float w2 = __builtin_amdgcn_exp2f(fmaf(go[5].lo(), LOG2E, Mn)) * live;
      const float Ls = x16_sum(fmaf(w2, go[5].hi(), fmaf(w1, go[4].hi(), w0 * go[3].hi())));
      const float o0 = x16_sum(fmaf(w2, go[2].lo(), fmaf(w1, go[1].lo(), w0 * go[0].lo())));
      const float o1 = x16_sum(fmaf(w2, go[2].hi(), fmaf(w1, go[1].hi(), w0 * go[0].hi())));
      const float rl = rcp_f(Ls);
      *(LAS unsigned*)(L + obw) = pkr(o0 * rl);
      *(LAS unsigned*)(L + obw + 16) = pkr(o1 * rl);
      asm volatile("s_waitcnt vmcnt(0)" ::: "memory");
      wo_phase(L, wa, tag, mk);
      STAMP(4);
    }
    if (redc >= 0) {
      u64 gg[8];
      gather<8>(ws, tag, gg, [&](int k) { return G_MOD + (k & 3) * NMOD + (k < 4 ? 2 : 5) * D + redc * 8 + (lane & 7); });
      float ga = rbf(fadd(fadd(gval(gg[0]), gval(gg[1])), fadd(gval(gg[2]), gval(gg[3]))));
      float gb = rbf(fadd(fadd(gval(gg[4]), gval(gg[5])), fadd(gval(gg[6]), gval(gg[7]))));
      gu64* xdst = (gu64*)ws + G_X2 + redc * 16 + (lane & 7);
      GAS u16* odst = (GAS u16*)p.out + redc * 8 + (lane & 7);
      PIN(ga); PIN(gb); PIN(xr); PIN(xdst); PIN(odst);
      float x2;
      {
        G2 gw[8];
        gather2<8>(rw, tag, gw, [&](int s) { return G_WO + (s * 16 + (lane >> 2)) * 576 + redc * 8 + 2 * (lane & 3); });
        STAMP(5);
        float t0_ = faddn(fadd(fadd(gw[0].lo(), gw[1].lo()), fadd(gw[2].lo(), gw[3].lo())), fadd(fadd(gw[4].lo(), gw[5].lo()), fadd(gw[6].lo(), gw[7].lo())));
        float t1_ = faddn(fadd(fadd(gw[0].hi(), gw[1].hi()), fadd(gw[2].hi(), gw[3].hi())), fadd(fadd(gw[4].hi(), gw[5].hi()), fadd(gw[6].hi(), gw[7].hi())));
        t0_ += dpp<ROR8>(t0_); t1_ += dpp<ROR8>(t1_); t0_ += dpp<ROR4>(t0_); t1_ += dpp<ROR4>(t1_);
        t0_ = x16_sum(t0_); t1_ = x16_sum(t1_);
        const float ts = pair_pick(t0_, t1_, lane);
        x2 = rbf(xr + rbf(ga * rbf(ts)));
        if (lane < 8) pubp(xdst, x2, tag);
      }
      STAMP(6);
      if (PAGE_TOUCH & 2) page_touch(L, p.out);
      {
        G2 gd[5];
        float w4 = lane < 32 ? 1.f : 0.f;
        PIN(w4);
        gather2<5>(rw, tag, gd, [&](int s) { const int f_ = s * 16 + (lane >> 2); return G_DN + (f_ < 72 ? f_ : 71) * 576 + redc * 8 + 2 * (lane & 3); });
        STAMP(7);
        float t0_ = fmaf(w4, gd[4].lo(), fadd(fadd(gd[0].lo(), gd[1].lo()), fadd(gd[2].lo(), gd[3].lo())));
        float t1_ = fmaf(w4, gd[4].hi(), fadd(fadd(gd[0].hi(), gd[1].hi()), fadd(gd[2].hi(), gd[3].hi())));
        t0_ += dpp<ROR8>(t0_); t1_ += dpp<ROR8>(t1_); t0_ += dpp<ROR4>(t0_); t1_ += dpp<ROR4>(t1_);
        t0_ = x16_sum(t0_); t1_ = x16_sum(t1_);
        const float ts = pair_pick(t0_, t1_, lane);
        const unsigned o = pkr(x2 + rbf(gb * rbf(ts)));
        if (lane < 8) *odst = (u16)o;
        if (redc == 0 && lane == 0) __hip_atomic_store((gu64*)ws + G_EPOCH, ((u64)tag.hi << 32) | (u64)tag.rest, RLX_AGENT);
      }
      STAMP(8);
    }
  }
}

DEV void role_ffn(const Params& p, LAS unsigned char* L, int x, int i, int wave, int lane, u64 t0) {
  const int f = 9 * x + i;
  u64* ws = p.ws;
  const Mask mk = make_mask(lane, true);
  LAS float* red = (LAS float*)(L + F_RED);
  const rsrc_t rm = mk_rsrc(p.wmod, D * NMOD * 2), rc = mk_rsrc(p.c, D * 2), rx = mk_rsrc(p.x, D * 2);
  const int hq = 4 * x + (i >> 1), qr = i & 1;
  const rsrc_t rw = mk_rsrc(ws, G_END * 8);
  Tag tag;
  if (i < 8) {
    v4u wa[12];
    if (wave >= 3) {
      const rsrc_t rq = mk_rsrc(p.wq, D * 4096 * 2);
      const unsigned v = (lane >> 4) * 8192 + (lane & 15) * 16, s = (qr * 288 + (wave - 3) * 48) * 8192 + hq * 256;
#pragma unroll
      for (int t = 0; t < 12; ++t) wa[t] = ldb(rq, v, s + t * 4 * 8192);
      __builtin_amdgcn_sched_barrier(0);
      STAMP(1);
    } else {
      __builtin_amdgcn_s_setprio(3);
      const u64 st = load_state(ws);
      float xv[9];
#pragma unroll
      for (int k = 0; k < 9; ++k) xv[k] = bf2f(ldb16(rx, (lane + 64 * k) * 2));
      const int p0 = wave * 64 + lane, pr = p0 < 144 ? p0 : 143;
      const unsigned xo = __builtin_amdgcn_raw_buffer_load_b32(rx, (qr * 288 + 2 * pr) * 2, 0, 0);
      float s = 0.f;
#pragma unroll
      for (int k = 0; k < 9; ++k) s += xv[k];
      const float mf = wave_sum(s) * (1.f / D);
      float v = 0.f;
#pragma unroll
      for (int k = 0; k < 9; ++k) { const float d = xv[k] - mf; v = fmaf(d, d, v); }
      const float rden = ln_rden(wave_sum(v) * (1.f / D)), mean = rbf(mf);
      tag = wave == 0 ? tag_put(L, st, lane) : make_tag(st);
      const int hw = pr / 24, hr = 2 * pr - hw * 48;
      unsigned hdst = F_H + ((hw * 4 + (hr & 3)) * 12 + (hr >> 2)) * 4;
      float ln0 = rbf(rbf(__uint_as_float(xo << 16) - mean) * rden), ln1 = rbf(rbf(__uint_as_float(xo & 0xFFFF0000u) - mean) * rden);
      PIN(hdst); PIN(ln0); PIN(ln1);
      STAMP(2);
      if (GATE_H1) gate_wait(t0, GATE_H1);
      G2 ga[8];
      gather2<8>(rw, tag, ga, [&](int k) { return G_MOD + (k & 3) * NMOD + (k >> 2) * D + qr * 288 + 2 * pr; });
      STAMP(3);
      const float sa0 = rbf(fadd(fadd(ga[0].lo(), ga[1].lo()), fadd(ga[2].lo(), ga[3].lo()))), sa1 = rbf(fadd(fadd(ga[0].hi(), ga[1].hi()), fadd(ga[2].hi(), ga[3].hi())));
      const float ca0 = rbf(fadd(fadd(ga[4].lo(), ga[5].lo()), fadd(ga[6].lo(), ga[7].lo()))), ca1 = rbf(fadd(fadd(ga[4].hi(), ga[5].hi()), fadd(ga[6].hi(), ga[7].hi())));
      *(LAS unsigned*)(L + hdst) = pkr(rbf(ln0 * rbf(1.f + ca0)) + sa0);
      *(LAS unsigned*)(L + hdst + 48) = pkr(rbf(ln1 * rbf(1.f + ca1)) + sa1);
      __builtin_amdgcn_s_setprio(0);
    }
    Mask mq = make_mask(lane, (lane & 8) == 0);
    const int qe = (wave & 1) * 64 + lane;
    unsigned hsrc = F_H + ((wave - 3) * 4 + (lane >> 4)) * 48, qst = F_RED + 4608 + (wave - 3) * 1024 + lane * 16;
    unsigned qsrc = F_RED + 4608 + ((16 * ((qe & 7) >> 2) + (qe >> 3)) * 4 + (qe & 3)) * 4;
    gu64* qdst = (gu64*)ws + G_Q + (hq * 2 + qr) * 128 + qe;
    PIN(mq.m[0]); PIN(mq.m[1]); PIN(mq.m[2]); PIN(mq.m[3]); PIN(hsrc); PIN(qst); PIN(qsrc); PIN(qdst);
    bar();
    if (wave >= 3) {
      const LAS v4u* hp = (const LAS v4u*)(L + hsrc);
      const v4u h0 = hp[0], h1 = hp[1], h2 = hp[2];
      f32x4 d0 = {0.f, 0.f, 0.f, 0.f}, d1 = {0.f, 0.f, 0.f, 0.f};
#pragma unroll
      for (int t = 0; t < 4; ++t) { d0 = mfma(afrag(h0[t], mq), wa[t], d0); d1 = mfma(afrag(h1[t], mq), wa[4 + t], d1); }
#pragma unroll
      for (int t = 0; t < 4; t += 2) { d0 = mfma(afrag(h2[t], mq), wa[8 + t], d0); d1 = mfma(afrag(h2[t + 1], mq), wa[9 + t], d1); }
      *(LAS f32x4*)(L + qst) = d0 + d1;
    }
    bar();
    if (wave < 2) {
      const LAS float* src = (const LAS float*)(L + qsrc);
      pubp(qdst, fadd(fadd(fadd(src[0], src[256]), fadd(src[512], src[768])), fadd(src[1024], src[1280])), tag);
    }
  } else {
    if (wave == 0) tag_put(L, load_state(ws), lane);
    if (GATE_MODB2) gate_wait(t0, GATE_MODB2);
    ModA ma;
    if (wave < 6) moda_load(p, 72 + 6 * x + wave, lane, ma);
    __builtin_amdgcn_sched_barrier(0);
    bar();
    if (wave < 6) {
      tag = tag_get(L);
      moda_finish(L, ws, 72 + 6 * x + wave, lane, ma, mk, (LAS float*)(L + F_RED + wave * 1024), tag);
    }
  }
  tag = tag_get(L);
  STAMP(4);
  pretouch<1>(L, ws, [&](int) { const int e = wave * 64 + lane; return G_X2 + (e >> 3) * 16 + (e & 7); });
  if ((PAGE_TOUCH & 1) && wave == 8) {
    const int k = lane & 7;
    const char* a = lane < 8 ? (const char*)p.wg + (size_t)(k < 7 ? k : 6) * 786432 + f * 128
                  : lane < 16 ? (const char*)p.wu + (size_t)(k < 7 ? k : 6) * 786432 + f * 128
                              : (const char*)p.wd + (size_t)f * 64 * D * 2 + (lane & 1) * 65536;
    page_touch(L, a);
  }
  const bool slow_tile = ((((unsigned)(uintptr_t)p.wg >> 7) + f) & 7) == 3;
  v4u wg[9], wu[9], wd[8];
#define THRF(n) do { if ((n) < F_N1 && !slow_tile && __builtin_amdgcn_s_memrealtime() - t0 < (u64)GATE_FFN2) asm volatile("s_waitcnt vmcnt(%0)" :: "n"(F_D1 - 1) : "memory"); } while (0)
  if (GATE_DN) {
    gate_wait(t0, GATE_DN);
    const rsrc_t rd = mk_rsrc(p.wd, FF * D * 2);
    const unsigned v = (lane >> 3) * (D * 2) + (lane & 7) * 16, s = f * 64 * (D * 2) + wave * 128;
#pragma unroll
    for (int t = 0; t < DN_FIRST; ++t) wd[t] = ldb(rd, v, s + t * 8 * (D * 2));
    __builtin_amdgcn_sched_barrier(0);
  }
  if (!slow_tile) gate_wait(t0, GATE_FFN);
  if (wave < 8) {
    const rsrc_t rg = mk_rsrc(p.wg, D * FF * 2), ru = mk_rsrc(p.wu, D * FF * 2);
    const unsigned v = (lane >> 3) * (FF * 2) + (lane & 7) * 16, s = wave * 72 * (FF * 2) + f * 128;
#pragma unroll
    for (int t = 0; t < 9; ++t) { THRF(2 * t); wg[t] = ldb(rg, v, s + t * 8 * (FF * 2)); THRF(2 * t + 1); wu[t] = ldb(ru, v, s + t * 8 * (FF * 2)); }
  }
  if (!GATE_DN) {
    const rsrc_t rd = mk_rsrc(p.wd, FF * D * 2);
    const unsigned v = (lane >> 3) * (D * 2) + (lane & 7) * 16, s = f * 64 * (D * 2) + wave * 128;
#pragma unroll
    for (int t = 0; t < DN_FIRST; ++t) { THRF(18 + t); wd[t] = ldb(rd, v, s + t * 8 * (D * 2)); }
  }
  __builtin_amdgcn_sched_barrier(0);
  STAMP(5);
  unsigned h2w, h2r, stw, str_, gur, guw0, actw, actr, trw, trr;
  gu64* ddst;
  {
    const int e = wave * 64 + lane, hw = e / 72, rem = e - hw * 72, c = (wave & 3) * 16 + (lane >> 2);
    h2w = F_H2 + ((hw * 8 + (rem & 7)) * 12 + (rem >> 3)) * 4;
    h2r = F_H2 + ((wave & 7) * 8 + (lane >> 3)) * 48;
    stw = F_ST + wave * 8;
    str_ = F_ST + ((lane & 15) < 9 ? (lane & 15) : 9) * 8;
    guw0 = F_RED + (wave & 7) * 1040 + red_off(lane, 256);
    gur = F_RED + ((lane & 3) * 520 + c) * 4;
    actw = F_ACT + ((lane & 3) == 0 ? (c & 7) * 8 + (c >> 3) : 64) * 4;
    actr = F_ACT + (lane >> 3) * 32;
    trw = F_TR + wave * 640 + red_off(lane, 128);
    trr = F_TR + wave * 640 + lane * 4;
    ddst = (gu64*)ws + G_DN + f * 576 + wave * 64 + lane;
    if (wave == 0 && lane == 0) *(LAS f32x2*)(L + F_ST + 72) = f32x2{0.f, 0.f};
  }
  PIN(h2w); PIN(h2r); PIN(stw); PIN(str_); PIN(guw0); PIN(gur); PIN(actw); PIN(actr); PIN(trw); PIN(trr); PIN(ddst);
  if (GATE_X2) gate_wait(t0, GATE_X2);
  {
    const int e = wave * 64 + lane;
    u64 gs[9];
    bool have_x2 = false;
    for (int spin = 0; spin < SPIN_CAP; ++spin) {
#pragma unroll
      for (int k = 0; k < 8; ++k) gs[k] = peek(ws, G_MOD + (k & 3) * NMOD + (k < 4 ? 3 : 4) * D + e);
      gs[8] = peek(ws, G_X2 + (e >> 3) * 16 + (e & 7));
      Chk c, cx;
#pragma unroll
      for (int k = 0; k < 8; ++k) c.add(gs[k], tag);
      cx.add(gs[8], tag);
      have_x2 = cx.all_ok();
      if (c.all_ok()) break;
    }
    float sb = rbf(fadd(fadd(gval(gs[0]), gval(gs[1])), fadd(gval(gs[2]), gval(gs[3]))));
    float c1b = rbf(1.f + rbf(fadd(fadd(gval(gs[4]), gval(gs[5])), fadd(gval(gs[6]), gval(gs[7])))));
    float on = (lane & 15) < 9 ? 64.f : 0.f;
    PIN(sb); PIN(c1b); PIN(on);
    STAMP(6);
    u64 gx[1] = {gs[8]};
    if (!have_x2) gather<1>(ws, tag, gx, [&](int) { return G_X2 + (e >> 3) * 16 + (e & 7); });
    STAMP(7);
    const float xv = gval(gx[0]);
    const float sh = __uint_as_float(__builtin_amdgcn_readfirstlane(__float_as_uint(xv)));
    const float d = xv - sh;
    float s1 = d, s2 = d * d;
    s1 += dpp<ROR8>(s1); s2 += dpp<ROR8>(s2); s1 += dpp<ROR4>(s1); s2 += dpp<ROR4>(s2);
    s1 += dpp<ROR2>(s1); s2 += dpp<ROR2>(s2); s1 += dpp<ROR1>(s1); s2 += dpp<ROR1>(s2);
    s1 = x16_sum(s1); s2 = x16_sum(s2);
    const float dm = s1 * (1.f / 64.f);
    if (lane == 0) *(LAS f32x2*)(L + stw) = f32x2{sh + dm, fmaf(-dm, s1, s2)};
    bar();
    const f32x2 st = *(const LAS f32x2*)(L + str_);
    __builtin_amdgcn_sched_barrier(0);
    {
      const rsrc_t rd = mk_rsrc(p.wd, FF * D * 2);
      const unsigned v = (lane >> 3) * (D * 2) + (lane & 7) * 16, s = f * 64 * (D * 2) + wave * 128;
#pragma unroll
      for (int t = DN_FIRST; t < DN_SECOND; ++t) wd[t] = ldb(rd, v, s + t * 8 * (D * 2));
      __builtin_amdgcn_sched_barrier(0);
    }
    const float mf = row_sum(st[0]) * (1.f / 9.f);
    const float dv = st[0] - mf;
    const float m2 = row_sum(fmaf(on * dv, dv, st[1]));
    *(LAS unsigned*)(L + h2w) = ln_apply_pk(xv, rbf(mf), ln_rden(m2 * (1.f / D)), c1b, sb);
  }
  bar();
  STAMP(8);
  v4u h0, h1;
  unsigned h8;
  {
    const LAS v4u* hp = (const LAS v4u*)(L + h2r);
    h0 = hp[0]; h1 = hp[1]; h8 = ((const LAS unsigned*)hp)[8];
  }
  __builtin_amdgcn_sched_barrier(0);
  if (DN_SECOND < 8) {
    const rsrc_t rd = mk_rsrc(p.wd, FF * D * 2);
    const unsigned v = (lane >> 3) * (D * 2) + (lane & 7) * 16, s = f * 64 * (D * 2) + wave * 128;
#pragma unroll
    for (int t = DN_SECOND; t < 8; ++t) wd[t] = ldb(rd, v, s + t * 8 * (D * 2));
    __builtin_amdgcn_sched_barrier(0);
  }
  if (wave < 8) {
    PIN(h0); PIN(h1); PIN(h8);
    f32x4 dg = {0.f, 0.f, 0.f, 0.f}, du = {0.f, 0.f, 0.f, 0.f};
#pragma unroll
    for (int t = 0; t < 9; ++t) {
      const v4u a = afrag(t < 4 ? h0[t & 3] : t < 8 ? h1[t & 3] : h8, mk);
      dg = mfma(a, wg[t], dg);
      du = mfma(a, wu[t], du);
    }
    *(LAS f32x4*)(L + guw0) = dg;
    *(LAS f32x4*)(L + guw0 + ((lane >> 5) == ((lane >> 3) & 1) ? 512 : 0)) = du;
  }
  bar();
  if (wave < 4) {
    const LAS float* r0 = (const LAS float*)(L + gur);
    float g = faddn(fadd(r0[0], r0[64]), fadd(r0[260], r0[324])), u = faddn(fadd(r0[128], r0[192]), fadd(r0[388], r0[452]));
    g += dpp<XOR1>(g); u += dpp<XOR1>(u);
    g += dpp<XOR2>(g); u += dpp<XOR2>(u);
    g = rbf(g); u = rbf(u);
    const float y = g * fmaf(g * g, 0.044715f * 0.7978845608028654f * 2.f * LOG2E, 0.7978845608028654f * 2.f * LOG2E);
    const float th = fmaf(-2.f, rcp_f(__builtin_amdgcn_exp2f(y) + 1.f), 1.f);
    const float act = rbf(0.5f * g * (1.f + th)) * u;
    *(LAS unsigned*)(L + actw) = pkr(act);
  }
  bar();
  STAMP(9);
  {
    const LAS v4u* ap = (const LAS v4u*)(L + actr);
    const v4u a0 = ap[0], a1 = ap[1];
    f32x4 d0 = {0.f, 0.f, 0.f, 0.f}, d1 = {0.f, 0.f, 0.f, 0.f};
#pragma unroll
    for (int t = 0; t < 4; ++t) { d0 = mfma(afrag(a0[t], mk), wd[t], d0); d1 = mfma(afrag(a1[t], mk), wd[4 + t], d1); }
    *(LAS f32x4*)(L + trw) = d0 + d1;
    lds_wait();
    pubp(ddst, fadd(*(const LAS float*)(L + trr), *(const LAS float*)(L + trr + 256)), tag);
  }
  STAMP(10);
}

DEV void role_modb(const Params& p, LAS unsigned char* L, int x, int wave, int lane, u64 t0, u64 code0) {
  if (CODE_PF && wave == 8) {
    const unsigned dump = (unsigned)(uintptr_t)(L + L_PF);
#pragma unroll
    for (int k = 0; k < (CODE_PF + 8191) / 8192; ++k) {
      const unsigned o = k * 8192 + lane * 128;
      asm volatile("s_mov_b32 m0, %0\n\ts_nop 0\n\tglobal_load_lds_dword %1, off" :: "s"(dump), "v"(code0 + (o < CODE_PF - 128 ? o : CODE_PF - 128)) : "m0", "memory");
    }
  }
  if ((PAGE_TOUCH & 4) && wave == 7) {
    const int k = lane & 7, m = lane >> 3;
    const char* base = m == 0 ? (const char*)p.wo : m == 1 ? (const char*)p.wg : m == 2 ? (const char*)p.wu : (const char*)p.wd;
    const size_t off = (size_t)(k < 6 ? k : 5) * 917504;
    page_touch(L, m < 4 ? base + off : (const char*)p.out);
  }
  const Mask mk = make_mask(lane, true);
  if (wave == 0) tag_put(L, load_state(p.ws), lane);
  bar();
  const Tag tag = tag_get(L);
  gate_wait(t0, GATE_MODB);
  for (int k = wave; k < 12; k += 9) {
    ModA ma;
    moda_load(p, 120 + x + 8 * k, lane, ma);
    __builtin_amdgcn_sched_barrier(0);
    moda_finish(L, p.ws, 120 + x + 8 * k, lane, ma, mk, (LAS float*)(L + wave * 1024), tag);
  }
  STAMP(2);
}

__global__ void __launch_bounds__(NTHR) adaln_block_kernel(Params p) {
  __shared__ __attribute__((aligned(16))) unsigned char smem[L_END + L_EXTRA];
  LAS unsigned char* L = (LAS unsigned char*)smem;
  const int lane = threadIdx.x & 63, wave = __builtin_amdgcn_readfirstlane(threadIdx.x >> 6);
  u64 code0;
  asm volatile("s_getpc_b64 %0" : "=s"(code0));
  code0 &= ~0xFFull;
  const u64 t0 = __builtin_amdgcn_s_memrealtime();
  STAMP_INIT();
  STAMP(0);
  const int x = blockIdx.x & 7, idx = blockIdx.x >> 3;
  if (idx < 22) role_att(p, L, x, idx, wave, lane, t0);
  else if (idx < 31) role_ffn(p, L, x, idx - 22, wave, lane, t0);
  else role_modb(p, L, x, wave, lane, t0, code0);
  STAMP_DUMP();
}

extern "C" void kernel_launch(void* const* d_in, const int* in_sizes, int n_in, void* d_out, int out_size, void* d_ws,
                              size_t ws_size, hipStream_t stream) {
  Params p;
  p.x = (const u16*)d_in[0];
  p.kc = (const u16*)d_in[1];
  p.vc = (const u16*)d_in[2];
  p.wq = (const u16*)d_in[3];
  p.wo = (const u16*)d_in[4];
  p.wg = (const u16*)d_in[6];
  p.wu = (const u16*)d_in[7];
  p.wd = (const u16*)d_in[8];
  p.c = (const u16*)d_in[9];
  p.wmod = (const u16*)d_in[10];
  p.out = (u16*)d_out;
  p.ws = (u64*)d_ws;
  hipLaunchKernelGGL(adaln_block_kernel, dim3(256), dim3(NTHR), 0, stream, p);
}
```

```cpp
#include <hip/hip_runtime.h>
#include <stdint.h>

typedef unsigned short u16;
typedef unsigned long long u64;
typedef unsigned v4u __attribute__((ext_vector_type(4)));
typedef float f32x4 __attribute__((ext_vector_type(4)));
typedef float f32x2 __attribute__((ext_vector_type(2)));
typedef __bf16 bf16x2 __attribute__((ext_vector_type(2)));
typedef short bf16x8 __attribute__((ext_vector_type(8)));
typedef __amdgpu_buffer_rsrc_t rsrc_t;
#define GAS __attribute__((address_space(1)))
#define LAS __attribute__((address_space(3)))
#define DEV __device__ __forceinline__
typedef GAS u64 gu64;
#define RLX_AGENT __ATOMIC_RELAXED, __HIP_MEMORY_SCOPE_AGENT

constexpr int D = 576, SK = 65536, WIN = 4096, DH = 128, FF = 4608, NMOD = 6 * D;
constexpr int NSPLIT = 11;
constexpr int NTHR = 576;
constexpr int SPIN_CAP = 1 << 16;
constexpr unsigned GATE_ATT = 140;
constexpr unsigned GATE_MODB = 200;
constexpr unsigned GATE_MODB2 = 120;
constexpr unsigned GATE_H1 = 260;
constexpr unsigned GATE_Q = 450;
constexpr unsigned GATE_FFN = 790;
constexpr int Q_STAGGER = 16;
constexpr int ATT_N1 = 5, ATT_D1 = 2;
constexpr int DN_SECOND = 4;
constexpr int CODE_PF = 21504;

constexpr int G_EPOCH = 0;
constexpr int G_MOD = 64;
constexpr int G_Q = G_MOD + 4 * NMOD;
constexpr int G_O = G_Q + 32 * 2 * 128;
constexpr int G_WO = G_O + 16 * 8 * NSPLIT * 64;
constexpr int G_X2 = G_WO + 128 * 576;
constexpr int G_DN = G_X2 + 72 * 16;
constexpr int G_END = G_DN + 72 * 576;

constexpr int L_K = 0;
constexpr int L_OL = 98304;
constexpr int L_Q = L_OL + 9216;
constexpr int L_MX = L_Q + 512;
constexpr int L_LS = L_MX + 64;
constexpr int L_OB = L_LS + 64;
constexpr int L_PF = L_OB + 128;
constexpr int L_WO = L_PF + 256;
constexpr int L_MA = L_WO + 36864;
constexpr int L_CNT = L_MA + 1536;
constexpr int L_TAG = L_MA + 1536 + 16;
constexpr int L_END = L_TAG + 16;
constexpr int F_RED = 0;
constexpr int F_H = 16384;
constexpr int F_H2 = 17664;
constexpr int F_ACT = 20736;
constexpr int F_TR = 21248;
constexpr int F_ST = 27136;

struct Params {
  const u16 *x, *kc, *vc, *wq, *wo, *wg, *wu, *wd, *c, *wmod;
  u16* out;
  u64* ws;
};

DEV float bf2f(unsigned b) { return __uint_as_float(b << 16); }
DEV unsigned cvt2(float lo, float hi) {
  const f32x2 v = {lo, hi};
  return __builtin_bit_cast(unsigned, __builtin_convertvector(v, bf16x2));
}
DEV unsigned pkr(float f) { return cvt2(f, f); }
DEV float rbf(float f) { return __uint_as_float(cvt2(f, f) & 0xFFFF0000u); }
constexpr int TAG_LO_BITS = 4;
constexpr unsigned TAGM = (1u << TAG_LO_BITS) - 1u;
struct Tag { unsigned hi, lo, rest; };
DEV Tag make_tag(u64 st) {
  const u64 nx = st * 6364136223846793005ull + 1442695040888963407ull;
  Tag t;
  t.hi = __builtin_amdgcn_readfirstlane((unsigned)(nx >> 32));
  t.rest = __builtin_amdgcn_readfirstlane((unsigned)nx);
  t.lo = t.rest >> (32 - TAG_LO_BITS);
  return t;
}
DEV u64 enc(float v, unsigned hi, unsigned lo) {
  const unsigned u = (__float_as_uint(v) + (TAGM + 1) / 2) & ~TAGM;
  return ((u64)hi << 32) | (u64)(u | lo);
}
DEV Tag tag_put(LAS unsigned char* L, u64 st, int lane) {
  const Tag t = make_tag(st);
  if (lane == 0) *(LAS v4u*)(L + L_TAG) = v4u{t.hi, t.lo, t.rest, 0u};
  return t;
}
DEV Tag tag_get(const LAS unsigned char* L) {
  const v4u v = *(const LAS v4u*)(L + L_TAG);
  Tag t; t.hi = __builtin_amdgcn_readfirstlane(v[0]); t.lo = __builtin_amdgcn_readfirstlane(v[1]); t.rest = __builtin_amdgcn_readfirstlane(v[2]);
  return t;
}
DEV unsigned or_xor(unsigned a, unsigned b, unsigned c) { return __builtin_amdgcn_bitop3_b32(a, b, c, 0xF6); }
struct Chk {
  unsigned a = 0, b = 0;
  DEV void add(u64 g, const Tag& t) { a = or_xor(a, (unsigned)(g >> 32), t.hi); b = or_xor(b, (unsigned)g, t.lo); }
  DEV bool all_ok() const { return __builtin_amdgcn_ballot_w64((a | (b & TAGM)) != 0u) == 0ull; }
};
DEV float gval(u64 g) { return __uint_as_float((unsigned)g & ~TAGM); }
DEV void pub(u64* ws, int idx, float v, const Tag& t) { __hip_atomic_store((gu64*)ws + idx, enc(v, t.hi, t.lo), RLX_AGENT); }
DEV void pubp(gu64* dst, float v, const Tag& t) { __hip_atomic_store(dst, enc(v, t.hi, t.lo), RLX_AGENT); }
DEV u64 peek(u64* ws, int idx) { return __hip_atomic_load((gu64*)ws + idx, RLX_AGENT); }
template <int N, class F> DEV void gather(u64* ws, const Tag& t, u64 (&g)[N], F idx) {
  for (int spin = 0; spin < SPIN_CAP; ++spin) {
#pragma unroll
    for (int k = 0; k < N; ++k) g[k] = peek(ws, idx(k));
    Chk c;
#pragma unroll
    for (int k = 0; k < N; ++k) c.add(g[k], t);
    if (c.all_ok()) break;
  }
}

struct G2 { v4u v; DEV float lo() const { return __uint_as_float(v[0] & ~TAGM); } DEV float hi() const { return __uint_as_float(v[2] & ~TAGM); } };
template <int N, class F> DEV void gather2(rsrc_t rw, const Tag& t, G2 (&g)[N], F idx) {
  for (int spin = 0; spin < SPIN_CAP; ++spin) {
    asm volatile("" ::: "memory");
#pragma unroll
    for (int k = 0; k < N; ++k) g[k].v = __builtin_amdgcn_raw_buffer_load_b128(rw, idx(k) * 8, 0, 16);
    unsigned a = 0, b = 0;
#pragma unroll
    for (int k = 0; k < N; ++k) { a = or_xor(or_xor(a, g[k].v[1], t.hi), g[k].v[3], t.hi); b = or_xor(or_xor(b, g[k].v[0], t.lo), g[k].v[2], t.lo); }
    if (__builtin_amdgcn_ballot_w64((a | (b & TAGM)) != 0u) == 0ull) break;
  }
}
DEV void q_handoff(rsrc_t rw, unsigned voff, unsigned qdst, const Tag& t) {
  static_assert(TAG_LO_BITS == 4, "masks below");
  unsigned cnt;
  asm volatile(
      "s_mov_b32 %[cnt], %[cap]\n\t"
      "buffer_load_dwordx4 v[100:103], %[vo], %[rs], 0 offen sc1\n\t"
      "buffer_load_dwordx4 v[104:107], %[vo], %[rs], 0 offen offset:1024 sc1\n\t"
      "buffer_load_dwordx4 v[108:111], %[vo], %[rs], 0 offen offset:2048 sc1\n\t"
      "buffer_load_dwordx4 v[112:115], %[vo], %[rs], 0 offen offset:3072 sc1\n\t"
      "s_sleep %[stag]\n\t"
      "1:\n\t"
      "buffer_load_dwordx4 v[116:119], %[vo], %[rs], 0 offen sc1\n\t"
      "buffer_load_dwordx4 v[120:123], %[vo], %[rs], 0 offen offset:1024 sc1\n\t"
      "buffer_load_dwordx4 v[124:127], %[vo], %[rs], 0 offen offset:2048 sc1\n\t"
      "buffer_load_dwordx4 v[128:131], %[vo], %[rs], 0 offen offset:3072 sc1\n\t"
      "s_waitcnt vmcnt(4)\n\t"
      "v_xor_b32 v132, %[th], v101\n\t"
      "v_bitop3_b32 v132, v132, v103, %[th] bitop3:0xf6\n\t"
      "v_bitop3_b32 v132, v132, v105, %[th] bitop3:0xf6\n\t"
      "v_bitop3_b32 v132, v132, v107, %[th] bitop3:0xf6\n\t"
      "v_bitop3_b32 v132, v132, v109, %[th] bitop3:0xf6\n\t"
      "v_bitop3_b32 v132, v132, v111, %[th] bitop3:0xf6\n\t"
      "v_bitop3_b32 v132, v132, v113, %[th] bitop3:0xf6\n\t"
      "v_bitop3_b32 v132, v132, v115, %[th] bitop3:0xf6\n\t"
      "v_xor_b32 v133, %[tl], v100\n\t"
      "v_bitop3_b32 v133, v133, v102, %[tl] bitop3:0xf6\n\t"
      "v_bitop3_b32 v133, v133, v104, %[tl] bitop3:0xf6\n\t"
      "v_bitop3_b32 v133, v133, v106, %[tl] bitop3:0xf6\n\t"
      "v_bitop3_b32 v133, v133, v108, %[tl] bitop3:0xf6\n\t"
      "v_bitop3_b32 v133, v133, v110, %[tl] bitop3:0xf6\n\t"
      "v_bitop3_b32 v133, v133, v112, %[tl] bitop3:0xf6\n\t"
      "v_bitop3_b32 v133, v133, v114, %[tl] bitop3:0xf6\n\t"
      "v_and_b32 v133, 15, v133\n\t"
      "v_or_b32 v132, v132, v133\n\t"
      "v_cmp_ne_u32 vcc, 0, v132\n\t"
      "s_cbranch_vccz 2f\n\t"
      "buffer_load_dwordx4 v[100:103], %[vo], %[rs], 0 offen sc1\n\t"
      "buffer_load_dwordx4 v[104:107], %[vo], %[rs], 0 offen offset:1024 sc1\n\t"
      "buffer_load_dwordx4 v[108:111], %[vo], %[rs], 0 offen offset:2048 sc1\n\t"
      "buffer_load_dwordx4 v[112:115], %[vo], %[rs], 0 offen offset:3072 sc1\n\t"
      "s_waitcnt vmcnt(4)\n\t"
      "v_xor_b32 v132, %[th], v117\n\t"
      "v_bitop3_b32 v132, v132, v119, %[th] bitop3:0xf6\n\t"
      "v_bitop3_b32 v132, v132, v121, %[th] bitop3:0xf6\n\t"
      "v_bitop3_b32 v132, v132, v123, %[th] bitop3:0xf6\n\t"
      "v_bitop3_b32 v132, v132, v125, %[th] bitop3:0xf6\n\t"
      "v_bitop3_b32 v132, v132, v127, %[th] bitop3:0xf6\n\t"
      "v_bitop3_b32 v132, v132, v129, %[th] bitop3:0xf6\n\t"
      "v_bitop3_b32 v132, v132, v131, %[th] bitop3:0xf6\n\t"
      "v_xor_b32 v133, %[tl], v116\n\t"
      "v_bitop3_b32 v133, v133, v118, %[tl] bitop3:0xf6\n\t"
      "v_bitop3_b32 v133, v133, v120, %[tl] bitop3:0xf6\n\t"
      "v_bitop3_b32 v133, v133, v122, %[tl] bitop3:0xf6\n\t"
      "v_bitop3_b32 v133, v133, v124, %[tl] bitop3:0xf6\n\t"
      "v_bitop3_b32 v133, v133, v126, %[tl] bitop3:0xf6\n\t"
      "v_bitop3_b32 v133, v133, v128, %[tl] bitop3:0xf6\n\t"
      "v_bitop3_b32 v133, v133, v130, %[tl] bitop3:0xf6\n\t"
      "v_and_b32 v133, 15, v133\n\t"
      "v_or_b32 v132, v132, v133\n\t"
      "v_cmp_ne_u32 vcc, 0, v132\n\t"
      "s_cbranch_vccz 3f\n\t"
      "s_sub_u32 %[cnt], %[cnt], 1\n\t"
      "s_cbranch_scc0 1b\n\t"
      "2:\n\t"
      "v_and_b32 v132, 0xfffffff0, v100\n\t"
      "v_and_b32 v133, 0xfffffff0, v104\n\t"
      "v_add_f32 v132, v132, v133\n\t"
      "v_and_b32 v133, 0xfffffff0, v102\n\t"
      "v_and_b32 v136, 0xfffffff0, v106\n\t"
      "v_add_f32 v133, v133, v136\n\t"
      "v_cvt_pk_bf16_f32 v132, v132, v133\n\t"
      "ds_write_b32 %[qd], v132\n\t"
      "v_and_b32 v134, 0xfffffff0, v108\n\t"
      "v_and_b32 v135, 0xfffffff0, v112\n\t"
      "v_add_f32 v134, v134, v135\n\t"
      "v_and_b32 v135, 0xfffffff0, v110\n\t"
      "v_and_b32 v136, 0xfffffff0, v114\n\t"
      "v_add_f32 v135, v135, v136\n\t"
      "v_cvt_pk_bf16_f32 v134, v134, v135\n\t"
      "ds_write_b32 %[qd], v134 offset:256\n\t"
      "s_branch 4f\n\t"
      "3:\n\t"
      "v_and_b32 v132, 0xfffffff0, v116\n\t"
      "v_and_b32 v133, 0xfffffff0, v120\n\t"
      "v_add_f32 v132, v132, v133\n\t"
      "v_and_b32 v133, 0xfffffff0, v118\n\t"
      "v_and_b32 v136, 0xfffffff0, v122\n\t"
      "v_add_f32 v133, v133, v136\n\t"
      "v_cvt_pk_bf16_f32 v132, v132, v133\n\t"
      "ds_write_b32 %[qd], v132\n\t"
      "v_and_b32 v134, 0xfffffff0, v124\n\t"
      "v_and_b32 v135, 0xfffffff0, v128\n\t"
      "v_add_f32 v134, v134, v135\n\t"
      "v_and_b32 v135, 0xfffffff0, v126\n\t"
      "v_and_b32 v136, 0xfffffff0, v130\n\t"
      "v_add_f32 v135, v135, v136\n\t"
      "v_cvt_pk_bf16_f32 v134, v134, v135\n\t"
      "ds_write_b32 %[qd], v134 offset:256\n\t"
      "4:\n\t"
      "s_waitcnt lgkmcnt(0)\n\t"
      "s_barrier\n\t"
      "s_waitcnt vmcnt(0)"
      : [cnt] "=&s"(cnt)
      : [vo] "v"(voff), [rs] "s"(rw), [th] "s"(t.hi), [tl] "s"(t.lo), [qd] "v"(qdst), [cap] "n"(SPIN_CAP / 2), [stag] "n"(Q_STAGGER)
      : "vcc", "scc", "memory", "v100", "v101", "v102", "v103", "v104", "v105", "v106", "v107", "v108", "v109", "v110", "v111", "v112", "v113", "v114", "v115", "v116", "v117", "v118", "v119", "v120", "v121", "v122", "v123", "v124", "v125", "v126", "v127", "v128", "v129", "v130", "v131", "v132", "v133", "v134", "v135", "v136");
}
template <int N, class F> DEV void pretouch(LAS unsigned char* L, u64* ws, F idx) {
  const unsigned dump = (unsigned)(uintptr_t)(L + L_PF);
#pragma unroll
  for (int k = 0; k < N; ++k)
    asm volatile("s_mov_b32 m0, %0\n\ts_nop 0\n\tglobal_load_lds_dword %1, off sc1" :: "s"(dump), "v"((gu64*)ws + idx(k)) : "m0", "memory");
}
DEV f32x4 mfma(v4u a, v4u b, f32x4 c) {
  return __builtin_amdgcn_mfma_f32_16x16x32_bf16(__builtin_bit_cast(bf16x8, a), __builtin_bit_cast(bf16x8, b), c, 0, 0, 0);
}
DEV rsrc_t mk_rsrc(const void* p, unsigned bytes) { return __builtin_amdgcn_make_buffer_rsrc((void*)p, 0, (int)bytes, 0x00020000); }
DEV v4u ldb(rsrc_t r, unsigned voff, unsigned soff) { return __builtin_amdgcn_raw_buffer_load_b128(r, (int)voff, (int)soff, 2); }
DEV unsigned ldb16(rsrc_t r, unsigned voff) { return __builtin_amdgcn_raw_buffer_load_b16(r, (int)voff, 0, 0); }
DEV u64 load_state(u64* ws) {
  int zoff = G_EPOCH * 8;
  asm volatile("" : "+v"(zoff));
  typedef unsigned v2u_ __attribute__((ext_vector_type(2)));
  const v2u_ v = __builtin_amdgcn_raw_buffer_load_b64(mk_rsrc(ws, 64), zoff, 0, 16);
  return ((u64)v[1] << 32) | v[0];
}
DEV void bar() {
  asm volatile("s_waitcnt lgkmcnt(0)" ::: "memory");
  __builtin_amdgcn_s_barrier();
  asm volatile("" ::: "memory");
}
#define PIN(x) asm volatile("" : "+v"(x))
DEV void lds_wait() { asm volatile("s_waitcnt lgkmcnt(0)" ::: "memory"); }
template <int CTRL> DEV float dpp(float x) {
  return __builtin_bit_cast(float, __builtin_amdgcn_mov_dpp(__builtin_bit_cast(int, x), CTRL, 0xf, 0xf, true));
}
constexpr int ROR8 = 0x128, ROR4 = 0x124, ROR2 = 0x122, ROR1 = 0x121, XOR1 = 0xB1, XOR2 = 0x4E;
DEV float fmx(float a, float b) { float r; asm("v_max_f32 %0, %1, %2\n\ts_nop 1" : "=v"(r) : "v"(a), "v"(b)); return r; }
DEV float fmx3(float a, float b, float c) { float r; asm("v_max3_f32 %0, %1, %2, %3" : "=v"(r) : "v"(a), "v"(b), "v"(c)); return r; }
DEV float fmx3n(float a, float b, float c) { float r; asm("v_max3_f32 %0, %1, %2, %3\n\ts_nop 1" : "=v"(r) : "v"(a), "v"(b), "v"(c)); return r; }
DEV float x16_sum(float x) {
  auto s = __builtin_amdgcn_permlane16_swap(__float_as_uint(x), __float_as_uint(x), false, false);
  x = __uint_as_float(s[0]) + __uint_as_float(s[1]);
  auto t = __builtin_amdgcn_permlane32_swap(__float_as_uint(x), __float_as_uint(x), false, false);
  return __uint_as_float(t[0]) + __uint_as_float(t[1]);
}
DEV float x16_max(float x) {
  auto s = __builtin_amdgcn_permlane16_swap(__float_as_uint(x), __float_as_uint(x), false, false);
  x = fmaxf(__uint_as_float(s[0]), __uint_as_float(s[1]));
  auto t = __builtin_amdgcn_permlane32_swap(__float_as_uint(x), __float_as_uint(x), false, false);
  return fmaxf(__uint_as_float(t[0]), __uint_as_float(t[1]));
}
DEV float row_sum(float x) { x += dpp<ROR8>(x); x += dpp<ROR4>(x); x += dpp<ROR2>(x); x += dpp<ROR1>(x); return x; }
DEV float wave_sum(float x) { return x16_sum(row_sum(x)); }
constexpr float LOG2E = 1.4426950408889634f;
DEV float fadd(float a, float b) { float r; asm("v_add_f32 %0, %1, %2" : "=v"(r) : "v"(a), "v"(b)); return r; }
DEV float faddn(float a, float b) { float r; asm("v_add_f32 %0, %1, %2\n\ts_nop 1" : "=v"(r) : "v"(a), "v"(b)); return r; }
DEV float exp_f(float x) { return __builtin_amdgcn_exp2f(x * LOG2E); }
DEV float rcp_f(float x) { return __builtin_amdgcn_rcpf(x); }
DEV float pair_pick(float a, float b, int lane) {
  const float a2 = dpp<0x50>(a), b2 = dpp<0x50>(b);
  const float a3 = dpp<0xFA>(a), b3 = dpp<0xFA>(b);
  const float lo = (lane & 1) ? b2 : a2, hi = (lane & 1) ? b3 : a3;
  return (lane & 4) ? hi : lo;
}
DEV void gate_wait(u64 t0, unsigned ticks) { while (__builtin_amdgcn_s_memrealtime() - t0 < (u64)ticks) __builtin_amdgcn_s_sleep(2); }

struct Mask { unsigned m[4]; };
DEV Mask make_mask(int lane, bool on) {
  Mask k;
  const unsigned h = on ? ((lane & 1) ? 0xFFFF0000u : 0x0000FFFFu) : 0u;
#pragma unroll
  for (int r = 0; r < 4; ++r) k.m[r] = (((lane & 7) >> 1) == r) ? h : 0u;
  return k;
}
DEV v4u afrag(unsigned pp, const Mask& k) { return v4u{pp & k.m[0], pp & k.m[1], pp & k.m[2], pp & k.m[3]}; }

DEV unsigned ln_apply_pk(float x, float mean_b, float rden, float c1, float shift) {
  const float ln = rbf(rbf(x - mean_b) * rden);
  return pkr(rbf(ln * c1) + shift);
}
DEV float ln_rden(float var) { return rcp_f(rbf(__builtin_amdgcn_sqrtf(rbf(rbf(var) + 1e-5f)))); }

DEV unsigned red_off(int lane, int dump) {
  const int u = (lane >> 3) & 1;
  return 4 * ((lane >> 5) == u ? u * 64 + 8 * (lane & 7) + 4 * ((lane >> 4) & 1) : dump);
}
DEV unsigned silu_pk(unsigned craw) { const float v = bf2f(craw); return pkr(v * rcp_f(1.f + exp_f(-v))); }

struct WoAddr { unsigned tro, tri, obo, wro; gu64* dst; };
DEV WoAddr wo_prep(u64* ws, int pidx, int wave, int lane) {
  WoAddr a;
  a.tro = L_OL + wave * 640 + red_off(lane, 128); a.tri = L_OL + wave * 640 + lane * 4; a.obo = L_OB + (lane >> 3) * 16; a.wro = L_WO + wave * 4096 + lane * 16;
  a.dst = (gu64*)ws + G_WO + pidx * 576 + wave * 64 + lane;
  PIN(a.tro); PIN(a.tri); PIN(a.obo); PIN(a.wro); PIN(a.dst);
  return a;
}
DEV void wo_phase(LAS unsigned char* L, const WoAddr& a, const Tag& tag, const Mask& mk) {
  bar();
  v4u ob = *(const LAS v4u*)(L + a.obo);
  const LAS v4u* wr = (const LAS v4u*)(L + a.wro);
  v4u w0 = wr[0], w1 = wr[64], w2 = wr[128], w3 = wr[192];
  PIN(ob); PIN(w0); PIN(w1); PIN(w2); PIN(w3);
  __builtin_amdgcn_sched_barrier(0);
  f32x4 d0 = {0.f, 0.f, 0.f, 0.f}, d1 = {0.f, 0.f, 0.f, 0.f};
  d0 = mfma(afrag(ob[0], mk), w0, d0); d1 = mfma(afrag(ob[1], mk), w1, d1);
  d0 = mfma(afrag(ob[2], mk), w2, d0); d1 = mfma(afrag(ob[3], mk), w3, d1);
  *(LAS f32x4*)(L + a.tro) = d0 + d1;
  lds_wait();
  pubp(a.dst, fadd(*(const LAS float*)(L + a.tri), *(const LAS float*)(L + a.tri + 256)), tag);
}

struct ModA { v4u w[18]; unsigned c[3]; };
DEV void moda_load(const Params& p, int mt, int lane, ModA& m) {
  const rsrc_t rm = mk_rsrc(p.wmod, D * NMOD * 2), rc = mk_rsrc(p.c, D * 2);
  const int r0 = (mt & 3) * 144;
#pragma unroll
  for (int k = 0; k < 3; ++k) m.c[k] = ldb16(rc, (r0 + (k < 2 || lane < 16 ? lane + 64 * k : 143)) * 2);
  const unsigned v = (r0 + (lane >> 3)) * (NMOD * 2) + (lane & 7) * 16;
#pragma unroll
  for (int t = 0; t < 18; ++t) m.w[t] = ldb(rm, v, (mt >> 2) * 128 + t * 8 * (NMOD * 2));
}
DEV void moda_finish(LAS unsigned char* L, u64* ws, int mt, int lane, ModA& m, const Mask& mk, LAS float* tr, const Tag& tag) {
  unsigned sp[18];
  {
    const unsigned s0 = silu_pk(m.c[0]), s1 = silu_pk(m.c[1]), s2 = silu_pk(m.c[2]);
#pragma unroll
    for (int t = 0; t < 18; ++t) sp[t] = __builtin_amdgcn_ds_bpermute(((8 * t + (lane >> 3)) & 63) * 4, t < 8 ? s0 : t < 16 ? s1 : s2);
  }
  gu64* dst = (gu64*)ws + G_MOD + (mt & 3) * NMOD + (mt >> 2) * 64 + lane;
  unsigned tro = red_off(lane, 128);
  PIN(dst); PIN(tro);
#pragma unroll
  for (int t = 0; t < 18; ++t) PIN(sp[t]);
  __builtin_amdgcn_sched_barrier(0);
  f32x4 d0 = {0.f, 0.f, 0.f, 0.f}, d1 = {0.f, 0.f, 0.f, 0.f};
#pragma unroll
  for (int t = 0; t < 18; t += 2) {
    d0 = mfma(afrag(sp[t], mk), m.w[t], d0);
    __builtin_amdgcn_sched_barrier(0);
    d1 = mfma(afrag(sp[t + 1], mk), m.w[t + 1], d1);
    __builtin_amdgcn_sched_barrier(0);
  }
  *(LAS f32x4*)((LAS unsigned char*)tr + tro) = d0 + d1;
  lds_wait();
  pubp(dst, fadd(tr[lane], tr[64 + lane]), tag);
}

DEV void role_att(const Params& p, LAS unsigned char* L, int x, int i, int wave, int lane, u64 t0) {
  const int g = 2 * x + (i >= NSPLIT ? 1 : 0), j = i >= NSPLIT ? i - NSPLIT : i;
  const bool odd = g & 1;
  const int nm = odd ? 4 : 5;
  const bool moda = j < nm;
  const int ntile = !moda ? 24 : (!odd && j >= 3) ? 23 : 22;
  const int tile0 = !moda ? (odd ? 88 : 112) + 24 * (j - nm) : (!odd && j == 4) ? 89 : 22 * j;
  const bool owner = j < 8;
  int redc = -1;
  if (j >= 8) redc = 9 * x + (g & 1) * 3 + (j - 8);
  else if (j == 0) redc = 9 * x + 6 + (g & 1);
  else if (j == 1 && (g & 1) == 0) redc = 9 * x + 8;
  u64* ws = p.ws;
  const Mask mk = make_mask(lane, true);
  const int kap = lane >> 4, cc = lane & 15;

  if (wave < 8) {
    const size_t hb = ((size_t)g * SK + (SK - WIN)) * DH;
    const rsrc_t rk = mk_rsrc(p.kc + hb, WIN * DH * 2), rv = mk_rsrc(p.vc + hb, WIN * DH * 2);
    const bool three = wave + 16 < ntile;
    const unsigned tso = (tile0 + wave) * 4096;
    const unsigned ldsk = (unsigned)(uintptr_t)(L + L_K) + wave * 3 * 4096;
    unsigned vk[4];
#pragma unroll
    for (int q = 0; q < 4; ++q) vk[q] = (4 * q + kap) * 256 + ((cc ^ (4 * q + kap)) << 4);
#define KDMA(s, q) asm volatile("s_mov_b32 m0, %0\n\ts_nop 0\n\tbuffer_load_dwordx4 %1, %2, %3 offen nt lds" \
                     :: "s"(ldsk + (s) * 4096 + (q) * 1024), "v"(vk[q]), "s"(rk), "s"(tso + (s) * 32768) : "m0", "memory")
#define THR(n) do { if ((n) < ATT_N1 && (n) >= ATT_D1 && moda) asm volatile("s_waitcnt vmcnt(%0)" :: "n"(ATT_D1 - 1) : "memory"); } while (0)
    if (moda) __builtin_amdgcn_s_barrier();
    __builtin_amdgcn_s_setprio(2);
    KDMA(0, 0);
    gate_wait(t0, GATE_ATT);
    THR(1); KDMA(0, 1); THR(2); KDMA(0, 2); THR(3); KDMA(0, 3);
    THR(4); KDMA(1, 0); KDMA(1, 1); KDMA(1, 2); KDMA(1, 3);
    if (three) { KDMA(2, 0); KDMA(2, 1); KDMA(2, 2); KDMA(2, 3); }
#undef KDMA
#undef THR
    __builtin_amdgcn_s_setprio(0);
    v4u vr[12];
    const unsigned vv = kap * 1024 + cc * 16;
#pragma unroll
    for (int t = 0; t < 8; ++t) vr[t] = ldb(rv, vv + (t & 3) * 256, tso + (t >> 2) * 32768);
    if (three) {
#pragma unroll
      for (int t = 8; t < 12; ++t) vr[t] = ldb(rv, vv + (t & 3) * 256, tso + 65536);
    } else {
#pragma unroll
      for (int t = 8; t < 12; ++t) vr[t] = v4u{0u, 0u, 0u, 0u};
    }
    __builtin_amdgcn_sched_barrier(0);
    if (three) asm volatile("s_waitcnt vmcnt(12)" ::: "memory");
    else asm volatile("s_waitcnt vmcnt(8)" ::: "memory");
    v4u kf[12];
#pragma unroll
    for (int s = 0; s < 3; ++s)
#pragma unroll
      for (int ks = 0; ks < 4; ++ks)
        kf[s * 4 + ks] = *(const LAS v4u*)(L + L_K + (wave * 3 + (s < 2 || three ? s : 0)) * 4096 + cc * 256 + (((4 * ks + kap) ^ cc) << 4));
#pragma unroll
    for (int t = 0; t < 12; ++t) PIN(kf[t]);
    const int head = (lane >> 3) & 1;
    const int me = (wave & 3) * 64 + lane;
    unsigned qsrc = L_Q + head * 256 + kap * 16, mxw = L_MX + (head * 8 + wave) * 4, mxr = L_MX + head * 32, olw = L_OL + wave * 1024 + lane * 16;
    unsigned msrc = L_OL + ((16 * (((me >> 7) * 8 + (me & 7)) >> 2) + ((me & 127) >> 3)) * 4 + (me & 3)) * 4;
    gu64* mdst = (gu64*)ws + G_O + ((g * 8 + (me >> 5)) * NSPLIT + j) * 64 + (me & 31);
    bool lead = (lane & 0x37) == 0;
    PIN(qsrc); PIN(mxw); PIN(mxr); PIN(olw); PIN(msrc); PIN(mdst);
    bar();
    v4u qf[4];
#pragma unroll
    for (int ks = 0; ks < 4; ++ks) qf[ks] = *(const LAS v4u*)(L + qsrc + ks * 64);
#pragma unroll
    for (int ks = 0; ks < 4; ++ks) PIN(qf[ks]);
    __builtin_amdgcn_sched_barrier(0);
    f32x4 acc[3] = {{0.f, 0.f, 0.f, 0.f}, {0.f, 0.f, 0.f, 0.f}, {0.f, 0.f, 0.f, 0.f}};
#pragma unroll
    for (int ks = 0; ks < 4; ++ks)
#pragma unroll
      for (int s = 0; s < 3; ++s) acc[s] = mfma(kf[s * 4 + ks], qf[ks], acc[s]);
    __builtin_amdgcn_sched_barrier(0);
    float sc[12];
#pragma unroll
    for (int t = 0; t < 12; t += 2) {
      const unsigned u = cvt2(acc[t >> 2][t & 3], acc[t >> 2][(t & 3) + 1]);
      const unsigned w = cvt2(__uint_as_float(u << 16) * 0.08838834764831845f, __uint_as_float(u & 0xFFFF0000u) * 0.08838834764831845f);
      sc[t] = __uint_as_float(w << 16);
      sc[t + 1] = __uint_as_float(w & 0xFFFF0000u);
    }
    if (!three) {
#pragma unroll
      for (int t = 8; t < 12; ++t) sc[t] = -INFINITY;
    }
    float mx = fmx3n(fmx3(fmx3(sc[0], sc[1], sc[2]), fmx3(sc[3], sc[4], sc[5]), fmx3(sc[6], sc[7], sc[8])), fmx3(sc[9], sc[10], sc[11]), sc[0]);
    mx = x16_max(mx);
    if (lead) *(LAS float*)(L + mxw) = mx;
    lds_wait();
    if (lane == 0) __hip_atomic_fetch_add((LAS unsigned*)(L + L_CNT), 1u, __ATOMIC_RELAXED, __HIP_MEMORY_SCOPE_WORKGROUP);
    while (*(volatile LAS unsigned*)(L + L_CNT) < 8u) {}
    asm volatile("" ::: "memory");
    float m;
    {
      const f32x4 a = *(const LAS f32x4*)(L + mxr), b = *(const LAS f32x4*)(L + mxr + 16);
      m = fmx3(fmx3(a[0], a[1], a[2]), fmx3(a[3], b[0], b[1]), fmx(b[2], b[3]));
    }
    v4u af[12];
    float ls0 = 0.f, ls1 = 0.f;
    const float mneg = -m * LOG2E;
#pragma unroll
    for (int t = 0; t < 12; t += 2) {
      const float e0 = __builtin_amdgcn_exp2f(fmaf(sc[t], LOG2E, mneg)), e1 = __builtin_amdgcn_exp2f(fmaf(sc[t + 1], LOG2E, mneg));
      ls0 += e0; ls1 += e1;
      af[t] = afrag(pkr(e0), mk); af[t + 1] = afrag(pkr(e1), mk);
    }
    const float ls = x16_sum(ls0 + ls1);
    if (lead) *(LAS float*)(L + mxw + 64) = ls;
    const Tag tag = tag_get(L);
    __builtin_amdgcn_sched_barrier(0);
    f32x4 o0 = {0.f, 0.f, 0.f, 0.f}, o1 = {0.f, 0.f, 0.f, 0.f};
#pragma unroll
    for (int t = 0; t < 12; t += 2) { o0 = mfma(af[t], vr[t], o0); o1 = mfma(af[t + 1], vr[t + 1], o1); }
    *(LAS f32x4*)(L + olw) = o0 + o1;
    bar();
    if (wave < 4) {
      const LAS float* src = (const LAS float*)(L + msrc);
      float s[8];
#pragma unroll
      for (int w = 0; w < 8; ++w) s[w] = src[w * 256];
      pubp(mdst, fadd(fadd(fadd(s[0], s[1]), fadd(s[2], s[3])), fadd(fadd(s[4], s[5]), fadd(s[6], s[7]))), tag);
    } else if (wave == 4 && lane < 16) {
      const int u = lane >> 1, hh = u >> 2;
      const f32x4 a = *(const LAS f32x4*)(L + ((lane & 1) ? L_LS : L_MX) + hh * 32), b = *(const LAS f32x4*)(L + ((lane & 1) ? L_LS : L_MX) + hh * 32 + 16);
      const float vs = ((a[0] + a[1]) + (a[2] + a[3])) + ((b[0] + b[1]) + (b[2] + b[3]));
      const float vm = fmaxf(fmaxf(fmaxf(a[0], a[1]), fmaxf(a[2], a[3])), fmaxf(fmaxf(b[0], b[1]), fmaxf(b[2], b[3])));
      pub(ws, G_O + ((g * 8 + u) * NSPLIT + j) * 64 + 32 + (lane & 1), (lane & 1) ? vs : vm, tag);
    }
    if (owner) {
      wo_phase(L, wo_prep(ws, g * 8 + j, wave, lane), tag, mk);
    }
  } else {
    __builtin_amdgcn_s_setprio(3);
    Tag tag;
    {
      if (moda) {
        u64 st = load_state(ws);
        ModA ma;
        moda_load(p, 9 * x + (odd ? 5 : 0) + j, lane, ma);
        __builtin_amdgcn_sched_barrier(0);
        __builtin_amdgcn_s_barrier();
        asm volatile("" : "+v"(st));
        tag = tag_put(L, st, lane);
        moda_finish(L, ws, 9 * x + (odd ? 5 : 0) + j, lane, ma, mk, (LAS float*)(L + L_MA), tag);
      } else {
        tag = tag_put(L, load_state(ws), lane);
      }
    }
    float xr = 0.f;
    if (redc >= 0) xr = bf2f(p.x[redc * 8 + (lane & 7)]);
    gate_wait(t0, GATE_Q);
    const rsrc_t rw = mk_rsrc(ws, G_END * 8);
    if (lane == 0) *(volatile LAS unsigned*)(L + L_CNT) = 0u;
    q_handoff(rw, (G_Q + 2 * g * 256 + 2 * lane) * 8, (unsigned)(uintptr_t)(L + L_Q) + lane * 4, tag);
    if (owner) {
      const int ob = G_O + (g * 8 + j) * NSPLIT * 64;
      pretouch<1>(L, ws, [&](int) { const int q_ = lane < 33 ? lane : 32; return ob + (q_ / 3) * 64 + (q_ % 3) * 16; });
    }
    if (redc >= 0) {
      pretouch<2>(L, ws, [&](int s) { return G_WO + (s * 64 + lane) * 576 + redc * 8; });
      pretouch<2>(L, ws, [&](int s) { const int f_ = s * 64 + lane; return G_DN + (f_ < 72 ? f_ : 71) * 576 + redc * 8; });
    }
    if (owner) {
      const rsrc_t rwo = mk_rsrc(p.wo, 4096 * D * 2);
      const unsigned wo_v = (lane >> 3) * (D * 2) + (lane & 7) * 16;
      const unsigned wo_s = (g * 256 + j * 32) * (D * 2), ldsw = (unsigned)(uintptr_t)(L + L_WO);
#pragma unroll
      for (int w = 0; w < 9; ++w)
#pragma unroll
        for (int t = 0; t < 4; ++t)
          asm volatile("s_mov_b32 m0, %0\n\ts_nop 0\n\tbuffer_load_dwordx4 %1, %2, %3 offen nt lds"
                       :: "s"(ldsw + (w * 4 + t) * 1024), "v"(wo_v), "s"(rwo), "s"(wo_s + w * 128 + t * 8 * (D * 2)) : "m0", "memory");
    }
    bar();
    if (owner) {
      const int ob = G_O + (g * 8 + j) * NSPLIT * 64;
      const int k2 = kap < 3 ? 8 + kap : 10;
      unsigned obw = L_OB + (((2 * cc) & 7) * 4 + (cc >> 2)) * 4;
      float live = kap < 3 ? 1.f : 0.f;
      PIN(obw); PIN(live);
      const WoAddr wa = wo_prep(ws, g * 8 + j, 8, lane);
      G2 go[6];
      gather2<6>(rw, tag, go, [&](int k) { const int sp = k % 3 == 0 ? kap : k % 3 == 1 ? 4 + kap : k2; return ob + sp * 64 + (k < 3 ? 2 * cc : 32); });
      const float M = x16_max(fmaxf(fmaxf(go[3].lo(), go[4].lo()), go[5].lo()));
      const float Mn = -M * LOG2E;
      const float w0 = __builtin_amdgcn_exp2f(fmaf(go[3].lo(), LOG2E, Mn)), w1 = __builtin_amdgcn_exp2f(fmaf(go[4].lo(), LOG2E, Mn));
      const float w2 = __builtin_amdgcn_exp2f(fmaf(go[5].lo(), LOG2E, Mn)) * live;
      const float Ls = x16_sum(fmaf(w2, go[5].hi(), fmaf(w1, go[4].hi(), w0 * go[3].hi())));
      const float o0 = x16_sum(fmaf(w2, go[2].lo(), fmaf(w1, go[1].lo(), w0 * go[0].lo())));
      const float o1 = x16_sum(fmaf(w2, go[2].hi(), fmaf(w1, go[1].hi(), w0 * go[0].hi())));
      const float rl = rcp_f(Ls);
      *(LAS unsigned*)(L + obw) = pkr(o0 * rl);
      *(LAS unsigned*)(L + obw + 16) = pkr(o1 * rl);
      asm volatile("s_waitcnt vmcnt(0)" ::: "memory");
      wo_phase(L, wa, tag, mk);
    }
    if (redc >= 0) {
      u64 gg[8];
      gather<8>(ws, tag, gg, [&](int k) { return G_MOD + (k & 3) * NMOD + (k < 4 ? 2 : 5) * D + redc * 8 + (lane & 7); });
      float ga = rbf(fadd(fadd(gval(gg[0]), gval(gg[1])), fadd(gval(gg[2]), gval(gg[3]))));
      float gb = rbf(fadd(fadd(gval(gg[4]), gval(gg[5])), fadd(gval(gg[6]), gval(gg[7]))));
      gu64* xdst = (gu64*)ws + G_X2 + redc * 16 + (lane & 7);
      GAS u16* odst = (GAS u16*)p.out + redc * 8 + (lane & 7);
      PIN(ga); PIN(gb); PIN(xr); PIN(xdst); PIN(odst);
      float x2;
      {
        G2 gw[8];
        gather2<8>(rw, tag, gw, [&](int s) { return G_WO + (s * 16 + (lane >> 2)) * 576 + redc * 8 + 2 * (lane & 3); });
        float t0_ = faddn(fadd(fadd(gw[0].lo(), gw[1].lo()), fadd(gw[2].lo(), gw[3].lo())), fadd(fadd(gw[4].lo(), gw[5].lo()), fadd(gw[6].lo(), gw[7].lo())));
        float t1_ = faddn(fadd(fadd(gw[0].hi(), gw[1].hi()), fadd(gw[2].hi(), gw[3].hi())), fadd(fadd(gw[4].hi(), gw[5].hi()), fadd(gw[6].hi(), gw[7].hi())));
        t0_ += dpp<ROR8>(t0_); t1_ += dpp<ROR8>(t1_); t0_ += dpp<ROR4>(t0_); t1_ += dpp<ROR4>(t1_);
        t0_ = x16_sum(t0_); t1_ = x16_sum(t1_);
        const float ts = pair_pick(t0_, t1_, lane);
        x2 = rbf(xr + rbf(ga * rbf(ts)));
        if (lane < 8) pubp(xdst, x2, tag);
      }
      {
        G2 gd[5];
        float w4 = lane < 32 ? 1.f : 0.f;
        PIN(w4);
        gather2<5>(rw, tag, gd, [&](int s) { const int f_ = s * 16 + (lane >> 2); return G_DN + (f_ < 72 ? f_ : 71) * 576 + redc * 8 + 2 * (lane & 3); });
        float t0_ = fmaf(w4, gd[4].lo(), fadd(fadd(gd[0].lo(), gd[1].lo()), fadd(gd[2].lo(), gd[3].lo())));
        float t1_ = fmaf(w4, gd[4].hi(), fadd(fadd(gd[0].hi(), gd[1].hi()), fadd(gd[2].hi(), gd[3].hi())));
        t0_ += dpp<ROR8>(t0_); t1_ += dpp<ROR8>(t1_); t0_ += dpp<ROR4>(t0_); t1_ += dpp<ROR4>(t1_);
        t0_ = x16_sum(t0_); t1_ = x16_sum(t1_);
        const float ts = pair_pick(t0_, t1_, lane);
        const unsigned o = pkr(x2 + rbf(gb * rbf(ts)));
        if (lane < 8) *odst = (u16)o;
      }
    }
  }
}

DEV void role_ffn(const Params& p, LAS unsigned char* L, int x, int i, int wave, int lane, u64 t0) {
  const int f = 9 * x + i;
  u64* ws = p.ws;
  const Mask mk = make_mask(lane, true);
  LAS float* red = (LAS float*)(L + F_RED);
  const rsrc_t rm = mk_rsrc(p.wmod, D * NMOD * 2), rc = mk_rsrc(p.c, D * 2), rx = mk_rsrc(p.x, D * 2);
  const int hq = 4 * x + (i >> 1), qr = i & 1;
  const rsrc_t rw = mk_rsrc(ws, G_END * 8);
  Tag tag;
  if (i < 8) {
    v4u wa[12];
    if (wave >= 3) {
      const rsrc_t rq = mk_rsrc(p.wq, D * 4096 * 2);
      const unsigned v = (lane >> 4) * 8192 + (lane & 15) * 16, s = (qr * 288 + (wave - 3) * 48) * 8192 + hq * 256;
#pragma unroll
      for (int t = 0; t < 12; ++t) wa[t] = ldb(rq, v, s + t * 4 * 8192);
      __builtin_amdgcn_sched_barrier(0);
    } else {
      __builtin_amdgcn_s_setprio(3);
      const u64 st = load_state(ws);
      float xv[9];
#pragma unroll
      for (int k = 0; k < 9; ++k) xv[k] = bf2f(ldb16(rx, (lane + 64 * k) * 2));
      const int p0 = wave * 64 + lane, pr = p0 < 144 ? p0 : 143;
      const unsigned xo = __builtin_amdgcn_raw_buffer_load_b32(rx, (qr * 288 + 2 * pr) * 2, 0, 0);
      float s = 0.f;
#pragma unroll
      for (int k = 0; k < 9; ++k) s += xv[k];
      const float mf = wave_sum(s) * (1.f / D);
      float v = 0.f;
#pragma unroll
      for (int k = 0; k < 9; ++k) { const float d = xv[k] - mf; v = fmaf(d, d, v); }
      const float rden = ln_rden(wave_sum(v) * (1.f / D)), mean = rbf(mf);
      tag = wave == 0 ? tag_put(L, st, lane) : make_tag(st);
      const int hw = pr / 24, hr = 2 * pr - hw * 48;
      unsigned hdst = F_H + ((hw * 4 + (hr & 3)) * 12 + (hr >> 2)) * 4;
      float ln0 = rbf(rbf(__uint_as_float(xo << 16) - mean) * rden), ln1 = rbf(rbf(__uint_as_float(xo & 0xFFFF0000u) - mean) * rden);
      PIN(hdst); PIN(ln0); PIN(ln1);
      gate_wait(t0, GATE_H1);
      G2 ga[8];
      gather2<8>(rw, tag, ga, [&](int k) { return G_MOD + (k & 3) * NMOD + (k >> 2) * D + qr * 288 + 2 * pr; });
      const float sa0 = rbf(fadd(fadd(ga[0].lo(), ga[1].lo()), fadd(ga[2].lo(), ga[3].lo()))), sa1 = rbf(fadd(fadd(ga[0].hi(), ga[1].hi()), fadd(ga[2].hi(), ga[3].hi())));
      const float ca0 = rbf(fadd(fadd(ga[4].lo(), ga[5].lo()), fadd(ga[6].lo(), ga[7].lo()))), ca1 = rbf(fadd(fadd(ga[4].hi(), ga[5].hi()), fadd(ga[6].hi(), ga[7].hi())));
      *(LAS unsigned*)(L + hdst) = pkr(rbf(ln0 * rbf(1.f + ca0)) + sa0);
      *(LAS unsigned*)(L + hdst + 48) = pkr(rbf(ln1 * rbf(1.f + ca1)) + sa1);
      __builtin_amdgcn_s_setprio(0);
    }
    Mask mq = make_mask(lane, (lane & 8) == 0);
    const int qe = (wave & 1) * 64 + lane;
    unsigned hsrc = F_H + ((wave - 3) * 4 + (lane >> 4)) * 48, qst = F_RED + 4608 + (wave - 3) * 1024 + lane * 16;
    unsigned qsrc = F_RED + 4608 + ((16 * ((qe & 7) >> 2) + (qe >> 3)) * 4 + (qe & 3)) * 4;
    gu64* qdst = (gu64*)ws + G_Q + (hq * 2 + qr) * 128 + qe;
    PIN(mq.m[0]); PIN(mq.m[1]); PIN(mq.m[2]); PIN(mq.m[3]); PIN(hsrc); PIN(qst); PIN(qsrc); PIN(qdst);
    bar();
    if (wave >= 3) {
      const LAS v4u* hp = (const LAS v4u*)(L + hsrc);
      const v4u h0 = hp[0], h1 = hp[1], h2 = hp[2];
      f32x4 d0 = {0.f, 0.f, 0.f, 0.f}, d1 = {0.f, 0.f, 0.f, 0.f};
#pragma unroll
      for (int t = 0; t < 4; ++t) { d0 = mfma(afrag(h0[t], mq), wa[t], d0); d1 = mfma(afrag(h1[t], mq), wa[4 + t], d1); }
#pragma unroll
      for (int t = 0; t < 4; t += 2) { d0 = mfma(afrag(h2[t], mq), wa[8 + t], d0); d1 = mfma(afrag(h2[t + 1], mq), wa[9 + t], d1); }
      *(LAS f32x4*)(L + qst) = d0 + d1;
    }
    bar();
    if (wave < 2) {
      const LAS float* src = (const LAS float*)(L + qsrc);
      pubp(qdst, fadd(fadd(fadd(src[0], src[256]), fadd(src[512], src[768])), fadd(src[1024], src[1280])), tag);
    }
  } else {
    if (wave == 0) tag_put(L, load_state(ws), lane);
    gate_wait(t0, GATE_MODB2);
    ModA ma;
    if (wave < 6) moda_load(p, 72 + 6 * x + wave, lane, ma);
    __builtin_amdgcn_sched_barrier(0);
    bar();
    if (wave < 6) {
      tag = tag_get(L);
      moda_finish(L, ws, 72 + 6 * x + wave, lane, ma, mk, (LAS float*)(L + F_RED + wave * 1024), tag);
    }
  }
  tag = tag_get(L);
  pretouch<1>(L, ws, [&](int) { const int e = wave * 64 + lane; return G_X2 + (e >> 3) * 16 + (e & 7); });
  const bool slow_tile = ((((unsigned)(uintptr_t)p.wg >> 7) + f) & 7) == 3;
  v4u wg[9], wu[9], wd[8];
  if (!slow_tile) gate_wait(t0, GATE_FFN);
  if (wave < 8) {
    const rsrc_t rg = mk_rsrc(p.wg, D * FF * 2), ru = mk_rsrc(p.wu, D * FF * 2);
    const unsigned v = (lane >> 3) * (FF * 2) + (lane & 7) * 16, s = wave * 72 * (FF * 2) + f * 128;
#pragma unroll
    for (int t = 0; t < 9; ++t) { wg[t] = ldb(rg, v, s + t * 8 * (FF * 2)); wu[t] = ldb(ru, v, s + t * 8 * (FF * 2)); }
  }
  __builtin_amdgcn_sched_barrier(0);
  unsigned h2w, h2r, stw, str_, gur, guw0, actw, actr, trw, trr;
  gu64* ddst;
  {
    const int e = wave * 64 + lane, hw = e / 72, rem = e - hw * 72, c = (wave & 3) * 16 + (lane >> 2);
    h2w = F_H2 + ((hw * 8 + (rem & 7)) * 12 + (rem >> 3)) * 4;
    h2r = F_H2 + ((wave & 7) * 8 + (lane >> 3)) * 48;
    stw = F_ST + wave * 8;
    str_ = F_ST + ((lane & 15) < 9 ? (lane & 15) : 9) * 8;
    guw0 = F_RED + (wave & 7) * 1040 + red_off(lane, 256);
    gur = F_RED + ((lane & 3) * 520 + c) * 4;
    actw = F_ACT + ((lane & 3) == 0 ? (c & 7) * 8 + (c >> 3) : 64) * 4;
    actr = F_ACT + (lane >> 3) * 32;
    trw = F_TR + wave * 640 + red_off(lane, 128);
    trr = F_TR + wave * 640 + lane * 4;
    ddst = (gu64*)ws + G_DN + f * 576 + wave * 64 + lane;
    if (wave == 0 && lane == 0) *(LAS f32x2*)(L + F_ST + 72) = f32x2{0.f, 0.f};
  }
  PIN(h2w); PIN(h2r); PIN(stw); PIN(str_); PIN(guw0); PIN(gur); PIN(actw); PIN(actr); PIN(trw); PIN(trr); PIN(ddst);
  {
    const int e = wave * 64 + lane;
    u64 gs[9];
    bool have_x2 = false;
    for (int spin = 0; spin < SPIN_CAP; ++spin) {
#pragma unroll
      for (int k = 0; k < 8; ++k) gs[k] = peek(ws, G_MOD + (k & 3) * NMOD + (k < 4 ? 3 : 4) * D + e);
      gs[8] = peek(ws, G_X2 + (e >> 3) * 16 + (e & 7));
      Chk c, cx;
#pragma unroll
      for (int k = 0; k < 8; ++k) c.add(gs[k], tag);
      cx.add(gs[8], tag);
      have_x2 = cx.all_ok();
      if (c.all_ok()) break;
    }
    float sb = rbf(fadd(fadd(gval(gs[0]), gval(gs[1])), fadd(gval(gs[2]), gval(gs[3]))));
    float c1b = rbf(1.f + rbf(fadd(fadd(gval(gs[4]), gval(gs[5])), fadd(gval(gs[6]), gval(gs[7])))));
    float on = (lane & 15) < 9 ? 64.f : 0.f;
    PIN(sb); PIN(c1b); PIN(on);
    u64 gx[1] = {gs[8]};
    if (!have_x2) gather<1>(ws, tag, gx, [&](int) { return G_X2 + (e >> 3) * 16 + (e & 7); });
    const float xv = gval(gx[0]);
    const float sh = __uint_as_float(__builtin_amdgcn_readfirstlane(__float_as_uint(xv)));
    const float d = xv - sh;
    float s1 = d, s2 = d * d;
    s1 += dpp<ROR8>(s1); s2 += dpp<ROR8>(s2); s1 += dpp<ROR4>(s1); s2 += dpp<ROR4>(s2);
    s1 += dpp<ROR2>(s1); s2 += dpp<ROR2>(s2); s1 += dpp<ROR1>(s1); s2 += dpp<ROR1>(s2);
    s1 = x16_sum(s1); s2 = x16_sum(s2);
    const float dm = s1 * (1.f / 64.f);
    if (lane == 0) *(LAS f32x2*)(L + stw) = f32x2{sh + dm, fmaf(-dm, s1, s2)};
    bar();
    const f32x2 st = *(const LAS f32x2*)(L + str_);
    __builtin_amdgcn_sched_barrier(0);
    {
      const rsrc_t rd = mk_rsrc(p.wd, FF * D * 2);
      const unsigned v = (lane >> 3) * (D * 2) + (lane & 7) * 16, s = f * 64 * (D * 2) + wave * 128;
#pragma unroll
      for (int t = 0; t < DN_SECOND; ++t) wd[t] = ldb(rd, v, s + t * 8 * (D * 2));
      __builtin_amdgcn_sched_barrier(0);
    }
    const float mf = row_sum(st[0]) * (1.f / 9.f);
    const float dv = st[0] - mf;
    const float m2 = row_sum(fmaf(on * dv, dv, st[1]));
    *(LAS unsigned*)(L + h2w) = ln_apply_pk(xv, rbf(mf), ln_rden(m2 * (1.f / D)), c1b, sb);
  }
  bar();
  if (f == 0 && threadIdx.x == 0) __hip_atomic_store((gu64*)ws + G_EPOCH, ((u64)tag.hi << 32) | (u64)tag.rest, RLX_AGENT);
  v4u h0, h1;
  unsigned h8;
  {
    const LAS v4u* hp = (const LAS v4u*)(L + h2r);
    h0 = hp[0]; h1 = hp[1]; h8 = ((const LAS unsigned*)hp)[8];
  }
  __builtin_amdgcn_sched_barrier(0);
  {
    const rsrc_t rd = mk_rsrc(p.wd, FF * D * 2);
    const unsigned v = (lane >> 3) * (D * 2) + (lane & 7) * 16, s = f * 64 * (D * 2) + wave * 128;
#pragma unroll
    for (int t = DN_SECOND; t < 8; ++t) wd[t] = ldb(rd, v, s + t * 8 * (D * 2));
    __builtin_amdgcn_sched_barrier(0);
  }
  if (wave < 8) {
    PIN(h0); PIN(h1); PIN(h8);
    f32x4 dg = {0.f, 0.f, 0.f, 0.f}, du = {0.f, 0.f, 0.f, 0.f};
#pragma unroll
    for (int t = 0; t < 9; ++t) {
      const v4u a = afrag(t < 4 ? h0[t & 3] : t < 8 ? h1[t & 3] : h8, mk);
      dg = mfma(a, wg[t], dg);
      du = mfma(a, wu[t], du);
    }
    *(LAS f32x4*)(L + guw0) = dg;
    *(LAS f32x4*)(L + guw0 + ((lane >> 5) == ((lane >> 3) & 1) ? 512 : 0)) = du;
  }
  bar();
  if (wave < 4) {
    const LAS float* r0 = (const LAS float*)(L + gur);
    float g = faddn(fadd(r0[0], r0[64]), fadd(r0[260], r0[324])), u = faddn(fadd(r0[128], r0[192]), fadd(r0[388], r0[452]));
    g += dpp<XOR1>(g); u += dpp<XOR1>(u);
    g += dpp<XOR2>(g); u += dpp<XOR2>(u);
    g = rbf(g); u = rbf(u);
    const float y = g * fmaf(g * g, 0.044715f * 0.7978845608028654f * 2.f * LOG2E, 0.7978845608028654f * 2.f * LOG2E);
    const float th = fmaf(-2.f, rcp_f(__builtin_amdgcn_exp2f(y) + 1.f), 1.f);
    const float act = rbf(0.5f * g * (1.f + th)) * u;
    *(LAS unsigned*)(L + actw) = pkr(act);
  }
  bar();
  {
    const LAS v4u* ap = (const LAS v4u*)(L + actr);
    const v4u a0 = ap[0], a1 = ap[1];
    f32x4 d0 = {0.f, 0.f, 0.f, 0.f}, d1 = {0.f, 0.f, 0.f, 0.f};
#pragma unroll
    for (int t = 0; t < 4; ++t) { d0 = mfma(afrag(a0[t], mk), wd[t], d0); d1 = mfma(afrag(a1[t], mk), wd[4 + t], d1); }
    *(LAS f32x4*)(L + trw) = d0 + d1;
    lds_wait();
    pubp(ddst, fadd(*(const LAS float*)(L + trr), *(const LAS float*)(L + trr + 256)), tag);
  }
}

DEV void role_modb(const Params& p, LAS unsigned char* L, int x, int wave, int lane, u64 t0, u64 code0) {
  if (wave == 8) {
    const unsigned dump = (unsigned)(uintptr_t)(L + L_PF);
#pragma unroll
    for (int k = 0; k < (CODE_PF + 8191) / 8192; ++k) {
      const unsigned o = k * 8192 + lane * 128;
      asm volatile("s_mov_b32 m0, %0\n\ts_nop 0\n\tglobal_load_lds_dword %1, off" :: "s"(dump), "v"(code0 + (o < CODE_PF - 128 ? o : CODE_PF - 128)) : "m0", "memory");
    }
  }
  const Mask mk = make_mask(lane, true);
  if (wave == 0) tag_put(L, load_state(p.ws), lane);
  bar();
  const Tag tag = tag_get(L);
  gate_wait(t0, GATE_MODB);
  for (int k = wave; k < 12; k += 9) {
    ModA ma;
    moda_load(p, 120 + x + 8 * k, lane, ma);
    __builtin_amdgcn_sched_barrier(0);
    moda_finish(L, p.ws, 120 + x + 8 * k, lane, ma, mk, (LAS float*)(L + wave * 1024), tag);
  }
}

__global__ void __launch_bounds__(NTHR) adaln_block_kernel(Params p) {
  __shared__ __attribute__((aligned(16))) unsigned char smem[L_END];
  LAS unsigned char* L = (LAS unsigned char*)smem;
  const int lane = threadIdx.x & 63, wave = __builtin_amdgcn_readfirstlane(threadIdx.x >> 6);
  u64 code0;
  asm volatile("s_getpc_b64 %0" : "=s"(code0));
  code0 &= ~0xFFull;
  const u64 t0 = __builtin_amdgcn_s_memrealtime();
  const int x = blockIdx.x & 7, idx = blockIdx.x >> 3;
  if (idx < 22) role_att(p, L, x, idx, wave, lane, t0);
  else if (idx < 31) role_ffn(p, L, x, idx - 22, wave, lane, t0);
  else role_modb(p, L, x, wave, lane, t0, code0);
}

__global__ void __launch_bounds__(576) k_null(u64* ws, long flag) { __shared__ unsigned big[36800]; if (flag) { big[threadIdx.x * 63] = 1; __syncthreads(); ws[0] = big[threadIdx.x + flag]; } }
extern "C" void kernel_launch(void* const* d_in, const int* in_sizes, int n_in, void* d_out, int out_size, void* d_ws,
                              size_t ws_size, hipStream_t stream) {
  Params p;
  p.x = (const u16*)d_in[0];
  p.kc = (const u16*)d_in[1];
  p.vc = (const u16*)d_in[2];
  p.wq = (const u16*)d_in[3];
  p.wo = (const u16*)d_in[4];
  p.wg = (const u16*)d_in[6];
  p.wu = (const u16*)d_in[7];
  p.wd = (const u16*)d_in[8];
  p.c = (const u16*)d_in[9];
  p.wmod = (const u16*)d_in[10];
  p.out = (u16*)d_out;
  p.ws = (u64*)d_ws;
  hipLaunchKernelGGL(adaln_block_kernel, dim3(256), dim3(NTHR), 0, stream, p);
  hipLaunchKernelGGL(k_null, dim3(256), dim3(576), 0, stream, p.ws, 0L);
}
```

```cpp
#include <hip/hip_runtime.h>
#include <stdint.h>

typedef unsigned short u16;
typedef unsigned long long u64;
typedef unsigned v4u __attribute__((ext_vector_type(4)));
typedef float f32x4 __attribute__((ext_vector_type(4)));
typedef float f32x2 __attribute__((ext_vector_type(2)));
typedef __bf16 bf16x2 __attribute__((ext_vector_type(2)));
typedef short bf16x8 __attribute__((ext_vector_type(8)));
typedef __amdgpu_buffer_rsrc_t rsrc_t;
#define GAS __attribute__((address_space(1)))
#define LAS __attribute__((address_space(3)))
#define DEV __device__ __forceinline__
typedef GAS u64 gu64;
#define RLX_AGENT __ATOMIC_RELAXED, __HIP_MEMORY_SCOPE_AGENT

constexpr int D = 576, SK = 65536, WIN = 4096, DH = 128, FF = 4608, NMOD = 6 * D;
constexpr int NSPLIT = 11;
constexpr int NTHR = 576;
constexpr int SPIN_CAP = 1 << 16;
constexpr unsigned GATE_ATT = 140;
constexpr unsigned GATE_MODB = 200;
constexpr unsigned GATE_MODB2 = 120;
constexpr unsigned GATE_H1 = 260;
constexpr unsigned GATE_Q = 450;
constexpr unsigned GATE_FFN = 790;
constexpr int Q_STAGGER = 16;
constexpr int ATT_N1 = 5, ATT_D1 = 2;
constexpr int DN_SECOND = 4;
constexpr int CODE_PF = 21504;

constexpr int G_EPOCH = 0;
constexpr int G_MOD = 64;
constexpr int G_Q = G_MOD + 4 * NMOD;
constexpr int G_O = G_Q + 32 * 2 * 128;
constexpr int G_WO = G_O + 16 * 8 * NSPLIT * 64;
constexpr int G_X2 = G_WO + 128 * 576;
constexpr int G_DN = G_X2 + 72 * 16;
constexpr int G_END = G_DN + 72 * 576;

constexpr int L_K = 0;
constexpr int L_OL = 98304;
constexpr int L_Q = L_OL + 9216;
constexpr int L_MX = L_Q + 512;
constexpr int L_LS = L_MX + 64;
constexpr int L_OB = L_LS + 64;
constexpr int L_PF = L_OB + 128;
constexpr int L_WO = L_PF + 256;
constexpr int L_MA = L_WO + 36864;
constexpr int L_CNT = L_MA + 1536;
constexpr int L_TAG = L_MA + 1536 + 16;
constexpr int L_END = L_TAG + 16;
constexpr int F_RED = 0;
constexpr int F_H = 16384;
constexpr int F_H2 = 17664;
constexpr int F_ACT = 20736;
constexpr int F_TR = 21248;
constexpr int F_ST = 27136;

struct Params {
  const u16 *x, *kc, *vc, *wq, *wo, *wg, *wu, *wd, *c, *wmod;
  u16* out;
  u64* ws;
};

DEV float bf2f(unsigned b) { return __uint_as_float(b << 16); }
DEV unsigned cvt2(float lo, float hi) {
  const f32x2 v = {lo, hi};
  return __builtin_bit_cast(unsigned, __builtin_convertvector(v, bf16x2));
}
DEV unsigned pkr(float f) { return cvt2(f, f); }
DEV float rbf(float f) { return __uint_as_float(cvt2(f, f) & 0xFFFF0000u); }
constexpr int TAG_LO_BITS = 4;
constexpr unsigned TAGM = (1u << TAG_LO_BITS) - 1u;
struct Tag { unsigned hi, lo, rest; };
DEV Tag make_tag(u64 st) {
  const u64 nx = st * 6364136223846793005ull + 1442695040888963407ull;
  Tag t;
  t.hi = __builtin_amdgcn_readfirstlane((unsigned)(nx >> 32));
  t.rest = __builtin_amdgcn_readfirstlane((unsigned)nx);
  t.lo = t.rest >> (32 - TAG_LO_BITS);
  return t;
}
DEV u64 enc(float v, unsigned hi, unsigned lo) {
  const unsigned u = (__float_as_uint(v) + (TAGM + 1) / 2) & ~TAGM;
  return ((u64)hi << 32) | (u64)(u | lo);
}
DEV Tag tag_put(LAS unsigned char* L, u64 st, int lane) {
  const Tag t = make_tag(st);
  if (lane == 0) *(LAS v4u*)(L + L_TAG) = v4u{t.hi, t.lo, t.rest, 0u};
  return t;
}
DEV Tag tag_get(const LAS unsigned char* L) {
  const v4u v = *(const LAS v4u*)(L + L_TAG);
  Tag t; t.hi = __builtin_amdgcn_readfirstlane(v[0]); t.lo = __builtin_amdgcn_readfirstlane(v[1]); t.rest = __builtin_amdgcn_readfirstlane(v[2]);
  return t;
}
DEV unsigned or_xor(unsigned a, unsigned b, unsigned c) { return __builtin_amdgcn_bitop3_b32(a, b, c, 0xF6); }
struct Chk {
  unsigned a = 0, b = 0;
  DEV void add(u64 g, const Tag& t) { a = or_xor(a, (unsigned)(g >> 32), t.hi); b = or_xor(b, (unsigned)g, t.lo); }
  DEV bool all_ok() const { return __builtin_amdgcn_ballot_w64((a | (b & TAGM)) != 0u) == 0ull; }
};
DEV float gval(u64 g) { return __uint_as_float((unsigned)g & ~TAGM); }
DEV void pub(u64* ws, int idx, float v, const Tag& t) { __hip_atomic_store((gu64*)ws + idx, enc(v, t.hi, t.lo), RLX_AGENT); }
DEV void pubp(gu64* dst, float v, const Tag& t) { __hip_atomic_store(dst, enc(v, t.hi, t.lo), RLX_AGENT); }
DEV u64 peek(u64* ws, int idx) { return __hip_atomic_load((gu64*)ws + idx, RLX_AGENT); }
template <int N, class F> DEV void gather(u64* ws, const Tag& t, u64 (&g)[N], F idx) {
  for (int spin = 0; spin < SPIN_CAP; ++spin) {
#pragma unroll
    for (int k = 0; k < N; ++k) g[k] = peek(ws, idx(k));
    Chk c;
#pragma unroll
    for (int k = 0; k < N; ++k) c.add(g[k], t);
    if (c.all_ok()) break;
  }
}

struct G2 { v4u v; DEV float lo() const { return __uint_as_float(v[0] & ~TAGM); } DEV float hi() const { return __uint_as_float(v[2] & ~TAGM); } };
template <int N, class F> DEV void gather2(rsrc_t rw, const Tag& t, G2 (&g)[N], F idx) {
  for (int spin = 0; spin < SPIN_CAP; ++spin) {
    asm volatile("" ::: "memory");
#pragma unroll
    for (int k = 0; k < N; ++k) g[k].v = __builtin_amdgcn_raw_buffer_load_b128(rw, idx(k) * 8, 0, 16);
    unsigned a = 0, b = 0;
#pragma unroll
    for (int k = 0; k < N; ++k) { a = or_xor(or_xor(a, g[k].v[1], t.hi), g[k].v[3], t.hi); b = or_xor(or_xor(b, g[k].v[0], t.lo), g[k].v[2], t.lo); }
    if (__builtin_amdgcn_ballot_w64((a | (b & TAGM)) != 0u) == 0ull) break;
  }
}
DEV void q_handoff(rsrc_t rw, unsigned voff, unsigned qdst, const Tag& t) {
  static_assert(TAG_LO_BITS == 4, "masks below");
  unsigned cnt;
  asm volatile(
      "s_mov_b32 %[cnt], %[cap]\n\t"
      "buffer_load_dwordx4 v[100:103], %[vo], %[rs], 0 offen sc1\n\t"
      "buffer_load_dwordx4 v[104:107], %[vo], %[rs], 0 offen offset:1024 sc1\n\t"
      "buffer_load_dwordx4 v[108:111], %[vo], %[rs], 0 offen offset:2048 sc1\n\t"
      "buffer_load_dwordx4 v[112:115], %[vo], %[rs], 0 offen offset:3072 sc1\n\t"
      "s_sleep %[stag]\n\t"
      "1:\n\t"
      "buffer_load_dwordx4 v[116:119], %[vo], %[rs], 0 offen sc1\n\t"
      "buffer_load_dwordx4 v[120:123], %[vo], %[rs], 0 offen offset:1024 sc1\n\t"
      "buffer_load_dwordx4 v[124:127], %[vo], %[rs], 0 offen offset:2048 sc1\n\t"
      "buffer_load_dwordx4 v[128:131], %[vo], %[rs], 0 offen offset:3072 sc1\n\t"
      "s_waitcnt vmcnt(4)\n\t"
      "v_xor_b32 v132, %[th], v101\n\t"
      "v_bitop3_b32 v132, v132, v103, %[th] bitop3:0xf6\n\t"
      "v_bitop3_b32 v132, v132, v105, %[th] bitop3:0xf6\n\t"
      "v_bitop3_b32 v132, v132, v107, %[th] bitop3:0xf6\n\t"
      "v_bitop3_b32 v132, v132, v109, %[th] bitop3:0xf6\n\t"
      "v_bitop3_b32 v132, v132, v111, %[th] bitop3:0xf6\n\t"
      "v_bitop3_b32 v132, v132, v113, %[th] bitop3:0xf6\n\t"
      "v_bitop3_b32 v132, v132, v115, %[th] bitop3:0xf6\n\t"
      "v_xor_b32 v133, %[tl], v100\n\t"
      "v_bitop3_b32 v133, v133, v102, %[tl] bitop3:0xf6\n\t"
      "v_bitop3_b32 v133, v133, v104, %[tl] bitop3:0xf6\n\t"
      "v_bitop3_b32 v133, v133, v106, %[tl] bitop3:0xf6\n\t"
      "v_bitop3_b32 v133, v133, v108, %[tl] bitop3:0xf6\n\t"
      "v_bitop3_b32 v133, v133, v110, %[tl] bitop3:0xf6\n\t"
      "v_bitop3_b32 v133, v133, v112, %[tl] bitop3:0xf6\n\t"
      "v_bitop3_b32 v133, v133, v114, %[tl] bitop3:0xf6\n\t"
      "v_and_b32 v133, 15, v133\n\t"
      "v_or_b32 v132, v132, v133\n\t"
      "v_cmp_ne_u32 vcc, 0, v132\n\t"
      "s_cbranch_vccz 2f\n\t"
      "buffer_load_dwordx4 v[100:103], %[vo], %[rs], 0 offen sc1\n\t"
      "buffer_load_dwordx4 v[104:107], %[vo], %[rs], 0 offen offset:1024 sc1\n\t"
      "buffer_load_dwordx4 v[108:111], %[vo], %[rs], 0 offen offset:2048 sc1\n\t"
      "buffer_load_dwordx4 v[112:115], %[vo], %[rs], 0 offen offset:3072 sc1\n\t"
      "s_waitcnt vmcnt(4)\n\t"
      "v_xor_b32 v132, %[th], v117\n\t"
      "v_bitop3_b32 v132, v132, v119, %[th] bitop3:0xf6\n\t"
      "v_bitop3_b32 v132, v132, v121, %[th] bitop3:0xf6\n\t"
      "v_bitop3_b32 v132, v132, v123, %[th] bitop3:0xf6\n\t"
      "v_bitop3_b32 v132, v132, v125, %[th] bitop3:0xf6\n\t"
      "v_bitop3_b32 v132, v132, v127, %[th] bitop3:0xf6\n\t"
      "v_bitop3_b32 v132, v132, v129, %[th] bitop3:0xf6\n\t"
      "v_bitop3_b32 v132, v132, v131, %[th] bitop3:0xf6\n\t"
      "v_xor_b32 v133, %[tl], v116\n\t"
      "v_bitop3_b32 v133, v133, v118, %[tl] bitop3:0xf6\n\t"
      "v_bitop3_b32 v133, v133, v120, %[tl] bitop3:0xf6\n\t"
      "v_bitop3_b32 v133, v133, v122, %[tl] bitop3:0xf6\n\t"
      "v_bitop3_b32 v133, v133, v124, %[tl] bitop3:0xf6\n\t"
      "v_bitop3_b32 v133, v133, v126, %[tl] bitop3:0xf6\n\t"
      "v_bitop3_b32 v133, v133, v128, %[tl] bitop3:0xf6\n\t"
      "v_bitop3_b32 v133, v133, v130, %[tl] bitop3:0xf6\n\t"
      "v_and_b32 v133, 15, v133\n\t"
      "v_or_b32 v132, v132, v133\n\t"
      "v_cmp_ne_u32 vcc, 0, v132\n\t"
      "s_cbranch_vccz 3f\n\t"
      "s_sub_u32 %[cnt], %[cnt], 1\n\t"
      "s_cbranch_scc0 1b\n\t"
      "2:\n\t"
      "v_and_b32 v132, 0xfffffff0, v100\n\t"
      "v_and_b32 v133, 0xfffffff0, v104\n\t"
      "v_add_f32 v132, v132, v133\n\t"
      "v_and_b32 v133, 0xfffffff0, v102\n\t"
      "v_and_b32 v136, 0xfffffff0, v106\n\t"
      "v_add_f32 v133, v133, v136\n\t"
      "v_cvt_pk_bf16_f32 v132, v132, v133\n\t"
      "ds_write_b32 %[qd], v132\n\t"
      "v_and_b32 v134, 0xfffffff0, v108\n\t"
      "v_and_b32 v135, 0xfffffff0, v112\n\t"
      "v_add_f32 v134, v134, v135\n\t"
      "v_and_b32 v135, 0xfffffff0, v110\n\t"
      "v_and_b32 v136, 0xfffffff0, v114\n\t"
      "v_add_f32 v135, v135, v136\n\t"
      "v_cvt_pk_bf16_f32 v134, v134, v135\n\t"
      "ds_write_b32 %[qd], v134 offset:256\n\t"
      "s_branch 4f\n\t"
      "3:\n\t"
      "v_and_b32 v132, 0xfffffff0, v116\n\t"
      "v_and_b32 v133, 0xfffffff0, v120\n\t"
      "v_add_f32 v132, v132, v133\n\t"
      "v_and_b32 v133, 0xfffffff0, v118\n\t"
      "v_and_b32 v136, 0xfffffff0, v122\n\t"
      "v_add_f32 v133, v133, v136\n\t"
      "v_cvt_pk_bf16_f32 v132, v132, v133\n\t"
      "ds_write_b32 %[qd], v132\n\t"
      "v_and_b32 v134, 0xfffffff0, v124\n\t"
      "v_and_b32 v135, 0xfffffff0, v128\n\t"
      "v_add_f32 v134, v134, v135\n\t"
      "v_and_b32 v135, 0xfffffff0, v126\n\t"
      "v_and_b32 v136, 0xfffffff0, v130\n\t"
      "v_add_f32 v135, v135, v136\n\t"
      "v_cvt_pk_bf16_f32 v134, v134, v135\n\t"
      "ds_write_b32 %[qd], v134 offset:256\n\t"
      "4:\n\t"
      "s_waitcnt lgkmcnt(0)\n\t"
      "s_barrier\n\t"
      "s_waitcnt vmcnt(0)"
      : [cnt] "=&s"(cnt)
      : [vo] "v"(voff), [rs] "s"(rw), [th] "s"(t.hi), [tl] "s"(t.lo), [qd] "v"(qdst), [cap] "n"(SPIN_CAP / 2), [stag] "n"(Q_STAGGER)
      : "vcc", "scc", "memory", "v100", "v101", "v102", "v103", "v104", "v105", "v106", "v107", "v108", "v109", "v110", "v111", "v112", "v113", "v114", "v115", "v116", "v117", "v118", "v119", "v120", "v121", "v122", "v123", "v124", "v125", "v126", "v127", "v128", "v129", "v130", "v131", "v132", "v133", "v134", "v135", "v136");
}
template <int N, class F> DEV void pretouch(LAS unsigned char* L, u64* ws, F idx) {
  const unsigned dump = (unsigned)(uintptr_t)(L + L_PF);
#pragma unroll
  for (int k = 0; k < N; ++k)
    asm volatile("s_mov_b32 m0, %0\n\ts_nop 0\n\tglobal_load_lds_dword %1, off sc1" :: "s"(dump), "v"((gu64*)ws + idx(k)) : "m0", "memory");
}
DEV f32x4 mfma(v4u a, v4u b, f32x4 c) {
  return __builtin_amdgcn_mfma_f32_16x16x32_bf16(__builtin_bit_cast(bf16x8, a), __builtin_bit_cast(bf16x8, b), c, 0, 0, 0);
}
DEV rsrc_t mk_rsrc(const void* p, unsigned bytes) { return __builtin_amdgcn_make_buffer_rsrc((void*)p, 0, (int)bytes, 0x00020000); }
DEV v4u ldb(rsrc_t r, unsigned voff, unsigned soff) { return __builtin_amdgcn_raw_buffer_load_b128(r, (int)voff, (int)soff, 2); }
DEV unsigned ldb16(rsrc_t r, unsigned voff) { return __builtin_amdgcn_raw_buffer_load_b16(r, (int)voff, 0, 0); }
DEV u64 load_state(u64* ws) {
  int zoff = G_EPOCH * 8;
  asm volatile("" : "+v"(zoff));
  typedef unsigned v2u_ __attribute__((ext_vector_type(2)));
  const v2u_ v = __builtin_amdgcn_raw_buffer_load_b64(mk_rsrc(ws, 64), zoff, 0, 16);
  return ((u64)v[1] << 32) | v[0];
}
DEV void bar() {
  asm volatile("s_waitcnt lgkmcnt(0)" ::: "memory");
  __builtin_amdgcn_s_barrier();
  asm volatile("" ::: "memory");
}
#define PIN(x) asm volatile("" : "+v"(x))
DEV void lds_wait() { asm volatile("s_waitcnt lgkmcnt(0)" ::: "memory"); }
template <int CTRL> DEV float dpp(float x) {
  return __builtin_bit_cast(float, __builtin_amdgcn_mov_dpp(__builtin_bit_cast(int, x), CTRL, 0xf, 0xf, true));
}
constexpr int ROR8 = 0x128, ROR4 = 0x124, ROR2 = 0x122, ROR1 = 0x121, XOR1 = 0xB1, XOR2 = 0x4E;
DEV float fmx(float a, float b) { float r; asm("v_max_f32 %0, %1, %2\n\ts_nop 1" : "=v"(r) : "v"(a), "v"(b)); return r; }
DEV float fmx3(float a, float b, float c) { float r; asm("v_max3_f32 %0, %1, %2, %3" : "=v"(r) : "v"(a), "v"(b), "v"(c)); return r; }
DEV float fmx3n(float a, float b, float c) { float r; asm("v_max3_f32 %0, %1, %2, %3\n\ts_nop 1" : "=v"(r) : "v"(a), "v"(b), "v"(c)); return r; }
DEV float x16_sum(float x) {
  auto s = __builtin_amdgcn_permlane16_swap(__float_as_uint(x), __float_as_uint(x), false, false);
  x = __uint_as_float(s[0]) + __uint_as_float(s[1]);
  auto t = __builtin_amdgcn_permlane32_swap(__float_as_uint(x), __float_as_uint(x), false, false);
  return __uint_as_float(t[0]) + __uint_as_float(t[1]);
}
DEV float x16_max(float x) {
  auto s = __builtin_amdgcn_permlane16_swap(__float_as_uint(x), __float_as_uint(x), false, false);
  x = fmaxf(__uint_as_float(s[0]), __uint_as_float(s[1]));
  auto t = __builtin_amdgcn_permlane32_swap(__float_as_uint(x), __float_as_uint(x), false, false);
  return fmaxf(__uint_as_float(t[0]), __uint_as_float(t[1]));
}
DEV float row_sum(float x) { x += dpp<ROR8>(x); x += dpp<ROR4>(x); x += dpp<ROR2>(x); x += dpp<ROR1>(x); return x; }
DEV float wave_sum(float x) { return x16_sum(row_sum(x)); }
constexpr float LOG2E = 1.4426950408889634f;
DEV float fadd(float a, float b) { float r; asm("v_add_f32 %0, %1, %2" : "=v"(r) : "v"(a), "v"(b)); return r; }
DEV float faddn(float a, float b) { float r; asm("v_add_f32 %0, %1, %2\n\ts_nop 1" : "=v"(r) : "v"(a), "v"(b)); return r; }
DEV float exp_f(float x) { return __builtin_amdgcn_exp2f(x * LOG2E); }
DEV float rcp_f(float x) { return __builtin_amdgcn_rcpf(x); }
DEV float pair_pick(float a, float b, int lane) {
  const float a2 = dpp<0x50>(a), b2 = dpp<0x50>(b);
  const float a3 = dpp<0xFA>(a), b3 = dpp<0xFA>(b);
  const float lo = (lane & 1) ? b2 : a2, hi = (lane & 1) ? b3 : a3;
  return (lane & 4) ? hi : lo;
}
DEV void gate_wait(u64 t0, unsigned ticks) { while (__builtin_amdgcn_s_memrealtime() - t0 < (u64)ticks) __builtin_amdgcn_s_sleep(2); }

struct Mask { unsigned m[4]; };
DEV Mask make_mask(int lane, bool on) {
  Mask k;
  const unsigned h = on ? ((lane & 1) ? 0xFFFF0000u : 0x0000FFFFu) : 0u;
#pragma unroll
  for (int r = 0; r < 4; ++r) k.m[r] = (((lane & 7) >> 1) == r) ? h : 0u;
  return k;
}
DEV v4u afrag(unsigned pp, const Mask& k) { return v4u{pp & k.m[0], pp & k.m[1], pp & k.m[2], pp & k.m[3]}; }

DEV unsigned ln_apply_pk(float x, float mean_b, float rden, float c1, float shift) {
  const float ln = rbf(rbf(x - mean_b) * rden);
  return pkr(rbf(ln * c1) + shift);
}
DEV float ln_rden(float var) { return rcp_f(rbf(__builtin_amdgcn_sqrtf(rbf(rbf(var) + 1e-5f)))); }

DEV unsigned red_off(int lane, int dump) {
  const int u = (lane >> 3) & 1;
  return 4 * ((lane >> 5) == u ? u * 64 + 8 * (lane & 7) + 4 * ((lane >> 4) & 1) : dump);
}
DEV unsigned silu_pk(unsigned craw) { const float v = bf2f(craw); return pkr(v * rcp_f(1.f + exp_f(-v))); }

struct WoAddr { unsigned tro, tri, obo, wro; gu64* dst; };
DEV WoAddr wo_prep(u64* ws, int pidx, int wave, int lane) {
  WoAddr a;
  a.tro = L_OL + wave * 640 + red_off(lane, 128); a.tri = L_OL + wave * 640 + lane * 4; a.obo = L_OB + (lane >> 3) * 16; a.wro = L_WO + wave * 4096 + lane * 16;
  a.dst = (gu64*)ws + G_WO + pidx * 576 + wave * 64 + lane;
  PIN(a.tro); PIN(a.tri); PIN(a.obo); PIN(a.wro); PIN(a.dst);
  return a;
}
DEV void wo_phase(LAS unsigned char* L, const WoAddr& a, const Tag& tag, const Mask& mk) {
  bar();
  v4u ob = *(const LAS v4u*)(L + a.obo);
  const LAS v4u* wr = (const LAS v4u*)(L + a.wro);
  v4u w0 = wr[0], w1 = wr[64], w2 = wr[128], w3 = wr[192];
  PIN(ob); PIN(w0); PIN(w1); PIN(w2); PIN(w3);
  __builtin_amdgcn_sched_barrier(0);
  f32x4 d0 = {0.f, 0.f, 0.f, 0.f}, d1 = {0.f, 0.f, 0.f, 0.f};
  d0 = mfma(afrag(ob[0], mk), w0, d0); d1 = mfma(afrag(ob[1], mk), w1, d1);
  d0 = mfma(afrag(ob[2], mk), w2, d0); d1 = mfma(afrag(ob[3], mk), w3, d1);
  *(LAS f32x4*)(L + a.tro) = d0 + d1;
  lds_wait();
  pubp(a.dst, fadd(*(const LAS float*)(L + a.tri), *(const LAS float*)(L + a.tri + 256)), tag);
}

struct ModA { v4u w[18]; unsigned c[3]; };
DEV void moda_load(const Params& p, int mt, int lane, ModA& m) {
  const rsrc_t rm = mk_rsrc(p.wmod, D * NMOD * 2), rc = mk_rsrc(p.c, D * 2);
  const int r0 = (mt & 3) * 144;
#pragma unroll
  for (int k = 0; k < 3; ++k) m.c[k] = ldb16(rc, (r0 + (k < 2 || lane < 16 ? lane + 64 * k : 143)) * 2);
  const unsigned v = (r0 + (lane >> 3)) * (NMOD * 2) + (lane & 7) * 16;
#pragma unroll
  for (int t = 0; t < 18; ++t) m.w[t] = ldb(rm, v, (mt >> 2) * 128 + t * 8 * (NMOD * 2));
}
DEV void moda_finish(LAS unsigned char* L, u64* ws, int mt, int lane, ModA& m, const Mask& mk, LAS float* tr, const Tag& tag) {
  unsigned sp[18];
  {
    const unsigned s0 = silu_pk(m.c[0]), s1 = silu_pk(m.c[1]), s2 = silu_pk(m.c[2]);
#pragma unroll
    for (int t = 0; t < 18; ++t) sp[t] = __builtin_amdgcn_ds_bpermute(((8 * t + (lane >> 3)) & 63) * 4, t < 8 ? s0 : t < 16 ? s1 : s2);
  }
  gu64* dst = (gu64*)ws + G_MOD + (mt & 3) * NMOD + (mt >> 2) * 64 + lane;
  unsigned tro = red_off(lane, 128);
  PIN(dst); PIN(tro);
#pragma unroll
  for (int t = 0; t < 18; ++t) PIN(sp[t]);
  __builtin_amdgcn_sched_barrier(0);
  f32x4 d0 = {0.f, 0.f, 0.f, 0.f}, d1 = {0.f, 0.f, 0.f, 0.f};
#pragma unroll
  for (int t = 0; t < 18; t += 2) {
    d0 = mfma(afrag(sp[t], mk), m.w[t], d0);
    __builtin_amdgcn_sched_barrier(0);
    d1 = mfma(afrag(sp[t + 1], mk), m.w[t + 1], d1);
    __builtin_amdgcn_sched_barrier(0);
  }
  *(LAS f32x4*)((LAS unsigned char*)tr + tro) = d0 + d1;
  lds_wait();
  pubp(dst, fadd(tr[lane], tr[64 + lane]), tag);
}

DEV void role_att(const Params& p, LAS unsigned char* L, int x, int i, int wave, int lane, u64 t0) {
  const int g = 2 * x + (i >= NSPLIT ? 1 : 0), j = i >= NSPLIT ? i - NSPLIT : i;
  const bool odd = g & 1;
  const int nm = odd ? 4 : 5;
  const bool moda = j < nm;
  const int ntile = !moda ? 24 : (!odd && j >= 3) ? 23 : 22;
  const int tile0 = !moda ? (odd ? 88 : 112) + 24 * (j - nm) : (!odd && j == 4) ? 89 : 22 * j;
  const bool owner = j < 8;
  int redc = -1;
  if (j >= 8) redc = 9 * x + (g & 1) * 3 + (j - 8);
  else if (j == 0) redc = 9 * x + 6 + (g & 1);
  else if (j == 1 && (g & 1) == 0) redc = 9 * x + 8;
  u64* ws = p.ws;
  const Mask mk = make_mask(lane, true);
  const int kap = lane >> 4, cc = lane & 15;

  if (wave < 8) {
    const size_t hb = ((size_t)g * SK + (SK - WIN)) * DH;
    const rsrc_t rk = mk_rsrc(p.kc + hb, WIN * DH * 2), rv = mk_rsrc(p.vc + hb, WIN * DH * 2);
    const bool three = wave + 16 < ntile;
    const unsigned tso = (tile0 + wave) * 4096;
    const unsigned ldsk = (unsigned)(uintptr_t)(L + L_K) + wave * 3 * 4096;
    unsigned vk[4];
#pragma unroll
    for (int q = 0; q < 4; ++q) vk[q] = (4 * q + kap) * 256 + ((cc ^ (4 * q + kap)) << 4);
#define KDMA(s, q) asm volatile("s_mov_b32 m0, %0\n\ts_nop 0\n\tbuffer_load_dwordx4 %1, %2, %3 offen nt lds" \
                     :: "s"(ldsk + (s) * 4096 + (q) * 1024), "v"(vk[q]), "s"(rk), "s"(tso + (s) * 32768) : "m0", "memory")
#define THR(n) do { if ((n) < ATT_N1 && (n) >= ATT_D1 && moda) asm volatile("s_waitcnt vmcnt(%0)" :: "n"(ATT_D1 - 1) : "memory"); } while (0)
    if (moda) __builtin_amdgcn_s_barrier();
    __builtin_amdgcn_s_setprio(2);
    KDMA(0, 0);
    gate_wait(t0, GATE_ATT);
    THR(1); KDMA(0, 1); THR(2); KDMA(0, 2); THR(3); KDMA(0, 3);
    THR(4); KDMA(1, 0); KDMA(1, 1); KDMA(1, 2); KDMA(1, 3);
    if (three) { KDMA(2, 0); KDMA(2, 1); KDMA(2, 2); KDMA(2, 3); }
#undef KDMA
#undef THR
    __builtin_amdgcn_s_setprio(0);
    v4u vr[12];
    const unsigned vv = kap * 1024 + cc * 16;
#pragma unroll
    for (int t = 0; t < 8; ++t) vr[t] = ldb(rv, vv + (t & 3) * 256, tso + (t >> 2) * 32768);
    if (three) {
#pragma unroll
      for (int t = 8; t < 12; ++t) vr[t] = ldb(rv, vv + (t & 3) * 256, tso + 65536);
    } else {
#pragma unroll
      for (int t = 8; t < 12; ++t) vr[t] = v4u{0u, 0u, 0u, 0u};
    }
    __builtin_amdgcn_sched_barrier(0);
    if (three) asm volatile("s_waitcnt vmcnt(12)" ::: "memory");
    else asm volatile("s_waitcnt vmcnt(8)" ::: "memory");
    v4u kf[12];
#pragma unroll
    for (int s = 0; s < 3; ++s)
#pragma unroll
      for (int ks = 0; ks < 4; ++ks)
        kf[s * 4 + ks] = *(const LAS v4u*)(L + L_K + (wave * 3 + (s < 2 || three ? s : 0)) * 4096 + cc * 256 + (((4 * ks + kap) ^ cc) << 4));
#pragma unroll
    for (int t = 0; t < 12; ++t) PIN(kf[t]);
    const int head = (lane >> 3) & 1;
    const int me = (wave & 3) * 64 + lane;
    unsigned qsrc = L_Q + head * 256 + kap * 16, mxw = L_MX + (head * 8 + wave) * 4, mxr = L_MX + head * 32, olw = L_OL + wave * 1024 + lane * 16;
    unsigned msrc = L_OL + ((16 * (((me >> 7) * 8 + (me & 7)) >> 2) + ((me & 127) >> 3)) * 4 + (me & 3)) * 4;
    gu64* mdst = (gu64*)ws + G_O + ((g * 8 + (me >> 5)) * NSPLIT + j) * 64 + (me & 31);
    bool lead = (lane & 0x37) == 0;
    PIN(qsrc); PIN(mxw); PIN(mxr); PIN(olw); PIN(msrc); PIN(mdst);
    bar();
    v4u qf[4];
#pragma unroll
    for (int ks = 0; ks < 4; ++ks) qf[ks] = *(const LAS v4u*)(L + qsrc + ks * 64);
#pragma unroll
    for (int ks = 0; ks < 4; ++ks) PIN(qf[ks]);
    __builtin_amdgcn_sched_barrier(0);
    f32x4 acc[3] = {{0.f, 0.f, 0.f, 0.f}, {0.f, 0.f, 0.f, 0.f}, {0.f, 0.f, 0.f, 0.f}};
#pragma unroll
    for (int ks = 0; ks < 4; ++ks)
#pragma unroll
      for (int s = 0; s < 3; ++s) acc[s] = mfma(kf[s * 4 + ks], qf[ks], acc[s]);
    __builtin_amdgcn_sched_barrier(0);
    float sc[12];
#pragma unroll
    for (int t = 0; t < 12; t += 2) {
      const unsigned u = cvt2(acc[t >> 2][t & 3], acc[t >> 2][(t & 3) + 1]);
      const unsigned w = cvt2(__uint_as_float(u << 16) * 0.08838834764831845f, __uint_as_float(u & 0xFFFF0000u) * 0.08838834764831845f);
      sc[t] = __uint_as_float(w << 16);
      sc[t + 1] = __uint_as_float(w & 0xFFFF0000u);
    }
    if (!three) {
#pragma unroll
      for (int t = 8; t < 12; ++t) sc[t] = -INFINITY;
    }
    float mx = fmx3n(fmx3(fmx3(sc[0], sc[1], sc[2]), fmx3(sc[3], sc[4], sc[5]), fmx3(sc[6], sc[7], sc[8])), fmx3(sc[9], sc[10], sc[11]), sc[0]);
    mx = x16_max(mx);
    if (lead) *(LAS float*)(L + mxw) = mx;
    lds_wait();
    if (lane == 0) __hip_atomic_fetch_add((LAS unsigned*)(L + L_CNT), 1u, __ATOMIC_RELAXED, __HIP_MEMORY_SCOPE_WORKGROUP);
    while (*(volatile LAS unsigned*)(L + L_CNT) < 8u) {}
    asm volatile("" ::: "memory");
    float m;
    {
      const f32x4 a = *(const LAS f32x4*)(L + mxr), b = *(const LAS f32x4*)(L + mxr + 16);
      m = fmx3(fmx3(a[0], a[1], a[2]), fmx3(a[3], b[0], b[1]), fmx(b[2], b[3]));
    }
    v4u af[12];
    float ls0 = 0.f, ls1 = 0.f;
    const float mneg = -m * LOG2E;
#pragma unroll
    for (int t = 0; t < 12; t += 2) {
      const float e0 = __builtin_amdgcn_exp2f(fmaf(sc[t], LOG2E, mneg)), e1 = __builtin_amdgcn_exp2f(fmaf(sc[t + 1], LOG2E, mneg));
      ls0 += e0; ls1 += e1;
      af[t] = afrag(pkr(e0), mk); af[t + 1] = afrag(pkr(e1), mk);
    }
    const float ls = x16_sum(ls0 + ls1);
    if (lead) *(LAS float*)(L + mxw + 64) = ls;
    const Tag tag = tag_get(L);
    __builtin_amdgcn_sched_barrier(0);
    f32x4 o0 = {0.f, 0.f, 0.f, 0.f}, o1 = {0.f, 0.f, 0.f, 0.f};
#pragma unroll
    for (int t = 0; t < 12; t += 2) { o0 = mfma(af[t], vr[t], o0); o1 = mfma(af[t + 1], vr[t + 1], o1); }
    *(LAS f32x4*)(L + olw) = o0 + o1;
    bar();
    if (wave < 4) {
      const LAS float* src = (const LAS float*)(L + msrc);
      float s[8];
#pragma unroll
      for (int w = 0; w < 8; ++w) s[w] = src[w * 256];
      pubp(mdst, fadd(fadd(fadd(s[0], s[1]), fadd(s[2], s[3])), fadd(fadd(s[4], s[5]), fadd(s[6], s[7]))), tag);
    } else if (wave == 4 && lane < 16) {
      const int u = lane >> 1, hh = u >> 2;
      const f32x4 a = *(const LAS f32x4*)(L + ((lane & 1) ? L_LS : L_MX) + hh * 32), b = *(const LAS f32x4*)(L + ((lane & 1) ? L_LS : L_MX) + hh * 32 + 16);
      const float vs = ((a[0] + a[1]) + (a[2] + a[3])) + ((b[0] + b[1]) + (b[2] + b[3]));
      const float vm = fmaxf(fmaxf(fmaxf(a[0], a[1]), fmaxf(a[2], a[3])), fmaxf(fmaxf(b[0], b[1]), fmaxf(b[2], b[3])));
      pub(ws, G_O + ((g * 8 + u) * NSPLIT + j) * 64 + 32 + (lane & 1), (lane & 1) ? vs : vm, tag);
    }
    if (owner) {
      wo_phase(L, wo_prep(ws, g * 8 + j, wave, lane), tag, mk);
    }
  } else {
    __builtin_amdgcn_s_setprio(3);
    Tag tag;
    {
      if (moda) {
        u64 st = load_state(ws);
        ModA ma;
        moda_load(p, 9 * x + (odd ? 5 : 0) + j, lane, ma);
        __builtin_amdgcn_sched_barrier(0);
        __builtin_amdgcn_s_barrier();
        asm volatile("" : "+v"(st));
        tag = tag_put(L, st, lane);
        moda_finish(L, ws, 9 * x + (odd ? 5 : 0) + j, lane, ma, mk, (LAS float*)(L + L_MA), tag);
      } else {
        tag = tag_put(L, load_state(ws), lane);
      }
    }
    float xr = 0.f;
    if (redc >= 0) xr = bf2f(p.x[redc * 8 + (lane & 7)]);
    gate_wait(t0, GATE_Q);
    const rsrc_t rw = mk_rsrc(ws, G_END * 8);
    if (lane == 0) *(volatile LAS unsigned*)(L + L_CNT) = 0u;
    q_handoff(rw, (G_Q + 2 * g * 256 + 2 * lane) * 8, (unsigned)(uintptr_t)(L + L_Q) + lane * 4, tag);
    if (owner) {
      const int ob = G_O + (g * 8 + j) * NSPLIT * 64;
      pretouch<1>(L, ws, [&](int) { const int q_ = lane < 33 ? lane : 32; return ob + (q_ / 3) * 64 + (q_ % 3) * 16; });
    }
    if (redc >= 0) {
      pretouch<2>(L, ws, [&](int s) { return G_WO + (s * 64 + lane) * 576 + redc * 8; });
      pretouch<2>(L, ws, [&](int s) { const int f_ = s * 64 + lane; return G_DN + (f_ < 72 ? f_ : 71) * 576 + redc * 8; });
    }
    if (owner) {
      const rsrc_t rwo = mk_rsrc(p.wo, 4096 * D * 2);
      const unsigned wo_v = (lane >> 3) * (D * 2) + (lane & 7) * 16;
      const unsigned wo_s = (g * 256 + j * 32) * (D * 2), ldsw = (unsigned)(uintptr_t)(L + L_WO);
#pragma unroll
      for (int w = 0; w < 9; ++w)
#pragma unroll
        for (int t = 0; t < 4; ++t)
          asm volatile("s_mov_b32 m0, %0\n\ts_nop 0\n\tbuffer_load_dwordx4 %1, %2, %3 offen nt lds"
                       :: "s"(ldsw + (w * 4 + t) * 1024), "v"(wo_v), "s"(rwo), "s"(wo_s + w * 128 + t * 8 * (D * 2)) : "m0", "memory");
    }
    bar();
    if (owner) {
      const int ob = G_O + (g * 8 + j) * NSPLIT * 64;
      const int k2 = kap < 3 ? 8 + kap : 10;
      unsigned obw = L_OB + (((2 * cc) & 7) * 4 + (cc >> 2)) * 4;
      float live = kap < 3 ? 1.f : 0.f;
      PIN(obw); PIN(live);
      const WoAddr wa = wo_prep(ws, g * 8 + j, 8, lane);
      G2 go[6];
      gather2<6>(rw, tag, go, [&](int k) { const int sp = k % 3 == 0 ? kap : k % 3 == 1 ? 4 + kap : k2; return ob + sp * 64 + (k < 3 ? 2 * cc : 32); });
      const float M = x16_max(fmaxf(fmaxf(go[3].lo(), go[4].lo()), go[5].lo()));
      const float Mn = -M * LOG2E;
      const float w0 = __builtin_amdgcn_exp2f(fmaf(go[3].lo(), LOG2E, Mn)), w1 = __builtin_amdgcn_exp2f(fmaf(go[4].lo(), LOG2E, Mn));
      const float w2 = __builtin_amdgcn_exp2f(fmaf(go[5].lo(), LOG2E, Mn)) * live;
      const float Ls = x16_sum(fmaf(w2, go[5].hi(), fmaf(w1, go[4].hi(), w0 * go[3].hi())));
      const float o0 = x16_sum(fmaf(w2, go[2].lo(), fmaf(w1, go[1].lo(), w0 * go[0].lo())));
      const float o1 = x16_sum(fmaf(w2, go[2].hi(), fmaf(w1, go[1].hi(), w0 * go[0].hi())));
      const float rl = rcp_f(Ls);
      *(LAS unsigned*)(L + obw) = pkr(o0 * rl);
      *(LAS unsigned*)(L + obw + 16) = pkr(o1 * rl);
      asm volatile("s_waitcnt vmcnt(0)" ::: "memory");
      wo_phase(L, wa, tag, mk);
    }
    if (redc >= 0) {
      u64 gg[8];
      gather<8>(ws, tag, gg, [&](int k) { return G_MOD + (k & 3) * NMOD + (k < 4 ? 2 : 5) * D + redc * 8 + (lane & 7); });
      float ga = rbf(fadd(fadd(gval(gg[0]), gval(gg[1])), fadd(gval(gg[2]), gval(gg[3]))));
      float gb = rbf(fadd(fadd(gval(gg[4]), gval(gg[5])), fadd(gval(gg[6]), gval(gg[7]))));
      gu64* xdst = (gu64*)ws + G_X2 + redc * 16 + (lane & 7);
      GAS u16* odst = (GAS u16*)p.out + redc * 8 + (lane & 7);
      PIN(ga); PIN(gb); PIN(xr); PIN(xdst); PIN(odst);
      float x2;
      {
        G2 gw[8];
        gather2<8>(rw, tag, gw, [&](int s) { return G_WO + (s * 16 + (lane >> 2)) * 576 + redc * 8 + 2 * (lane & 3); });
        float t0_ = faddn(fadd(fadd(gw[0].lo(), gw[1].lo()), fadd(gw[2].lo(), gw[3].lo())), fadd(fadd(gw[4].lo(), gw[5].lo()), fadd(gw[6].lo(), gw[7].lo())));
        float t1_ = faddn(fadd(fadd(gw[0].hi(), gw[1].hi()), fadd(gw[2].hi(), gw[3].hi())), fadd(fadd(gw[4].hi(), gw[5].hi()), fadd(gw[6].hi(), gw[7].hi())));
        t0_ += dpp<ROR8>(t0_); t1_ += dpp<ROR8>(t1_); t0_ += dpp<ROR4>(t0_); t1_ += dpp<ROR4>(t1_);
        t0_ = x16_sum(t0_); t1_ = x16_sum(t1_);
        const float ts = pair_pick(t0_, t1_, lane);
        x2 = rbf(xr + rbf(ga * rbf(ts)));
        if (lane < 8) pubp(xdst, x2, tag);
      }
      {
        G2 gd[5];
        float w4 = lane < 32 ? 1.f : 0.f;
        PIN(w4);
        gather2<5>(rw, tag, gd, [&](int s) { const int f_ = s * 16 + (lane >> 2); return G_DN + (f_ < 72 ? f_ : 71) * 576 + redc * 8 + 2 * (lane & 3); });
        float t0_ = fmaf(w4, gd[4].lo(), fadd(fadd(gd[0].lo(), gd[1].lo()), fadd(gd[2].lo(), gd[3].lo())));
        float t1_ = fmaf(w4, gd[4].hi(), fadd(fadd(gd[0].hi(), gd[1].hi()), fadd(gd[2].hi(), gd[3].hi())));
        t0_ += dpp<ROR8>(t0_); t1_ += dpp<ROR8>(t1_); t0_ += dpp<ROR4>(t0_); t1_ += dpp<ROR4>(t1_);
        t0_ = x16_sum(t0_); t1_ = x16_sum(t1_);
        const float ts = pair_pick(t0_, t1_, lane);
        const unsigned o = pkr(x2 + rbf(gb * rbf(ts)));
        if (lane < 8) *odst = (u16)o;
      }
    }
  }
}

DEV void role_ffn(const Params& p, LAS unsigned char* L, int x, int i, int wave, int lane, u64 t0) {
  const int f = 9 * x + i;
  u64* ws = p.ws;
  const Mask mk = make_mask(lane, true);
  LAS float* red = (LAS float*)(L + F_RED);
  const rsrc_t rm = mk_rsrc(p.wmod, D * NMOD * 2), rc = mk_rsrc(p.c, D * 2), rx = mk_rsrc(p.x, D * 2);
  const int hq = 4 * x + (i >> 1), qr = i & 1;
  const rsrc_t rw = mk_rsrc(ws, G_END * 8);
  Tag tag;
  if (i < 8) {
    v4u wa[12];
    if (wave >= 3) {
      const rsrc_t rq = mk_rsrc(p.wq, D * 4096 * 2);
      const unsigned v = (lane >> 4) * 8192 + (lane & 15) * 16, s = (qr * 288 + (wave - 3) * 48) * 8192 + hq * 256;
#pragma unroll
      for (int t = 0; t < 12; ++t) wa[t] = ldb(rq, v, s + t * 4 * 8192);
      __builtin_amdgcn_sched_barrier(0);
    } else {
      __builtin_amdgcn_s_setprio(3);
      const u64 st = load_state(ws);
      float xv[9];
#pragma unroll
      for (int k = 0; k < 9; ++k) xv[k] = bf2f(ldb16(rx, (lane + 64 * k) * 2));
      const int p0 = wave * 64 + lane, pr = p0 < 144 ? p0 : 143;
      const unsigned xo = __builtin_amdgcn_raw_buffer_load_b32(rx, (qr * 288 + 2 * pr) * 2, 0, 0);
      float s = 0.f;
#pragma unroll
      for (int k = 0; k < 9; ++k) s += xv[k];
      const float mf = wave_sum(s) * (1.f / D);
      float v = 0.f;
#pragma unroll
      for (int k = 0; k < 9; ++k) { const float d = xv[k] - mf; v = fmaf(d, d, v); }
      const float rden = ln_rden(wave_sum(v) * (1.f / D)), mean = rbf(mf);
      tag = wave == 0 ? tag_put(L, st, lane) : make_tag(st);
      const int hw = pr / 24, hr = 2 * pr - hw * 48;
      unsigned hdst = F_H + ((hw * 4 + (hr & 3)) * 12 + (hr >> 2)) * 4;
      float ln0 = rbf(rbf(__uint_as_float(xo << 16) - mean) * rden), ln1 = rbf(rbf(__uint_as_float(xo & 0xFFFF0000u) - mean) * rden);
      PIN(hdst); PIN(ln0); PIN(ln1);
      gate_wait(t0, GATE_H1);
      G2 ga[8];
      gather2<8>(rw, tag, ga, [&](int k) { return G_MOD + (k & 3) * NMOD + (k >> 2) * D + qr * 288 + 2 * pr; });
      const float sa0 = rbf(fadd(fadd(ga[0].lo(), ga[1].lo()), fadd(ga[2].lo(), ga[3].lo()))), sa1 = rbf(fadd(fadd(ga[0].hi(), ga[1].hi()), fadd(ga[2].hi(), ga[3].hi())));
      const float ca0 = rbf(fadd(fadd(ga[4].lo(), ga[5].lo()), fadd(ga[6].lo(), ga[7].lo()))), ca1 = rbf(fadd(fadd(ga[4].hi(), ga[5].hi()), fadd(ga[6].hi(), ga[7].hi())));
      *(LAS unsigned*)(L + hdst) = pkr(rbf(ln0 * rbf(1.f + ca0)) + sa0);
      *(LAS unsigned*)(L + hdst + 48) = pkr(rbf(ln1 * rbf(1.f + ca1)) + sa1);
      __builtin_amdgcn_s_setprio(0);
    }
    Mask mq = make_mask(lane, (lane & 8) == 0);
    const int qe = (wave & 1) * 64 + lane;
    unsigned hsrc = F_H + ((wave - 3) * 4 + (lane >> 4)) * 48, qst = F_RED + 4608 + (wave - 3) * 1024 + lane * 16;
    unsigned qsrc = F_RED + 4608 + ((16 * ((qe & 7) >> 2) + (qe >> 3)) * 4 + (qe & 3)) * 4;
    gu64* qdst = (gu64*)ws + G_Q + (hq * 2 + qr) * 128 + qe;
    PIN(mq.m[0]); PIN(mq.m[1]); PIN(mq.m[2]); PIN(mq.m[3]); PIN(hsrc); PIN(qst); PIN(qsrc); PIN(qdst);
    bar();
    if (wave >= 3) {
      const LAS v4u* hp = (const LAS v4u*)(L + hsrc);
      const v4u h0 = hp[0], h1 = hp[1], h2 = hp[2];
      f32x4 d0 = {0.f, 0.f, 0.f, 0.f}, d1 = {0.f, 0.f, 0.f, 0.f};
#pragma unroll
      for (int t = 0; t < 4; ++t) { d0 = mfma(afrag(h0[t], mq), wa[t], d0); d1 = mfma(afrag(h1[t], mq), wa[4 + t], d1); }
#pragma unroll
      for (int t = 0; t < 4; t += 2) { d0 = mfma(afrag(h2[t], mq), wa[8 + t], d0); d1 = mfma(afrag(h2[t + 1], mq), wa[9 + t], d1); }
      *(LAS f32x4*)(L + qst) = d0 + d1;
    }
    bar();
    if (wave < 2) {
      const LAS float* src = (const LAS float*)(L + qsrc);
      pubp(qdst, fadd(fadd(fadd(src[0], src[256]), fadd(src[512], src[768])), fadd(src[1024], src[1280])), tag);
    }
  } else {
    if (wave == 0) tag_put(L, load_state(ws), lane);
    gate_wait(t0, GATE_MODB2);
    ModA ma;
    if (wave < 6) moda_load(p, 72 + 6 * x + wave, lane, ma);
    __builtin_amdgcn_sched_barrier(0);
    bar();
    if (wave < 6) {
      tag = tag_get(L);
      moda_finish(L, ws, 72 + 6 * x + wave, lane, ma, mk, (LAS float*)(L + F_RED + wave * 1024), tag);
    }
  }
  tag = tag_get(L);
  pretouch<1>(L, ws, [&](int) { const int e = wave * 64 + lane; return G_X2 + (e >> 3) * 16 + (e & 7); });
  const bool slow_tile = ((((unsigned)(uintptr_t)p.wg >> 7) + f) & 7) == 3;
  v4u wg[9], wu[9], wd[8];
  if (!slow_tile) gate_wait(t0, GATE_FFN);
  if (wave < 8) {
    const rsrc_t rg = mk_rsrc(p.wg, D * FF * 2), ru = mk_rsrc(p.wu, D * FF * 2);
    const unsigned v = (lane >> 3) * (FF * 2) + (lane & 7) * 16, s = wave * 72 * (FF * 2) + f * 128;
#pragma unroll
    for (int t = 0; t < 9; ++t) { wg[t] = ldb(rg, v, s + t * 8 * (FF * 2)); wu[t] = ldb(ru, v, s + t * 8 * (FF * 2)); }
  }
  __builtin_amdgcn_sched_barrier(0);
  unsigned h2w, h2r, stw, str_, gur, guw0, actw, actr, trw, trr;
  gu64* ddst;
  {
    const int e = wave * 64 + lane, hw = e / 72, rem = e - hw * 72, c = (wave & 3) * 16 + (lane >> 2);
    h2w = F_H2 + ((hw * 8 + (rem & 7)) * 12 + (rem >> 3)) * 4;
    h2r = F_H2 + ((wave & 7) * 8 + (lane >> 3)) * 48;
    stw = F_ST + wave * 8;
    str_ = F_ST + ((lane & 15) < 9 ? (lane & 15) : 9) * 8;
    guw0 = F_RED + (wave & 7) * 1040 + red_off(lane, 256);
    gur = F_RED + ((lane & 3) * 520 + c) * 4;
    actw = F_ACT + ((lane & 3) == 0 ? (c & 7) * 8 + (c >> 3) : 64) * 4;
    actr = F_ACT + (lane >> 3) * 32;
    trw = F_TR + wave * 640 + red_off(lane, 128);
    trr = F_TR + wave * 640 + lane * 4;
    ddst = (gu64*)ws + G_DN + f * 576 + wave * 64 + lane;
    if (wave == 0 && lane == 0) *(LAS f32x2*)(L + F_ST + 72) = f32x2{0.f, 0.f};
  }
  PIN(h2w); PIN(h2r); PIN(stw); PIN(str_); PIN(guw0); PIN(gur); PIN(actw); PIN(actr); PIN(trw); PIN(trr); PIN(ddst);
  {
    const int e = wave * 64 + lane;
    u64 gs[9];
    bool have_x2 = false;
    for (int spin = 0; spin < SPIN_CAP; ++spin) {
#pragma unroll
      for (int k = 0; k < 8; ++k) gs[k] = peek(ws, G_MOD + (k & 3) * NMOD + (k < 4 ? 3 : 4) * D + e);
      gs[8] = peek(ws, G_X2 + (e >> 3) * 16 + (e & 7));
      Chk c, cx;
#pragma unroll
      for (int k = 0; k < 8; ++k) c.add(gs[k], tag);
      cx.add(gs[8], tag);
      have_x2 = cx.all_ok();
      if (c.all_ok()) break;
    }
    float sb = rbf(fadd(fadd(gval(gs[0]), gval(gs[1])), fadd(gval(gs[2]), gval(gs[3]))));
    float c1b = rbf(1.f + rbf(fadd(fadd(gval(gs[4]), gval(gs[5])), fadd(gval(gs[6]), gval(gs[7])))));
    float on = (lane & 15) < 9 ? 64.f : 0.f;
    PIN(sb); PIN(c1b); PIN(on);
    u64 gx[1] = {gs[8]};
    if (!have_x2) gather<1>(ws, tag, gx, [&](int) { return G_X2 + (e >> 3) * 16 + (e & 7); });
    const float xv = gval(gx[0]);
    const float sh = __uint_as_float(__builtin_amdgcn_readfirstlane(__float_as_uint(xv)));
    const float d = xv - sh;
    float s1 = d, s2 = d * d;
    s1 += dpp<ROR8>(s1); s2 += dpp<ROR8>(s2); s1 += dpp<ROR4>(s1); s2 += dpp<ROR4>(s2);
    s1 += dpp<ROR2>(s1); s2 += dpp<ROR2>(s2); s1 += dpp<ROR1>(s1); s2 += dpp<ROR1>(s2);
    s1 = x16_sum(s1); s2 = x16_sum(s2);
    const float dm = s1 * (1.f / 64.f);
    if (lane == 0) *(LAS f32x2*)(L + stw) = f32x2{sh + dm, fmaf(-dm, s1, s2)};
    bar();
    const f32x2 st = *(const LAS f32x2*)(L + str_);
    __builtin_amdgcn_sched_barrier(0);
    {
      const rsrc_t rd = mk_rsrc(p.wd, FF * D * 2);
      const unsigned v = (lane >> 3) * (D * 2) + (lane & 7) * 16, s = f * 64 * (D * 2) + wave * 128;
#pragma unroll
      for (int t = 0; t < DN_SECOND; ++t) wd[t] = ldb(rd, v, s + t * 8 * (D * 2));
      __builtin_amdgcn_sched_barrier(0);
    }
    const float mf = row_sum(st[0]) * (1.f / 9.f);
    const float dv = st[0] - mf;
    const float m2 = row_sum(fmaf(on * dv, dv, st[1]));
    *(LAS unsigned*)(L + h2w) = ln_apply_pk(xv, rbf(mf), ln_rden(m2 * (1.f / D)), c1b, sb);
  }
  bar();
  if (f == 0 && threadIdx.x == 0) __hip_atomic_store((gu64*)ws + G_EPOCH, ((u64)tag.hi << 32) | (u64)tag.rest, RLX_AGENT);
  v4u h0, h1;
  unsigned h8;
  {
    const LAS v4u* hp = (const LAS v4u*)(L + h2r);
    h0 = hp[0]; h1 = hp[1]; h8 = ((const LAS unsigned*)hp)[8];
  }
  __builtin_amdgcn_sched_barrier(0);
  {
    const rsrc_t rd = mk_rsrc(p.wd, FF * D * 2);
    const unsigned v = (lane >> 3) * (D * 2) + (lane & 7) * 16, s = f * 64 * (D * 2) + wave * 128;
#pragma unroll
    for (int t = DN_SECOND; t < 8; ++t) wd[t] = ldb(rd, v, s + t * 8 * (D * 2));
    __builtin_amdgcn_sched_barrier(0);
  }
  if (wave < 8) {
    PIN(h0); PIN(h1); PIN(h8);
    f32x4 dg = {0.f, 0.f, 0.f, 0.f}, du = {0.f, 0.f, 0.f, 0.f};
#pragma unroll
    for (int t = 0; t < 9; ++t) {
      const v4u a = afrag(t < 4 ? h0[t & 3] : t < 8 ? h1[t & 3] : h8, mk);
      dg = mfma(a, wg[t], dg);
      du = mfma(a, wu[t], du);
    }
    *(LAS f32x4*)(L + guw0) = dg;
    *(LAS f32x4*)(L + guw0 + ((lane >> 5) == ((lane >> 3) & 1) ? 512 : 0)) = du;
  }
  bar();
  if (wave < 4) {
    const LAS float* r0 = (const LAS float*)(L + gur);
    float g = faddn(fadd(r0[0], r0[64]), fadd(r0[260], r0[324])), u = faddn(fadd(r0[128], r0[192]), fadd(r0[388], r0[452]));
    g += dpp<XOR1>(g); u += dpp<XOR1>(u);
    g += dpp<XOR2>(g); u += dpp<XOR2>(u);
    g = rbf(g); u = rbf(u);
    const float y = g * fmaf(g * g, 0.044715f * 0.7978845608028654f * 2.f * LOG2E, 0.7978845608028654f * 2.f * LOG2E);
    const float th = fmaf(-2.f, rcp_f(__builtin_amdgcn_exp2f(y) + 1.f), 1.f);
    const float act = rbf(0.5f * g * (1.f + th)) * u;
    *(LAS unsigned*)(L + actw) = pkr(act);
  }
  bar();
  {
    const LAS v4u* ap = (const LAS v4u*)(L + actr);
    const v4u a0 = ap[0], a1 = ap[1];
    f32x4 d0 = {0.f, 0.f, 0.f, 0.f}, d1 = {0.f, 0.f, 0.f, 0.f};
#pragma unroll
    for (int t = 0; t < 4; ++t) { d0 = mfma(afrag(a0[t], mk), wd[t], d0); d1 = mfma(afrag(a1[t], mk), wd[4 + t], d1); }
    *(LAS f32x4*)(L + trw) = d0 + d1;
    lds_wait();
    pubp(ddst, fadd(*(const LAS float*)(L + trr), *(const LAS float*)(L + trr + 256)), tag);
  }
}

DEV void role_modb(const Params& p, LAS unsigned char* L, int x, int wave, int lane, u64 t0, u64 code0) {
  if (wave == 8) {
    const unsigned dump = (unsigned)(uintptr_t)(L + L_PF);
#pragma unroll
    for (int k = 0; k < (CODE_PF + 8191) / 8192; ++k) {
      const unsigned o = k * 8192 + lane * 128;
      asm volatile("s_mov_b32 m0, %0\n\ts_nop 0\n\tglobal_load_lds_dword %1, off" :: "s"(dump), "v"(code0 + (o < CODE_PF - 128 ? o : CODE_PF - 128)) : "m0", "memory");
    }
  }
  const Mask mk = make_mask(lane, true);
  if (wave == 0) tag_put(L, load_state(p.ws), lane);
  bar();
  if (wave == 8) {
    gate_wait(t0, 1350);
    asm volatile("buffer_inv sc1\n\ts_waitcnt vmcnt(0)" ::: "memory");
    return;
  }
  const Tag tag = tag_get(L);
  gate_wait(t0, GATE_MODB);
  for (int k = wave; k < 12; k += 8) {
    ModA ma;
    moda_load(p, 120 + x + 8 * k, lane, ma);
    __builtin_amdgcn_sched_barrier(0);
    moda_finish(L, p.ws, 120 + x + 8 * k, lane, ma, mk, (LAS float*)(L + wave * 1024), tag);
  }
}

__global__ void __launch_bounds__(NTHR) adaln_block_kernel(Params p) {
  __shared__ __attribute__((aligned(16))) unsigned char smem[L_END];
  LAS unsigned char* L = (LAS unsigned char*)smem;
  const int lane = threadIdx.x & 63, wave = __builtin_amdgcn_readfirstlane(threadIdx.x >> 6);
  u64 code0;
  asm volatile("s_getpc_b64 %0" : "=s"(code0));
  code0 &= ~0xFFull;
  const u64 t0 = __builtin_amdgcn_s_memrealtime();
  const int x = blockIdx.x & 7, idx = blockIdx.x >> 3;
  if (idx < 22) role_att(p, L, x, idx, wave, lane, t0);
  else if (idx < 31) role_ffn(p, L, x, idx - 22, wave, lane, t0);
  else role_modb(p, L, x, wave, lane, t0, code0);
}

extern "C" void kernel_launch(void* const* d_in, const int* in_sizes, int n_in, void* d_out, int out_size, void* d_ws,
                              size_t ws_size, hipStream_t stream) {
  Params p;
  p.x = (const u16*)d_in[0];
  p.kc = (const u16*)d_in[1];
  p.vc = (const u16*)d_in[2];
  p.wq = (const u16*)d_in[3];
  p.wo = (const u16*)d_in[4];
  p.wg = (const u16*)d_in[6];
  p.wu = (const u16*)d_in[7];
  p.wd = (const u16*)d_in[8];
  p.c = (const u16*)d_in[9];
  p.wmod = (const u16*)d_in[10];
  p.out = (u16*)d_out;
  p.ws = (u64*)d_ws;
  hipLaunchKernelGGL(adaln_block_kernel, dim3(256), dim3(NTHR), 0, stream, p);
}
```

```cpp
#include <hip/hip_runtime.h>
#include <stdint.h>

typedef unsigned short u16;
typedef unsigned long long u64;
typedef unsigned v4u __attribute__((ext_vector_type(4)));
typedef float f32x4 __attribute__((ext_vector_type(4)));
typedef float f32x2 __attribute__((ext_vector_type(2)));
typedef __bf16 bf16x2 __attribute__((ext_vector_type(2)));
typedef short bf16x8 __attribute__((ext_vector_type(8)));
typedef __amdgpu_buffer_rsrc_t rsrc_t;
#define GAS __attribute__((address_space(1)))
#define LAS __attribute__((address_space(3)))
#define DEV __device__ __forceinline__
typedef GAS u64 gu64;
#define RLX_AGENT __ATOMIC_RELAXED, __HIP_MEMORY_SCOPE_AGENT

constexpr int D = 576, SK = 65536, WIN = 4096, DH = 128, FF = 4608, NMOD = 6 * D;
constexpr int NSPLIT = 11;
constexpr int NTHR = 576;
constexpr int SPIN_CAP = 1 << 16;
constexpr unsigned GATE_ATT = 140;
constexpr unsigned GATE_MODB = 200;
constexpr unsigned GATE_MODB2 = 120;
constexpr unsigned GATE_H1 = 260;
constexpr unsigned GATE_Q = 450;
constexpr unsigned GATE_FFN = 790;
constexpr int Q_STAGGER = 16;
constexpr int ATT_N1 = 5, ATT_D1 = 2;
constexpr int DN_SECOND = 4;
constexpr int CODE_PF = 21504;

constexpr int G_EPOCH = 0;
constexpr int G_MOD = 64;
constexpr int G_Q = G_MOD + 4 * NMOD;
constexpr int G_O = G_Q + 32 * 2 * 128;
constexpr int G_WO = G_O + 16 * 8 * NSPLIT * 64;
constexpr int G_X2 = G_WO + 128 * 576;
constexpr int G_DN = G_X2 + 72 * 16;
constexpr int G_END = G_DN + 72 * 576;

constexpr int L_K = 0;
constexpr int L_OL = 98304;
constexpr int L_Q = L_OL + 9216;
constexpr int L_MX = L_Q + 512;
constexpr int L_LS = L_MX + 64;
constexpr int L_OB = L_LS + 64;
constexpr int L_PF = L_OB + 128;
constexpr int L_WO = L_PF + 256;
constexpr int L_MA = L_WO + 36864;
constexpr int L_CNT = L_MA + 1536;
constexpr int L_TAG = L_MA + 1536 + 16;
constexpr int L_END = L_TAG + 16;
constexpr int F_RED = 0;
constexpr int F_H = 16384;
constexpr int F_H2 = 17664;
constexpr int F_ACT = 20736;
constexpr int F_TR = 21248;
constexpr int F_ST = 27136;

struct Params {
  const u16 *x, *kc, *vc, *wq, *wo, *wg, *wu, *wd, *c, *wmod;
  u16* out;
  u64* ws;
};

DEV float bf2f(unsigned b) { return __uint_as_float(b << 16); }
DEV unsigned cvt2(float lo, float hi) {
  const f32x2 v = {lo, hi};
  return __builtin_bit_cast(unsigned, __builtin_convertvector(v, bf16x2));
}
DEV unsigned pkr(float f) { return cvt2(f, f); }
DEV float rbf(float f) { return __uint_as_float(cvt2(f, f) & 0xFFFF0000u); }
constexpr int TAG_LO_BITS = 4;
constexpr unsigned TAGM = (1u << TAG_LO_BITS) - 1u;
struct Tag { unsigned hi, lo, rest; };
DEV Tag make_tag(u64 st) {
  const u64 nx = st * 6364136223846793005ull + 1442695040888963407ull;
  Tag t;
  t.hi = __builtin_amdgcn_readfirstlane((unsigned)(nx >> 32));
  t.rest = __builtin_amdgcn_readfirstlane((unsigned)nx);
  t.lo = t.rest >> (32 - TAG_LO_BITS);
  return t;
}
DEV u64 enc(float v, unsigned hi, unsigned lo) {
  const unsigned u = (__float_as_uint(v) + (TAGM + 1) / 2) & ~TAGM;
  return ((u64)hi << 32) | (u64)(u | lo);
}
DEV Tag tag_put(LAS unsigned char* L, u64 st, int lane) {
  const Tag t = make_tag(st);
  if (lane == 0) *(LAS v4u*)(L + L_TAG) = v4u{t.hi, t.lo, t.rest, 0u};
  return t;
}
DEV Tag tag_get(const LAS unsigned char* L) {
  const v4u v = *(const LAS v4u*)(L + L_TAG);
  Tag t; t.hi = __builtin_amdgcn_readfirstlane(v[0]); t.lo = __builtin_amdgcn_readfirstlane(v[1]); t.rest = __builtin_amdgcn_readfirstlane(v[2]);
  return t;
}
DEV unsigned or_xor(unsigned a, unsigned b, unsigned c) { return __builtin_amdgcn_bitop3_b32(a, b, c, 0xF6); }
struct Chk {
  unsigned a = 0, b = 0;
  DEV void add(u64 g, const Tag& t) { a = or_xor(a, (unsigned)(g >> 32), t.hi); b = or_xor(b, (unsigned)g, t.lo); }
  DEV bool all_ok() const { return __builtin_amdgcn_ballot_w64((a | (b & TAGM)) != 0u) == 0ull; }
};
DEV float gval(u64 g) { return __uint_as_float((unsigned)g & ~TAGM); }
DEV void pub(u64* ws, int idx, float v, const Tag& t) { __hip_atomic_store((gu64*)ws + idx, enc(v, t.hi, t.lo), RLX_AGENT); }
DEV void pubp(gu64* dst, float v, const Tag& t) { __hip_atomic_store(dst, enc(v, t.hi, t.lo), RLX_AGENT); }
DEV u64 peek(u64* ws, int idx) { return __hip_atomic_load((gu64*)ws + idx, RLX_AGENT); }
template <int N, class F> DEV void gather(u64* ws, const Tag& t, u64 (&g)[N], F idx) {
  for (int spin = 0; spin < SPIN_CAP; ++spin) {
#pragma unroll
    for (int k = 0; k < N; ++k) g[k] = peek(ws, idx(k));
    Chk c;
#pragma unroll
    for (int k = 0; k < N; ++k) c.add(g[k], t);
    if (c.all_ok()) break;
  }
}

struct G2 { v4u v; DEV float lo() const { return __uint_as_float(v[0] & ~TAGM); } DEV float hi() const { return __uint_as_float(v[2] & ~TAGM); } };
template <int N, class F> DEV void gather2(rsrc_t rw, const Tag& t, G2 (&g)[N], F idx) {
  for (int spin = 0; spin < SPIN_CAP; ++spin) {
    asm volatile("" ::: "memory");
#pragma unroll
    for (int k = 0; k < N; ++k) g[k].v = __builtin_amdgcn_raw_buffer_load_b128(rw, idx(k) * 8, 0, 16);
    unsigned a = 0, b = 0;
#pragma unroll
    for (int k = 0; k < N; ++k) { a = or_xor(or_xor(a, g[k].v[1], t.hi), g[k].v[3], t.hi); b = or_xor(or_xor(b, g[k].v[0], t.lo), g[k].v[2], t.lo); }
    if (__builtin_amdgcn_ballot_w64((a | (b & TAGM)) != 0u) == 0ull) break;
  }
}
DEV void q_handoff(rsrc_t rw, unsigned voff, unsigned qdst, const Tag& t) {
  static_assert(TAG_LO_BITS == 4, "masks below");
  unsigned cnt;
  asm volatile(
      "s_mov_b32 %[cnt], %[cap]\n\t"
      "buffer_load_dwordx4 v[100:103], %[vo], %[rs], 0 offen sc1\n\t"
      "buffer_load_dwordx4 v[104:107], %[vo], %[rs], 0 offen offset:1024 sc1\n\t"
      "buffer_load_dwordx4 v[108:111], %[vo], %[rs], 0 offen offset:2048 sc1\n\t"
      "buffer_load_dwordx4 v[112:115], %[vo], %[rs], 0 offen offset:3072 sc1\n\t"
      "s_sleep %[stag]\n\t"
      "1:\n\t"
      "buffer_load_dwordx4 v[116:119], %[vo], %[rs], 0 offen sc1\n\t"
      "buffer_load_dwordx4 v[120:123], %[vo], %[rs], 0 offen offset:1024 sc1\n\t"
      "buffer_load_dwordx4 v[124:127], %[vo], %[rs], 0 offen offset:2048 sc1\n\t"
      "buffer_load_dwordx4 v[128:131], %[vo], %[rs], 0 offen offset:3072 sc1\n\t"
      "s_waitcnt vmcnt(4)\n\t"
      "v_xor_b32 v132, %[th], v101\n\t"
      "v_bitop3_b32 v132, v132, v103, %[th] bitop3:0xf6\n\t"
      "v_bitop3_b32 v132, v132, v105, %[th] bitop3:0xf6\n\t"
      "v_bitop3_b32 v132, v132, v107, %[th] bitop3:0xf6\n\t"
      "v_bitop3_b32 v132, v132, v109, %[th] bitop3:0xf6\n\t"
      "v_bitop3_b32 v132, v132, v111, %[th] bitop3:0xf6\n\t"
      "v_bitop3_b32 v132, v132, v113, %[th] bitop3:0xf6\n\t"
      "v_bitop3_b32 v132, v132, v115, %[th] bitop3:0xf6\n\t"
      "v_xor_b32 v133, %[tl], v100\n\t"
      "v_bitop3_b32 v133, v133, v102, %[tl] bitop3:0xf6\n\t"
      "v_bitop3_b32 v133, v133, v104, %[tl] bitop3:0xf6\n\t"
      "v_bitop3_b32 v133, v133, v106, %[tl] bitop3:0xf6\n\t"
      "v_bitop3_b32 v133, v133, v108, %[tl] bitop3:0xf6\n\t"
      "v_bitop3_b32 v133, v133, v110, %[tl] bitop3:0xf6\n\t"
      "v_bitop3_b32 v133, v133, v112, %[tl] bitop3:0xf6\n\t"
      "v_bitop3_b32 v133, v133, v114, %[tl] bitop3:0xf6\n\t"
      "v_and_b32 v133, 15, v133\n\t"
      "v_or_b32 v132, v132, v133\n\t"
      "v_cmp_ne_u32 vcc, 0, v132\n\t"
      "s_cbranch_vccz 2f\n\t"
      "buffer_load_dwordx4 v[100:103], %[vo], %[rs], 0 offen sc1\n\t"
      "buffer_load_dwordx4 v[104:107], %[vo], %[rs], 0 offen offset:1024 sc1\n\t"
      "buffer_load_dwordx4 v[108:111], %[vo], %[rs], 0 offen offset:2048 sc1\n\t"
      "buffer_load_dwordx4 v[112:115], %[vo], %[rs], 0 offen offset:3072 sc1\n\t"
      "s_waitcnt vmcnt(4)\n\t"
      "v_xor_b32 v132, %[th], v117\n\t"
      "v_bitop3_b32 v132, v132, v119, %[th] bitop3:0xf6\n\t"
      "v_bitop3_b32 v132, v132, v121, %[th] bitop3:0xf6\n\t"
      "v_bitop3_b32 v132, v132, v123, %[th] bitop3:0xf6\n\t"
      "v_bitop3_b32 v132, v132, v125, %[th] bitop3:0xf6\n\t"
      "v_bitop3_b32 v132, v132, v127, %[th] bitop3:0xf6\n\t"
      "v_bitop3_b32 v132, v132, v129, %[th] bitop3:0xf6\n\t"
      "v_bitop3_b32 v132, v132, v131, %[th] bitop3:0xf6\n\t"
      "v_xor_b32 v133, %[tl], v116\n\t"
      "v_bitop3_b32 v133, v133, v118, %[tl] bitop3:0xf6\n\t"
      "v_bitop3_b32 v133, v133, v120, %[tl] bitop3:0xf6\n\t"
      "v_bitop3_b32 v133, v133, v122, %[tl] bitop3:0xf6\n\t"
      "v_bitop3_b32 v133, v133, v124, %[tl] bitop3:0xf6\n\t"
      "v_bitop3_b32 v133, v133, v126, %[tl] bitop3:0xf6\n\t"
      "v_bitop3_b32 v133, v133, v128, %[tl] bitop3:0xf6\n\t"
      "v_bitop3_b32 v133, v133, v130, %[tl] bitop3:0xf6\n\t"
      "v_and_b32 v133, 15, v133\n\t"
      "v_or_b32 v132, v132, v133\n\t"
      "v_cmp_ne_u32 vcc, 0, v132\n\t"
      "s_cbranch_vccz 3f\n\t"
      "s_sub_u32 %[cnt], %[cnt], 1\n\t"
      "s_cbranch_scc0 1b\n\t"
      "2:\n\t"
      "v_and_b32 v132, 0xfffffff0, v100\n\t"
      "v_and_b32 v133, 0xfffffff0, v104\n\t"
      "v_add_f32 v132, v132, v133\n\t"
      "v_and_b32 v133, 0xfffffff0, v102\n\t"
      "v_and_b32 v136, 0xfffffff0, v106\n\t"
      "v_add_f32 v133, v133, v136\n\t"
      "v_cvt_pk_bf16_f32 v132, v132, v133\n\t"
      "ds_write_b32 %[qd], v132\n\t"
      "v_and_b32 v134, 0xfffffff0, v108\n\t"
      "v_and_b32 v135, 0xfffffff0, v112\n\t"
      "v_add_f32 v134, v134, v135\n\t"
      "v_and_b32 v135, 0xfffffff0, v110\n\t"
      "v_and_b32 v136, 0xfffffff0, v114\n\t"
      "v_add_f32 v135, v135, v136\n\t"
      "v_cvt_pk_bf16_f32 v134, v134, v135\n\t"
      "ds_write_b32 %[qd], v134 offset:256\n\t"
      "s_branch 4f\n\t"
      "3:\n\t"
      "v_and_b32 v132, 0xfffffff0, v116\n\t"
      "v_and_b32 v133, 0xfffffff0, v120\n\t"
      "v_add_f32 v132, v132, v133\n\t"
      "v_and_b32 v133, 0xfffffff0, v118\n\t"
      "v_and_b32 v136, 0xfffffff0, v122\n\t"
      "v_add_f32 v133, v133, v136\n\t"
      "v_cvt_pk_bf16_f32 v132, v132, v133\n\t"
      "ds_write_b32 %[qd], v132\n\t"
      "v_and_b32 v134, 0xfffffff0, v124\n\t"
      "v_and_b32 v135, 0xfffffff0, v128\n\t"
      "v_add_f32 v134, v134, v135\n\t"
      "v_and_b32 v135, 0xfffffff0, v126\n\t"
      "v_and_b32 v136, 0xfffffff0, v130\n\t"
      "v_add_f32 v135, v135, v136\n\t"
      "v_cvt_pk_bf16_f32 v134, v134, v135\n\t"
      "ds_write_b32 %[qd], v134 offset:256\n\t"
      "4:\n\t"
      "s_waitcnt lgkmcnt(0)\n\t"
      "s_barrier\n\t"
      "s_waitcnt vmcnt(0)"
      : [cnt] "=&s"(cnt)
      : [vo] "v"(voff), [rs] "s"(rw), [th] "s"(t.hi), [tl] "s"(t.lo), [qd] "v"(qdst), [cap] "n"(SPIN_CAP / 2), [stag] "n"(Q_STAGGER)
      : "vcc", "scc", "memory", "v100", "v101", "v102", "v103", "v104", "v105", "v106", "v107", "v108", "v109", "v110", "v111", "v112", "v113", "v114", "v115", "v116", "v117", "v118", "v119", "v120", "v121", "v122", "v123", "v124", "v125", "v126", "v127", "v128", "v129", "v130", "v131", "v132", "v133", "v134", "v135", "v136");
}
template <int N, class F> DEV void pretouch(LAS unsigned char* L, u64* ws, F idx) {
  const unsigned dump = (unsigned)(uintptr_t)(L + L_PF);
#pragma unroll
  for (int k = 0; k < N; ++k)
    asm volatile("s_mov_b32 m0, %0\n\ts_nop 0\n\tglobal_load_lds_dword %1, off sc1" :: "s"(dump), "v"((gu64*)ws + idx(k)) : "m0", "memory");
}
DEV f32x4 mfma(v4u a, v4u b, f32x4 c) {
  return __builtin_amdgcn_mfma_f32_16x16x32_bf16(__builtin_bit_cast(bf16x8, a), __builtin_bit_cast(bf16x8, b), c, 0, 0, 0);
}
DEV rsrc_t mk_rsrc(const void* p, unsigned bytes) { return __builtin_amdgcn_make_buffer_rsrc((void*)p, 0, (int)bytes, 0x00020000); }
DEV v4u ldb(rsrc_t r, unsigned voff, unsigned soff) { return __builtin_amdgcn_raw_buffer_load_b128(r, (int)voff, (int)soff, 2); }
DEV unsigned ldb16(rsrc_t r, unsigned voff) { return __builtin_amdgcn_raw_buffer_load_b16(r, (int)voff, 0, 0); }
DEV u64 load_state(u64* ws) {
  int zoff = G_EPOCH * 8;
  asm volatile("" : "+v"(zoff));
  typedef unsigned v2u_ __attribute__((ext_vector_type(2)));
  const v2u_ v = __builtin_amdgcn_raw_buffer_load_b64(mk_rsrc(ws, 64), zoff, 0, 16);
  return ((u64)v[1] << 32) | v[0];
}
DEV void bar() {
  asm volatile("s_waitcnt lgkmcnt(0)" ::: "memory");
  __builtin_amdgcn_s_barrier();
  asm volatile("" ::: "memory");
}
#define PIN(x) asm volatile("" : "+v"(x))
DEV void lds_wait() { asm volatile("s_waitcnt lgkmcnt(0)" ::: "memory"); }
template <int CTRL> DEV float dpp(float x) {
  return __builtin_bit_cast(float, __builtin_amdgcn_mov_dpp(__builtin_bit_cast(int, x), CTRL, 0xf, 0xf, true));
}
constexpr int ROR8 = 0x128, ROR4 = 0x124, ROR2 = 0x122, ROR1 = 0x121, XOR1 = 0xB1, XOR2 = 0x4E;
DEV float fmx(float a, float b) { float r; asm("v_max_f32 %0, %1, %2\n\ts_nop 1" : "=v"(r) : "v"(a), "v"(b)); return r; }
DEV float fmx3(float a, float b, float c) { float r; asm("v_max3_f32 %0, %1, %2, %3" : "=v"(r) : "v"(a), "v"(b), "v"(c)); return r; }
DEV float fmx3n(float a, float b, float c) { float r; asm("v_max3_f32 %0, %1, %2, %3\n\ts_nop 1" : "=v"(r) : "v"(a), "v"(b), "v"(c)); return r; }
DEV float x16_sum(float x) {
  auto s = __builtin_amdgcn_permlane16_swap(__float_as_uint(x), __float_as_uint(x), false, false);
  x = __uint_as_float(s[0]) + __uint_as_float(s[1]);
  auto t = __builtin_amdgcn_permlane32_swap(__float_as_uint(x), __float_as_uint(x), false, false);
  return __uint_as_float(t[0]) + __uint_as_float(t[1]);
}
DEV float x16_max(float x) {
  auto s = __builtin_amdgcn_permlane16_swap(__float_as_uint(x), __float_as_uint(x), false, false);
  x = fmaxf(__uint_as_float(s[0]), __uint_as_float(s[1]));
  auto t = __builtin_amdgcn_permlane32_swap(__float_as_uint(x), __float_as_uint(x), false, false);
  return fmaxf(__uint_as_float(t[0]), __uint_as_float(t[1]));
}
DEV float row_sum(float x) { x += dpp<ROR8>(x); x += dpp<ROR4>(x); x += dpp<ROR2>(x); x += dpp<ROR1>(x); return x; }
DEV float wave_sum(float x) { return x16_sum(row_sum(x)); }
constexpr float LOG2E = 1.4426950408889634f;
DEV float fadd(float a, float b) { float r; asm("v_add_f32 %0, %1, %2" : "=v"(r) : "v"(a), "v"(b)); return r; }
DEV float faddn(float a, float b) { float r; asm("v_add_f32 %0, %1, %2\n\ts_nop 1" : "=v"(r) : "v"(a), "v"(b)); return r; }
DEV float exp_f(float x) { return __builtin_amdgcn_exp2f(x * LOG2E); }
DEV float rcp_f(float x) { return __builtin_amdgcn_rcpf(x); }
DEV float pair_pick(float a, float b, int lane) {
  const float a2 = dpp<0x50>(a), b2 = dpp<0x50>(b);
  const float a3 = dpp<0xFA>(a), b3 = dpp<0xFA>(b);
  const float lo = (lane & 1) ? b2 : a2, hi = (lane & 1) ? b3 : a3;
  return (lane & 4) ? hi : lo;
}
DEV void gate_wait(u64 t0, unsigned ticks) { while (__builtin_amdgcn_s_memrealtime() - t0 < (u64)ticks) __builtin_amdgcn_s_sleep(2); }

struct Mask { unsigned m[4]; };
DEV Mask make_mask(int lane, bool on) {
  Mask k;
  const unsigned h = on ? ((lane & 1) ? 0xFFFF0000u : 0x0000FFFFu) : 0u;
#pragma unroll
  for (int r = 0; r < 4; ++r) k.m[r] = (((lane & 7) >> 1) == r) ? h : 0u;
  return k;
}
DEV v4u afrag(unsigned pp, const Mask& k) { return v4u{pp & k.m[0], pp & k.m[1], pp & k.m[2], pp & k.m[3]}; }

DEV unsigned ln_apply_pk(float x, float mean_b, float rden, float c1, float shift) {
  const float ln = rbf(rbf(x - mean_b) * rden);
  return pkr(rbf(ln * c1) + shift);
}
DEV float ln_rden(float var) { return rcp_f(rbf(__builtin_amdgcn_sqrtf(rbf(rbf(var) + 1e-5f)))); }

DEV unsigned red_off(int lane, int dump) {
  const int u = (lane >> 3) & 1;
  return 4 * ((lane >> 5) == u ? u * 64 + 8 * (lane & 7) + 4 * ((lane >> 4) & 1) : dump);
}
DEV unsigned silu_pk(unsigned craw) { const float v = bf2f(craw); return pkr(v * rcp_f(1.f + exp_f(-v))); }

struct WoAddr { unsigned tro, tri, obo, wro; gu64* dst; };
DEV WoAddr wo_prep(u64* ws, int pidx, int wave, int lane) {
  WoAddr a;
  a.tro = L_OL + wave * 640 + red_off(lane, 128); a.tri = L_OL + wave * 640 + lane * 4; a.obo = L_OB + (lane >> 3) * 16; a.wro = L_WO + wave * 4096 + lane * 16;
  a.dst = (gu64*)ws + G_WO + pidx * 576 + wave * 64 + lane;
  PIN(a.tro); PIN(a.tri); PIN(a.obo); PIN(a.wro); PIN(a.dst);
  return a;
}
DEV void wo_phase(LAS unsigned char* L, const WoAddr& a, const Tag& tag, const Mask& mk) {
  bar();
  v4u ob = *(const LAS v4u*)(L + a.obo);
  const LAS v4u* wr = (const LAS v4u*)(L + a.wro);
  v4u w0 = wr[0], w1 = wr[64], w2 = wr[128], w3 = wr[192];
  PIN(ob); PIN(w0); PIN(w1); PIN(w2); PIN(w3);
  __builtin_amdgcn_sched_barrier(0);
  f32x4 d0 = {0.f, 0.f, 0.f, 0.f}, d1 = {0.f, 0.f, 0.f, 0.f};
  d0 = mfma(afrag(ob[0], mk), w0, d0); d1 = mfma(afrag(ob[1], mk), w1, d1);
  d0 = mfma(afrag(ob[2], mk), w2, d0); d1 = mfma(afrag(ob[3], mk), w3, d1);
  *(LAS f32x4*)(L + a.tro) = d0 + d1;
  lds_wait();
  pubp(a.dst, fadd(*(const LAS float*)(L + a.tri), *(const LAS float*)(L + a.tri + 256)), tag);
}

struct ModA { v4u w[18]; unsigned c[3]; };
DEV void moda_load(const Params& p, int mt, int lane, ModA& m) {
  const rsrc_t rm = mk_rsrc(p.wmod, D * NMOD * 2), rc = mk_rsrc(p.c, D * 2);
  const int r0 = (mt & 3) * 144;
#pragma unroll
  for (int k = 0; k < 3; ++k) m.c[k] = ldb16(rc, (r0 + (k < 2 || lane < 16 ? lane + 64 * k : 143)) * 2);
  const unsigned v = (r0 + (lane >> 3)) * (NMOD * 2) + (lane & 7) * 16;
#pragma unroll
  for (int t = 0; t < 18; ++t) m.w[t] = ldb(rm, v, (mt >> 2) * 128 + t * 8 * (NMOD * 2));
}
DEV void moda_finish(LAS unsigned char* L, u64* ws, int mt, int lane, ModA& m, const Mask& mk, LAS float* tr, const Tag& tag) {
  unsigned sp[18];
  {
    const unsigned s0 = silu_pk(m.c[0]), s1 = silu_pk(m.c[1]), s2 = silu_pk(m.c[2]);
#pragma unroll
    for (int t = 0; t < 18; ++t) sp[t] = __builtin_amdgcn_ds_bpermute(((8 * t + (lane >> 3)) & 63) * 4, t < 8 ? s0 : t < 16 ? s1 : s2);
  }
  gu64* dst = (gu64*)ws + G_MOD + (mt & 3) * NMOD + (mt >> 2) * 64 + lane;
  unsigned tro = red_off(lane, 128);
  PIN(dst); PIN(tro);
#pragma unroll
  for (int t = 0; t < 18; ++t) PIN(sp[t]);
  __builtin_amdgcn_sched_barrier(0);
  f32x4 d0 = {0.f, 0.f, 0.f, 0.f}, d1 = {0.f, 0.f, 0.f, 0.f};
#pragma unroll
  for (int t = 0; t < 18; t += 2) {
    d0 = mfma(afrag(sp[t], mk), m.w[t], d0);
    __builtin_amdgcn_sched_barrier(0);
    d1 = mfma(afrag(sp[t + 1], mk), m.w[t + 1], d1);
    __builtin_amdgcn_sched_barrier(0);
  }
  *(LAS f32x4*)((LAS unsigned char*)tr + tro) = d0 + d1;
  lds_wait();
  pubp(dst, fadd(tr[lane], tr[64 + lane]), tag);
}

DEV void role_att(const Params& p, LAS unsigned char* L, int x, int i, int wave, int lane, u64 t0) {
  const int g = 2 * x + (i >= NSPLIT ? 1 : 0), j = i >= NSPLIT ? i - NSPLIT : i;
  const bool odd = g & 1;
  const int nm = odd ? 4 : 5;
  const bool moda = j < nm;
  const int ntile = !moda ? 24 : (!odd && j >= 3) ? 23 : 22;
  const int tile0 = !moda ? (odd ? 88 : 112) + 24 * (j - nm) : (!odd && j == 4) ? 89 : 22 * j;
  const bool owner = j < 8;
  int redc = -1;
  if (j >= 8) redc = 9 * x + (g & 1) * 3 + (j - 8);
  else if (j == 0) redc = 9 * x + 6 + (g & 1);
  else if (j == 1 && (g & 1) == 0) redc = 9 * x + 8;
  u64* ws = p.ws;
  const Mask mk = make_mask(lane, true);
  const int kap = lane >> 4, cc = lane & 15;

  if (wave < 8) {
    const size_t hb = ((size_t)g * SK + (SK - WIN)) * DH;
    const rsrc_t rk = mk_rsrc(p.kc + hb, WIN * DH * 2), rv = mk_rsrc(p.vc + hb, WIN * DH * 2);
    const bool three = wave + 16 < ntile;
    const unsigned tso = (tile0 + wave) * 4096;
    const unsigned ldsk = (unsigned)(uintptr_t)(L + L_K) + wave * 3 * 4096;
    unsigned vk[4];
#pragma unroll
    for (int q = 0; q < 4; ++q) vk[q] = (4 * q + kap) * 256 + ((cc ^ (4 * q + kap)) << 4);
#define KDMA(s, q) asm volatile("s_mov_b32 m0, %0\n\ts_nop 0\n\tbuffer_load_dwordx4 %1, %2, %3 offen nt lds" \
                     :: "s"(ldsk + (s) * 4096 + (q) * 1024), "v"(vk[q]), "s"(rk), "s"(tso + (s) * 32768) : "m0", "memory")
#define THR(n) do { if ((n) < ATT_N1 && (n) >= ATT_D1 && moda) asm volatile("s_waitcnt vmcnt(%0)" :: "n"(ATT_D1 - 1) : "memory"); } while (0)
    if (moda) __builtin_amdgcn_s_barrier();
    __builtin_amdgcn_s_setprio(2);
    KDMA(0, 0);
    gate_wait(t0, GATE_ATT);
    THR(1); KDMA(0, 1); THR(2); KDMA(0, 2); THR(3); KDMA(0, 3);
    THR(4); KDMA(1, 0); KDMA(1, 1); KDMA(1, 2); KDMA(1, 3);
    if (three) { KDMA(2, 0); KDMA(2, 1); KDMA(2, 2); KDMA(2, 3); }
#undef KDMA
#undef THR
    __builtin_amdgcn_s_setprio(0);
    v4u vr[12];
    const unsigned vv = kap * 1024 + cc * 16;
#pragma unroll
    for (int t = 0; t < 8; ++t) vr[t] = ldb(rv, vv + (t & 3) * 256, tso + (t >> 2) * 32768);
    if (three) {
#pragma unroll
      for (int t = 8; t < 12; ++t) vr[t] = ldb(rv, vv + (t & 3) * 256, tso + 65536);
    } else {
#pragma unroll
      for (int t = 8; t < 12; ++t) vr[t] = v4u{0u, 0u, 0u, 0u};
    }
    __builtin_amdgcn_sched_barrier(0);
    if (three) asm volatile("s_waitcnt vmcnt(12)" ::: "memory");
    else asm volatile("s_waitcnt vmcnt(8)" ::: "memory");
    v4u kf[12];
#pragma unroll
    for (int s = 0; s < 3; ++s)
#pragma unroll
      for (int ks = 0; ks < 4; ++ks)
        kf[s * 4 + ks] = *(const LAS v4u*)(L + L_K + (wave * 3 + (s < 2 || three ? s : 0)) * 4096 + cc * 256 + (((4 * ks + kap) ^ cc) << 4));
#pragma unroll
    for (int t = 0; t < 12; ++t) PIN(kf[t]);
    const int head = (lane >> 3) & 1;
    const int me = (wave & 3) * 64 + lane;
    unsigned qsrc = L_Q + head * 256 + kap * 16, mxw = L_MX + (head * 8 + wave) * 4, mxr = L_MX + head * 32, olw = L_OL + wave * 1024 + lane * 16;
    unsigned msrc = L_OL + ((16 * (((me >> 7) * 8 + (me & 7)) >> 2) + ((me & 127) >> 3)) * 4 + (me & 3)) * 4;
    gu64* mdst = (gu64*)ws + G_O + ((g * 8 + (me >> 5)) * NSPLIT + j) * 64 + (me & 31);
    bool lead = (lane & 0x37) == 0;
    PIN(qsrc); PIN(mxw); PIN(mxr); PIN(olw); PIN(msrc); PIN(mdst);
    bar();
    v4u qf[4];
#pragma unroll
    for (int ks = 0; ks < 4; ++ks) qf[ks] = *(const LAS v4u*)(L + qsrc + ks * 64);
#pragma unroll
    for (int ks = 0; ks < 4; ++ks) PIN(qf[ks]);
    __builtin_amdgcn_sched_barrier(0);
    f32x4 acc[3] = {{0.f, 0.f, 0.f, 0.f}, {0.f, 0.f, 0.f, 0.f}, {0.f, 0.f, 0.f, 0.f}};
#pragma unroll
    for (int ks = 0; ks < 4; ++ks)
#pragma unroll
      for (int s = 0; s < 3; ++s) acc[s] = mfma(kf[s * 4 + ks], qf[ks], acc[s]);
    __builtin_amdgcn_sched_barrier(0);
    float sc[12];
#pragma unroll
    for (int t = 0; t < 12; t += 2) {
      const unsigned u = cvt2(acc[t >> 2][t & 3], acc[t >> 2][(t & 3) + 1]);
      const unsigned w = cvt2(__uint_as_float(u << 16) * 0.08838834764831845f, __uint_as_float(u & 0xFFFF0000u) * 0.08838834764831845f);
      sc[t] = __uint_as_float(w << 16);
      sc[t + 1] = __uint_as_float(w & 0xFFFF0000u);
    }
    if (!three) {
#pragma unroll
      for (int t = 8; t < 12; ++t) sc[t] = -INFINITY;
    }
    float mx = fmx3n(fmx3(fmx3(sc[0], sc[1], sc[2]), fmx3(sc[3], sc[4], sc[5]), fmx3(sc[6], sc[7], sc[8])), fmx3(sc[9], sc[10], sc[11]), sc[0]);
    mx = x16_max(mx);
    if (lead) *(LAS float*)(L + mxw) = mx;
    lds_wait();
    if (lane == 0) __hip_atomic_fetch_add((LAS unsigned*)(L + L_CNT), 1u, __ATOMIC_RELAXED, __HIP_MEMORY_SCOPE_WORKGROUP);
    while (*(volatile LAS unsigned*)(L + L_CNT) < 8u) {}
    asm volatile("" ::: "memory");
    float m;
    {
      const f32x4 a = *(const LAS f32x4*)(L + mxr), b = *(const LAS f32x4*)(L + mxr + 16);
      m = fmx3(fmx3(a[0], a[1], a[2]), fmx3(a[3], b[0], b[1]), fmx(b[2], b[3]));
    }
    v4u af[12];
    float ls0 = 0.f, ls1 = 0.f;
    const float mneg = -m * LOG2E;
#pragma unroll
    for (int t = 0; t < 12; t += 2) {
      const float e0 = __builtin_amdgcn_exp2f(fmaf(sc[t], LOG2E, mneg)), e1 = __builtin_amdgcn_exp2f(fmaf(sc[t + 1], LOG2E, mneg));
      ls0 += e0; ls1 += e1;
      af[t] = afrag(pkr(e0), mk); af[t + 1] = afrag(pkr(e1), mk);
    }
    const float ls = x16_sum(ls0 + ls1);
    if (lead) *(LAS float*)(L + mxw + 64) = ls;
    const Tag tag = tag_get(L);
    __builtin_amdgcn_sched_barrier(0);
    f32x4 o0 = {0.f, 0.f, 0.f, 0.f}, o1 = {0.f, 0.f, 0.f, 0.f};
#pragma unroll
    for (int t = 0; t < 12; t += 2) { o0 = mfma(af[t], vr[t], o0); o1 = mfma(af[t + 1], vr[t + 1], o1); }
    *(LAS f32x4*)(L + olw) = o0 + o1;
    bar();
    if (wave < 4) {
      const LAS float* src = (const LAS float*)(L + msrc);
      float s[8];
#pragma unroll
      for (int w = 0; w < 8; ++w) s[w] = src[w * 256];
      pubp(mdst, fadd(fadd(fadd(s[0], s[1]), fadd(s[2], s[3])), fadd(fadd(s[4], s[5]), fadd(s[6], s[7]))), tag);
    } else if (wave == 4 && lane < 16) {
      const int u = lane >> 1, hh = u >> 2;
      const f32x4 a = *(const LAS f32x4*)(L + ((lane & 1) ? L_LS : L_MX) + hh * 32), b = *(const LAS f32x4*)(L + ((lane & 1) ? L_LS : L_MX) + hh * 32 + 16);
      const float vs = ((a[0] + a[1]) + (a[2] + a[3])) + ((b[0] + b[1]) + (b[2] + b[3]));
      const float vm = fmaxf(fmaxf(fmaxf(a[0], a[1]), fmaxf(a[2], a[3])), fmaxf(fmaxf(b[0], b[1]), fmaxf(b[2], b[3])));
      pub(ws, G_O + ((g * 8 + u) * NSPLIT + j) * 64 + 32 + (lane & 1), (lane & 1) ? vs : vm, tag);
    }
    if (owner) {
      wo_phase(L, wo_prep(ws, g * 8 + j, wave, lane), tag, mk);
    }
  } else {
    __builtin_amdgcn_s_setprio(3);
    Tag tag;
    {
      if (moda) {
        u64 st = load_state(ws);
        ModA ma;
        moda_load(p, 9 * x + (odd ? 5 : 0) + j, lane, ma);
        __builtin_amdgcn_sched_barrier(0);
        __builtin_amdgcn_s_barrier();
        asm volatile("" : "+v"(st));
        tag = tag_put(L, st, lane);
        moda_finish(L, ws, 9 * x + (odd ? 5 : 0) + j, lane, ma, mk, (LAS float*)(L + L_MA), tag);
      } else {
        tag = tag_put(L, load_state(ws), lane);
      }
    }
    float xr = 0.f;
    if (redc >= 0) xr = bf2f(p.x[redc * 8 + (lane & 7)]);
    gate_wait(t0, GATE_Q);
    const rsrc_t rw = mk_rsrc(ws, G_END * 8);
    if (lane == 0) *(volatile LAS unsigned*)(L + L_CNT) = 0u;
    q_handoff(rw, (G_Q + 2 * g * 256 + 2 * lane) * 8, (unsigned)(uintptr_t)(L + L_Q) + lane * 4, tag);
    if (owner) {
      const int ob = G_O + (g * 8 + j) * NSPLIT * 64;
      pretouch<1>(L, ws, [&](int) { const int q_ = lane < 33 ? lane : 32; return ob + (q_ / 3) * 64 + (q_ % 3) * 16; });
    }
    if (redc >= 0) {
      pretouch<2>(L, ws, [&](int s) { return G_WO + (s * 64 + lane) * 576 + redc * 8; });
      pretouch<2>(L, ws, [&](int s) { const int f_ = s * 64 + lane; return G_DN + (f_ < 72 ? f_ : 71) * 576 + redc * 8; });
    }
    if (owner) {
      const rsrc_t rwo = mk_rsrc(p.wo, 4096 * D * 2);
      const unsigned wo_v = (lane >> 3) * (D * 2) + (lane & 7) * 16;
      const unsigned wo_s = (g * 256 + j * 32) * (D * 2), ldsw = (unsigned)(uintptr_t)(L + L_WO);
#pragma unroll
      for (int w = 0; w < 9; ++w)
#pragma unroll
        for (int t = 0; t < 4; ++t)
          asm volatile("s_mov_b32 m0, %0\n\ts_nop 0\n\tbuffer_load_dwordx4 %1, %2, %3 offen nt lds"
                       :: "s"(ldsw + (w * 4 + t) * 1024), "v"(wo_v), "s"(rwo), "s"(wo_s + w * 128 + t * 8 * (D * 2)) : "m0", "memory");
    }
    bar();
    if (owner) {
      const int ob = G_O + (g * 8 + j) * NSPLIT * 64;
      const int k2 = kap < 3 ? 8 + kap : 10;
      unsigned obw = L_OB + (((2 * cc) & 7) * 4 + (cc >> 2)) * 4;
      float live = kap < 3 ? 1.f : 0.f;
      PIN(obw); PIN(live);
      const WoAddr wa = wo_prep(ws, g * 8 + j, 8, lane);
      G2 go[6];
      gather2<6>(rw, tag, go, [&](int k) { const int sp = k % 3 == 0 ? kap : k % 3 == 1 ? 4 + kap : k2; return ob + sp * 64 + (k < 3 ? 2 * cc : 32); });
      const float M = x16_max(fmaxf(fmaxf(go[3].lo(), go[4].lo()), go[5].lo()));
      const float Mn = -M * LOG2E;
      const float w0 = __builtin_amdgcn_exp2f(fmaf(go[3].lo(), LOG2E, Mn)), w1 = __builtin_amdgcn_exp2f(fmaf(go[4].lo(), LOG2E, Mn));
      const float w2 = __builtin_amdgcn_exp2f(fmaf(go[5].lo(), LOG2E, Mn)) * live;
      const float Ls = x16_sum(fmaf(w2, go[5].hi(), fmaf(w1, go[4].hi(), w0 * go[3].hi())));
      const float o0 = x16_sum(fmaf(w2, go[2].lo(), fmaf(w1, go[1].lo(), w0 * go[0].lo())));
      const float o1 = x16_sum(fmaf(w2, go[2].hi(), fmaf(w1, go[1].hi(), w0 * go[0].hi())));
      const float rl = rcp_f(Ls);
      *(LAS unsigned*)(L + obw) = pkr(o0 * rl);
      *(LAS unsigned*)(L + obw + 16) = pkr(o1 * rl);
      asm volatile("s_waitcnt vmcnt(0)" ::: "memory");
      wo_phase(L, wa, tag, mk);
    }
    if (redc >= 0) {
      u64 gg[8];
      gather<8>(ws, tag, gg, [&](int k) { return G_MOD + (k & 3) * NMOD + (k < 4 ? 2 : 5) * D + redc * 8 + (lane & 7); });
      float ga = rbf(fadd(fadd(gval(gg[0]), gval(gg[1])), fadd(gval(gg[2]), gval(gg[3]))));
      float gb = rbf(fadd(fadd(gval(gg[4]), gval(gg[5])), fadd(gval(gg[6]), gval(gg[7]))));
      gu64* xdst = (gu64*)ws + G_X2 + redc * 16 + (lane & 7);
      GAS u16* odst = (GAS u16*)p.out + redc * 8 + (lane & 7);
      PIN(ga); PIN(gb); PIN(xr); PIN(xdst); PIN(odst);
      float x2;
      {
        G2 gw[8];
        gather2<8>(rw, tag, gw, [&](int s) { return G_WO + (s * 16 + (lane >> 2)) * 576 + redc * 8 + 2 * (lane & 3); });
        float t0_ = faddn(fadd(fadd(gw[0].lo(), gw[1].lo()), fadd(gw[2].lo(), gw[3].lo())), fadd(fadd(gw[4].lo(), gw[5].lo()), fadd(gw[6].lo(), gw[7].lo())));
        float t1_ = faddn(fadd(fadd(gw[0].hi(), gw[1].hi()), fadd(gw[2].hi(), gw[3].hi())), fadd(fadd(gw[4].hi(), gw[5].hi()), fadd(gw[6].hi(), gw[7].hi())));
        t0_ += dpp<ROR8>(t0_); t1_ += dpp<ROR8>(t1_); t0_ += dpp<ROR4>(t0_); t1_ += dpp<ROR4>(t1_);
        t0_ = x16_sum(t0_); t1_ = x16_sum(t1_);
        const float ts = pair_pick(t0_, t1_, lane);
        x2 = rbf(xr + rbf(ga * rbf(ts)));
        if (lane < 8) pubp(xdst, x2, tag);
      }
      {
        G2 gd[5];
        float w4 = lane < 32 ? 1.f : 0.f;
        PIN(w4);
        gather2<5>(rw, tag, gd, [&](int s) { const int f_ = s * 16 + (lane >> 2); return G_DN + (f_ < 72 ? f_ : 71) * 576 + redc * 8 + 2 * (lane & 3); });
        float t0_ = fmaf(w4, gd[4].lo(), fadd(fadd(gd[0].lo(), gd[1].lo()), fadd(gd[2].lo(), gd[3].lo())));
        float t1_ = fmaf(w4, gd[4].hi(), fadd(fadd(gd[0].hi(), gd[1].hi()), fadd(gd[2].hi(), gd[3].hi())));
        t0_ += dpp<ROR8>(t0_); t1_ += dpp<ROR8>(t1_); t0_ += dpp<ROR4>(t0_); t1_ += dpp<ROR4>(t1_);
        t0_ = x16_sum(t0_); t1_ = x16_sum(t1_);
        const float ts = pair_pick(t0_, t1_, lane);
        const unsigned o = pkr(x2 + rbf(gb * rbf(ts)));
        if (lane < 8) *odst = (u16)o;
      }
    }
  }
}

DEV void role_ffn(const Params& p, LAS unsigned char* L, int x, int i, int wave, int lane, u64 t0) {
  const int f = 9 * x + i;
  u64* ws = p.ws;
  const Mask mk = make_mask(lane, true);
  LAS float* red = (LAS float*)(L + F_RED);
  const rsrc_t rm = mk_rsrc(p.wmod, D * NMOD * 2), rc = mk_rsrc(p.c, D * 2), rx = mk_rsrc(p.x, D * 2);
  const int hq = 4 * x + (i >> 1), qr = i & 1;
  const rsrc_t rw = mk_rsrc(ws, G_END * 8);
  Tag tag;
  if (i < 8) {
    v4u wa[12];
    if (wave >= 3) {
      const rsrc_t rq = mk_rsrc(p.wq, D * 4096 * 2);
      const unsigned v = (lane >> 4) * 8192 + (lane & 15) * 16, s = (qr * 288 + (wave - 3) * 48) * 8192 + hq * 256;
#pragma unroll
      for (int t = 0; t < 12; ++t) wa[t] = ldb(rq, v, s + t * 4 * 8192);
      __builtin_amdgcn_sched_barrier(0);
    } else {
      __builtin_amdgcn_s_setprio(3);
      const u64 st = load_state(ws);
      float xv[9];
#pragma unroll
      for (int k = 0; k < 9; ++k) xv[k] = bf2f(ldb16(rx, (lane + 64 * k) * 2));
      const int p0 = wave * 64 + lane, pr = p0 < 144 ? p0 : 143;
      const unsigned xo = __builtin_amdgcn_raw_buffer_load_b32(rx, (qr * 288 + 2 * pr) * 2, 0, 0);
      float s = 0.f;
#pragma unroll
      for (int k = 0; k < 9; ++k) s += xv[k];
      const float mf = wave_sum(s) * (1.f / D);
      float v = 0.f;
#pragma unroll
      for (int k = 0; k < 9; ++k) { const float d = xv[k] - mf; v = fmaf(d, d, v); }
      const float rden = ln_rden(wave_sum(v) * (1.f / D)), mean = rbf(mf);
      tag = wave == 0 ? tag_put(L, st, lane) : make_tag(st);
      const int hw = pr / 24, hr = 2 * pr - hw * 48;
      unsigned hdst = F_H + ((hw * 4 + (hr & 3)) * 12 + (hr >> 2)) * 4;
      float ln0 = rbf(rbf(__uint_as_float(xo << 16) - mean) * rden), ln1 = rbf(rbf(__uint_as_float(xo & 0xFFFF0000u) - mean) * rden);
      PIN(hdst); PIN(ln0); PIN(ln1);
      gate_wait(t0, GATE_H1);
      G2 ga[8];
      gather2<8>(rw, tag, ga, [&](int k) { return G_MOD + (k & 3) * NMOD + (k >> 2) * D + qr * 288 + 2 * pr; });
      const float sa0 = rbf(fadd(fadd(ga[0].lo(), ga[1].lo()), fadd(ga[2].lo(), ga[3].lo()))), sa1 = rbf(fadd(fadd(ga[0].hi(), ga[1].hi()), fadd(ga[2].hi(), ga[3].hi())));
      const float ca0 = rbf(fadd(fadd(ga[4].lo(), ga[5].lo()), fadd(ga[6].lo(), ga[7].lo()))), ca1 = rbf(fadd(fadd(ga[4].hi(), ga[5].hi()), fadd(ga[6].hi(), ga[7].hi())));
      *(LAS unsigned*)(L + hdst) = pkr(rbf(ln0 * rbf(1.f + ca0)) + sa0);
      *(LAS unsigned*)(L + hdst + 48) = pkr(rbf(ln1 * rbf(1.f + ca1)) + sa1);
      __builtin_amdgcn_s_setprio(0);
    }
    Mask mq = make_mask(lane, (lane & 8) == 0);
    const int qe = (wave & 1) * 64 + lane;
    unsigned hsrc = F_H + ((wave - 3) * 4 + (lane >> 4)) * 48, qst = F_RED + 4608 + (wave - 3) * 1024 + lane * 16;
    unsigned qsrc = F_RED + 4608 + ((16 * ((qe & 7) >> 2) + (qe >> 3)) * 4 + (qe & 3)) * 4;
    gu64* qdst = (gu64*)ws + G_Q + (hq * 2 + qr) * 128 + qe;
    PIN(mq.m[0]); PIN(mq.m[1]); PIN(mq.m[2]); PIN(mq.m[3]); PIN(hsrc); PIN(qst); PIN(qsrc); PIN(qdst);
    bar();
    if (wave >= 3) {
      const LAS v4u* hp = (const LAS v4u*)(L + hsrc);
      const v4u h0 = hp[0], h1 = hp[1], h2 = hp[2];
      f32x4 d0 = {0.f, 0.f, 0.f, 0.f}, d1 = {0.f, 0.f, 0.f, 0.f};
#pragma unroll
      for (int t = 0; t < 4; ++t) { d0 = mfma(afrag(h0[t], mq), wa[t], d0); d1 = mfma(afrag(h1[t], mq), wa[4 + t], d1); }
#pragma unroll
      for (int t = 0; t < 4; t += 2) { d0 = mfma(afrag(h2[t], mq), wa[8 + t], d0); d1 = mfma(afrag(h2[t + 1], mq), wa[9 + t], d1); }
      *(LAS f32x4*)(L + qst) = d0 + d1;
    }
    bar();
    if (wave < 2) {
      const LAS float* src = (const LAS float*)(L + qsrc);
      pubp(qdst, fadd(fadd(fadd(src[0], src[256]), fadd(src[512], src[768])), fadd(src[1024], src[1280])), tag);
    }
  } else {
    if (wave == 0) tag_put(L, load_state(ws), lane);
    gate_wait(t0, GATE_MODB2);
    ModA ma;
    if (wave < 6) moda_load(p, 72 + 6 * x + wave, lane, ma);
    __builtin_amdgcn_sched_barrier(0);
    bar();
    if (wave < 6) {
      tag = tag_get(L);
      moda_finish(L, ws, 72 + 6 * x + wave, lane, ma, mk, (LAS float*)(L + F_RED + wave * 1024), tag);
    }
  }
  tag = tag_get(L);
  pretouch<1>(L, ws, [&](int) { const int e = wave * 64 + lane; return G_X2 + (e >> 3) * 16 + (e & 7); });
  const bool slow_tile = ((((unsigned)(uintptr_t)p.wg >> 7) + f) & 7) == 3;
  v4u wg[9], wu[9], wd[8];
  if (!slow_tile) gate_wait(t0, GATE_FFN);
  if (wave < 8) {
    const rsrc_t rg = mk_rsrc(p.wg, D * FF * 2), ru = mk_rsrc(p.wu, D * FF * 2);
    const unsigned v = (lane >> 3) * (FF * 2) + (lane & 7) * 16, s = wave * 72 * (FF * 2) + f * 128;
#pragma unroll
    for (int t = 0; t < 9; ++t) { wg[t] = ldb(rg, v, s + t * 8 * (FF * 2)); wu[t] = ldb(ru, v, s + t * 8 * (FF * 2)); }
  }
  __builtin_amdgcn_sched_barrier(0);
  unsigned h2w, h2r, stw, str_, gur, guw0, actw, actr, trw, trr;
  gu64* ddst;
  {
    const int e = wave * 64 + lane, hw = e / 72, rem = e - hw * 72, c = (wave & 3) * 16 + (lane >> 2);
    h2w = F_H2 + ((hw * 8 + (rem & 7)) * 12 + (rem >> 3)) * 4;
    h2r = F_H2 + ((wave & 7) * 8 + (lane >> 3)) * 48;
    stw = F_ST + wave * 8;
    str_ = F_ST + ((lane & 15) < 9 ? (lane & 15) : 9) * 8;
    guw0 = F_RED + (wave & 7) * 1040 + red_off(lane, 256);
    gur = F_RED + ((lane & 3) * 520 + c) * 4;
    actw = F_ACT + ((lane & 3) == 0 ? (c & 7) * 8 + (c >> 3) : 64) * 4;
    actr = F_ACT + (lane >> 3) * 32;
    trw = F_TR + wave * 640 + red_off(lane, 128);
    trr = F_TR + wave * 640 + lane * 4;
    ddst = (gu64*)ws + G_DN + f * 576 + wave * 64 + lane;
    if (wave == 0 && lane == 0) *(LAS f32x2*)(L + F_ST + 72) = f32x2{0.f, 0.f};
  }
  PIN(h2w); PIN(h2r); PIN(stw); PIN(str_); PIN(guw0); PIN(gur); PIN(actw); PIN(actr); PIN(trw); PIN(trr); PIN(ddst);
  {
    const int e = wave * 64 + lane;
    u64 gs[9];
    bool have_x2 = false;
    for (int spin = 0; spin < SPIN_CAP; ++spin) {
#pragma unroll
      for (int k = 0; k < 8; ++k) gs[k] = peek(ws, G_MOD + (k & 3) * NMOD + (k < 4 ? 3 : 4) * D + e);
      gs[8] = peek(ws, G_X2 + (e >> 3) * 16 + (e & 7));
      Chk c, cx;
#pragma unroll
      for (int k = 0; k < 8; ++k) c.add(gs[k], tag);
      cx.add(gs[8], tag);
      have_x2 = cx.all_ok();
      if (c.all_ok()) break;
    }
    float sb = rbf(fadd(fadd(gval(gs[0]), gval(gs[1])), fadd(gval(gs[2]), gval(gs[3]))));
    float c1b = rbf(1.f + rbf(fadd(fadd(gval(gs[4]), gval(gs[5])), fadd(gval(gs[6]), gval(gs[7])))));
    float on = (lane & 15) < 9 ? 64.f : 0.f;
    PIN(sb); PIN(c1b); PIN(on);
    u64 gx[1] = {gs[8]};
    if (!have_x2) gather<1>(ws, tag, gx, [&](int) { return G_X2 + (e >> 3) * 16 + (e & 7); });
    const float xv = gval(gx[0]);
    const float sh = __uint_as_float(__builtin_amdgcn_readfirstlane(__float_as_uint(xv)));
    const float d = xv - sh;
    float s1 = d, s2 = d * d;
    s1 += dpp<ROR8>(s1); s2 += dpp<ROR8>(s2); s1 += dpp<ROR4>(s1); s2 += dpp<ROR4>(s2);
    s1 += dpp<ROR2>(s1); s2 += dpp<ROR2>(s2); s1 += dpp<ROR1>(s1); s2 += dpp<ROR1>(s2);
    s1 = x16_sum(s1); s2 = x16_sum(s2);
    const float dm = s1 * (1.f / 64.f);
    if (lane == 0) *(LAS f32x2*)(L + stw) = f32x2{sh + dm, fmaf(-dm, s1, s2)};
    bar();
    const f32x2 st = *(const LAS f32x2*)(L + str_);
    __builtin_amdgcn_sched_barrier(0);
    {
      const rsrc_t rd = mk_rsrc(p.wd, FF * D * 2);
      const unsigned v = (lane >> 3) * (D * 2) + (lane & 7) * 16, s = f * 64 * (D * 2) + wave * 128;
#pragma unroll
      for (int t = 0; t < DN_SECOND; ++t) wd[t] = ldb(rd, v, s + t * 8 * (D * 2));
      __builtin_amdgcn_sched_barrier(0);
    }
    const float mf = row_sum(st[0]) * (1.f / 9.f);
    const float dv = st[0] - mf;
    const float m2 = row_sum(fmaf(on * dv, dv, st[1]));
    *(LAS unsigned*)(L + h2w) = ln_apply_pk(xv, rbf(mf), ln_rden(m2 * (1.f / D)), c1b, sb);
  }
  bar();
  if (f == 0 && threadIdx.x == 0) __hip_atomic_store((gu64*)ws + G_EPOCH, ((u64)tag.hi << 32) | (u64)tag.rest, RLX_AGENT);
  v4u h0, h1;
  unsigned h8;
  {
    const LAS v4u* hp = (const LAS v4u*)(L + h2r);
    h0 = hp[0]; h1 = hp[1]; h8 = ((const LAS unsigned*)hp)[8];
  }
  __builtin_amdgcn_sched_barrier(0);
  {
    const rsrc_t rd = mk_rsrc(p.wd, FF * D * 2);
    const unsigned v = (lane >> 3) * (D * 2) + (lane & 7) * 16, s = f * 64 * (D * 2) + wave * 128;
#pragma unroll
    for (int t = DN_SECOND; t < 8; ++t) wd[t] = ldb(rd, v, s + t * 8 * (D * 2));
    __builtin_amdgcn_sched_barrier(0);
  }
  if (wave < 8) {
    PIN(h0); PIN(h1); PIN(h8);
    f32x4 dg = {0.f, 0.f, 0.f, 0.f}, du = {0.f, 0.f, 0.f, 0.f};
#pragma unroll
    for (int t = 0; t < 9; ++t) {
      const v4u a = afrag(t < 4 ? h0[t & 3] : t < 8 ? h1[t & 3] : h8, mk);
      dg = mfma(a, wg[t], dg);
      du = mfma(a, wu[t], du);
    }
    *(LAS f32x4*)(L + guw0) = dg;
    *(LAS f32x4*)(L + guw0 + ((lane >> 5) == ((lane >> 3) & 1) ? 512 : 0)) = du;
  }
  bar();
  if (wave < 4) {
    const LAS float* r0 = (const LAS float*)(L + gur);
    float g = faddn(fadd(r0[0], r0[64]), fadd(r0[260], r0[324])), u = faddn(fadd(r0[128], r0[192]), fadd(r0[388], r0[452]));
    g += dpp<XOR1>(g); u += dpp<XOR1>(u);
    g += dpp<XOR2>(g); u += dpp<XOR2>(u);
    g = rbf(g); u = rbf(u);
    const float y = g * fmaf(g * g, 0.044715f * 0.7978845608028654f * 2.f * LOG2E, 0.7978845608028654f * 2.f * LOG2E);
    const float th = fmaf(-2.f, rcp_f(__builtin_amdgcn_exp2f(y) + 1.f), 1.f);
    const float act = rbf(0.5f * g * (1.f + th)) * u;
    *(LAS unsigned*)(L + actw) = pkr(act);
  }
  bar();
  {
    const LAS v4u* ap = (const LAS v4u*)(L + actr);
    const v4u a0 = ap[0], a1 = ap[1];
    f32x4 d0 = {0.f, 0.f, 0.f, 0.f}, d1 = {0.f, 0.f, 0.f, 0.f};
#pragma unroll
    for (int t = 0; t < 4; ++t) { d0 = mfma(afrag(a0[t], mk), wd[t], d0); d1 = mfma(afrag(a1[t], mk), wd[4 + t], d1); }
    *(LAS f32x4*)(L + trw) = d0 + d1;
    lds_wait();
    pubp(ddst, fadd(*(const LAS float*)(L + trr), *(const LAS float*)(L + trr + 256)), tag);
  }
}

DEV void role_modb(const Params& p, LAS unsigned char* L, int x, int wave, int lane, u64 t0, u64 code0) {
  if (wave == 8) {
    const unsigned dump = (unsigned)(uintptr_t)(L + L_PF);
#pragma unroll
    for (int k = 0; k < (CODE_PF + 8191) / 8192; ++k) {
      const unsigned o = k * 8192 + lane * 128;
      asm volatile("s_mov_b32 m0, %0\n\ts_nop 0\n\tglobal_load_lds_dword %1, off" :: "s"(dump), "v"(code0 + (o < CODE_PF - 128 ? o : CODE_PF - 128)) : "m0", "memory");
    }
  }
  const Mask mk = make_mask(lane, true);
  if (wave == 0) tag_put(L, load_state(p.ws), lane);
  bar();
  const Tag tag = tag_get(L);
  gate_wait(t0, GATE_MODB);
  for (int k = wave; k < 12; k += 9) {
    ModA ma;
    moda_load(p, 120 + x + 8 * k, lane, ma);
    __builtin_amdgcn_sched_barrier(0);
    moda_finish(L, p.ws, 120 + x + 8 * k, lane, ma, mk, (LAS float*)(L + wave * 1024), tag);
  }
}

__global__ void __launch_bounds__(NTHR) adaln_block_kernel(Params p) {
  __shared__ __attribute__((aligned(16))) unsigned char smem[L_END];
  LAS unsigned char* L = (LAS unsigned char*)smem;
  const int lane = threadIdx.x & 63, wave = __builtin_amdgcn_readfirstlane(threadIdx.x >> 6);
  u64 code0;
  asm volatile("s_getpc_b64 %0" : "=s"(code0));
  code0 &= ~0xFFull;
  const u64 t0 = __builtin_amdgcn_s_memrealtime();
  const int x = blockIdx.x & 7, idx = blockIdx.x >> 3;
  if (idx < 22) role_att(p, L, x, idx, wave, lane, t0);
  else if (idx < 31) role_ffn(p, L, x, idx - 22, wave, lane, t0);
  else role_modb(p, L, x, wave, lane, t0, code0);
}

__global__ void __launch_bounds__(576) k_null(u64* ws, long flag) { asm volatile("v_mov_b32 v159, 0" ::: "v159"); if (flag) ws[0] = 1; }
extern "C" void kernel_launch(void* const* d_in, const int* in_sizes, int n_in, void* d_out, int out_size, void* d_ws,
                              size_t ws_size, hipStream_t stream) {
  Params p;
  p.x = (const u16*)d_in[0];
  p.kc = (const u16*)d_in[1];
  p.vc = (const u16*)d_in[2];
  p.wq = (const u16*)d_in[3];
  p.wo = (const u16*)d_in[4];
  p.wg = (const u16*)d_in[6];
  p.wu = (const u16*)d_in[7];
  p.wd = (const u16*)d_in[8];
  p.c = (const u16*)d_in[9];
  p.wmod = (const u16*)d_in[10];
  p.out = (u16*)d_out;
  p.ws = (u64*)d_ws;
  hipLaunchKernelGGL(adaln_block_kernel, dim3(256), dim3(NTHR), 0, stream, p);
  hipLaunchKernelGGL(k_null, dim3(256), dim3(576), 0, stream, p.ws, 0L);
}
```

```cpp
#include <hip/hip_runtime.h>
#include <stdint.h>

typedef unsigned short u16;
typedef unsigned long long u64;
typedef unsigned v4u __attribute__((ext_vector_type(4)));
typedef float f32x4 __attribute__((ext_vector_type(4)));
typedef float f32x2 __attribute__((ext_vector_type(2)));
typedef __bf16 bf16x2 __attribute__((ext_vector_type(2)));
typedef short bf16x8 __attribute__((ext_vector_type(8)));
typedef __amdgpu_buffer_rsrc_t rsrc_t;
#define GAS __attribute__((address_space(1)))
#define LAS __attribute__((address_space(3)))
#define DEV __device__ __forceinline__
typedef GAS u64 gu64;
#define RLX_AGENT __ATOMIC_RELAXED, __HIP_MEMORY_SCOPE_AGENT

constexpr int D = 576, SK = 65536, WIN = 4096, DH = 128, FF = 4608, NMOD = 6 * D;
constexpr int NSPLIT = 11;
constexpr int NTHR = 576;
constexpr int SPIN_CAP = 1 << 16;
constexpr unsigned GATE_ATT = 140;
constexpr unsigned GATE_MODB = 200;
constexpr unsigned GATE_MODB2 = 120;
constexpr unsigned GATE_H1 = 260;
constexpr unsigned GATE_Q = 450;
constexpr unsigned GATE_FFN = 790;
constexpr int Q_STAGGER = 16;
constexpr int ATT_N1 = 5, ATT_D1 = 2;
constexpr int DN_SECOND = 4;
constexpr int CODE_PF = 21504;

constexpr int G_EPOCH = 0;
constexpr int G_MOD = 64;
constexpr int G_Q = G_MOD + 4 * NMOD;
constexpr int G_O = G_Q + 32 * 2 * 128;
constexpr int G_WO = G_O + 16 * 8 * NSPLIT * 64;
constexpr int G_X2 = G_WO + 128 * 576;
constexpr int G_DN = G_X2 + 72 * 16;
constexpr int G_END = G_DN + 72 * 576;

constexpr int L_K = 0;
constexpr int L_OL = 98304;
constexpr int L_Q = L_OL + 9216;
constexpr int L_MX = L_Q + 512;
constexpr int L_LS = L_MX + 64;
constexpr int L_OB = L_LS + 64;
constexpr int L_PF = L_OB + 128;
constexpr int L_WO = L_PF + 256;
constexpr int L_MA = L_WO + 36864;
constexpr int L_CNT = L_MA + 1536;
constexpr int L_TAG = L_MA + 1536 + 16;
constexpr int L_END = L_TAG + 16;
constexpr int F_RED = 0;
constexpr int F_H = 16384;
constexpr int F_H2 = 17664;
constexpr int F_ACT = 20736;
constexpr int F_TR = 21248;
constexpr int F_ST = 27136;

struct Params {
  const u16 *x, *kc, *vc, *wq, *wo, *wg, *wu, *wd, *c, *wmod;
  u16* out;
  u64* ws;
};

DEV float bf2f(unsigned b) { return __uint_as_float(b << 16); }
DEV unsigned cvt2(float lo, float hi) {
  const f32x2 v = {lo, hi};
  return __builtin_bit_cast(unsigned, __builtin_convertvector(v, bf16x2));
}
DEV unsigned pkr(float f) { return cvt2(f, f); }
DEV float rbf(float f) { return __uint_as_float(cvt2(f, f) & 0xFFFF0000u); }
constexpr int TAG_LO_BITS = 4;
constexpr unsigned TAGM = (1u << TAG_LO_BITS) - 1u;
struct Tag { unsigned hi, lo, rest; };
DEV Tag make_tag(u64 st) {
  const u64 nx = st * 6364136223846793005ull + 1442695040888963407ull;
  Tag t;
  t.hi = __builtin_amdgcn_readfirstlane((unsigned)(nx >> 32));
  t.rest = __builtin_amdgcn_readfirstlane((unsigned)nx);
  t.lo = t.rest >> (32 - TAG_LO_BITS);
  return t;
}
DEV u64 enc(float v, unsigned hi, unsigned lo) {
  const unsigned u = (__float_as_uint(v) + (TAGM + 1) / 2) & ~TAGM;
  return ((u64)hi << 32) | (u64)(u | lo);
}
DEV Tag tag_put(LAS unsigned char* L, u64 st, int lane) {
  const Tag t = make_tag(st);
  if (lane == 0) *(LAS v4u*)(L + L_TAG) = v4u{t.hi, t.lo, t.rest, 0u};
  return t;
}
DEV Tag tag_get(const LAS unsigned char* L) {
  const v4u v = *(const LAS v4u*)(L + L_TAG);
  Tag t; t.hi = __builtin_amdgcn_readfirstlane(v[0]); t.lo = __builtin_amdgcn_readfirstlane(v[1]); t.rest = __builtin_amdgcn_readfirstlane(v[2]);
  return t;
}
DEV unsigned or_xor(unsigned a, unsigned b, unsigned c) { return __builtin_amdgcn_bitop3_b32(a, b, c, 0xF6); }
struct Chk {
  unsigned a = 0, b = 0;
  DEV void add(u64 g, const Tag& t) { a = or_xor(a, (unsigned)(g >> 32), t.hi); b = or_xor(b, (unsigned)g, t.lo); }
  DEV bool all_ok() const { return __builtin_amdgcn_ballot_w64((a | (b & TAGM)) != 0u) == 0ull; }
};
DEV float gval(u64 g) { return __uint_as_float((unsigned)g & ~TAGM); }
DEV void pub(u64* ws, int idx, float v, const Tag& t) { __hip_atomic_store((gu64*)ws + idx, enc(v, t.hi, t.lo), RLX_AGENT); }
DEV void pubp(gu64* dst, float v, const Tag& t) { __hip_atomic_store(dst, enc(v, t.hi, t.lo), RLX_AGENT); }
DEV u64 peek(u64* ws, int idx) { return __hip_atomic_load((gu64*)ws + idx, RLX_AGENT); }
template <int N, class F> DEV void gather(u64* ws, const Tag& t, u64 (&g)[N], F idx) {
  for (int spin = 0; spin < SPIN_CAP; ++spin) {
#pragma unroll
    for (int k = 0; k < N; ++k) g[k] = peek(ws, idx(k));
    Chk c;
#pragma unroll
    for (int k = 0; k < N; ++k) c.add(g[k], t);
    if (c.all_ok()) break;
  }
}

struct G2 { v4u v; DEV float lo() const { return __uint_as_float(v[0] & ~TAGM); } DEV float hi() const { return __uint_as_float(v[2] & ~TAGM); } };
template <int N, class F> DEV void gather2(rsrc_t rw, const Tag& t, G2 (&g)[N], F idx) {
  for (int spin = 0; spin < SPIN_CAP; ++spin) {
    asm volatile("" ::: "memory");
#pragma unroll
    for (int k = 0; k < N; ++k) g[k].v = __builtin_amdgcn_raw_buffer_load_b128(rw, idx(k) * 8, 0, 16);
    unsigned a = 0, b = 0;
#pragma unroll
    for (int k = 0; k < N; ++k) { a = or_xor(or_xor(a, g[k].v[1], t.hi), g[k].v[3], t.hi); b = or_xor(or_xor(b, g[k].v[0], t.lo), g[k].v[2], t.lo); }
    if (__builtin_amdgcn_ballot_w64((a | (b & TAGM)) != 0u) == 0ull) break;
  }
}
DEV void q_handoff(rsrc_t rw, unsigned voff, unsigned qdst, const Tag& t) {
  static_assert(TAG_LO_BITS == 4, "masks below");
  unsigned cnt;
  asm volatile(
      "s_mov_b32 %[cnt], %[cap]\n\t"
      "buffer_load_dwordx4 v[100:103], %[vo], %[rs], 0 offen sc1\n\t"
      "buffer_load_dwordx4 v[104:107], %[vo], %[rs], 0 offen offset:1024 sc1\n\t"
      "buffer_load_dwordx4 v[108:111], %[vo], %[rs], 0 offen offset:2048 sc1\n\t"
      "buffer_load_dwordx4 v[112:115], %[vo], %[rs], 0 offen offset:3072 sc1\n\t"
      "s_sleep %[stag]\n\t"
      "1:\n\t"
      "buffer_load_dwordx4 v[116:119], %[vo], %[rs], 0 offen sc1\n\t"
      "buffer_load_dwordx4 v[120:123], %[vo], %[rs], 0 offen offset:1024 sc1\n\t"
      "buffer_load_dwordx4 v[124:127], %[vo], %[rs], 0 offen offset:2048 sc1\n\t"
      "buffer_load_dwordx4 v[128:131], %[vo], %[rs], 0 offen offset:3072 sc1\n\t"
      "s_waitcnt vmcnt(4)\n\t"
      "v_xor_b32 v132, %[th], v101\n\t"
      "v_bitop3_b32 v132, v132, v103, %[th] bitop3:0xf6\n\t"
      "v_bitop3_b32 v132, v132, v105, %[th] bitop3:0xf6\n\t"
      "v_bitop3_b32 v132, v132, v107, %[th] bitop3:0xf6\n\t"
      "v_bitop3_b32 v132, v132, v109, %[th] bitop3:0xf6\n\t"
      "v_bitop3_b32 v132, v132, v111, %[th] bitop3:0xf6\n\t"
      "v_bitop3_b32 v132, v132, v113, %[th] bitop3:0xf6\n\t"
      "v_bitop3_b32 v132, v132, v115, %[th] bitop3:0xf6\n\t"
      "v_xor_b32 v133, %[tl], v100\n\t"
      "v_bitop3_b32 v133, v133, v102, %[tl] bitop3:0xf6\n\t"
      "v_bitop3_b32 v133, v133, v104, %[tl] bitop3:0xf6\n\t"
      "v_bitop3_b32 v133, v133, v106, %[tl] bitop3:0xf6\n\t"
      "v_bitop3_b32 v133, v133, v108, %[tl] bitop3:0xf6\n\t"
      "v_bitop3_b32 v133, v133, v110, %[tl] bitop3:0xf6\n\t"
      "v_bitop3_b32 v133, v133, v112, %[tl] bitop3:0xf6\n\t"
      "v_bitop3_b32 v133, v133, v114, %[tl] bitop3:0xf6\n\t"
      "v_and_b32 v133, 15, v133\n\t"
      "v_or_b32 v132, v132, v133\n\t"
      "v_cmp_ne_u32 vcc, 0, v132\n\t"
      "s_cbranch_vccz 2f\n\t"
      "buffer_load_dwordx4 v[100:103], %[vo], %[rs], 0 offen sc1\n\t"
      "buffer_load_dwordx4 v[104:107], %[vo], %[rs], 0 offen offset:1024 sc1\n\t"
      "buffer_load_dwordx4 v[108:111], %[vo], %[rs], 0 offen offset:2048 sc1\n\t"
      "buffer_load_dwordx4 v[112:115], %[vo], %[rs], 0 offen offset:3072 sc1\n\t"
      "s_waitcnt vmcnt(4)\n\t"
      "v_xor_b32 v132, %[th], v117\n\t"
      "v_bitop3_b32 v132, v132, v119, %[th] bitop3:0xf6\n\t"
      "v_bitop3_b32 v132, v132, v121, %[th] bitop3:0xf6\n\t"
      "v_bitop3_b32 v132, v132, v123, %[th] bitop3:0xf6\n\t"
      "v_bitop3_b32 v132, v132, v125, %[th] bitop3:0xf6\n\t"
      "v_bitop3_b32 v132, v132, v127, %[th] bitop3:0xf6\n\t"
      "v_bitop3_b32 v132, v132, v129, %[th] bitop3:0xf6\n\t"
      "v_bitop3_b32 v132, v132, v131, %[th] bitop3:0xf6\n\t"
      "v_xor_b32 v133, %[tl], v116\n\t"
      "v_bitop3_b32 v133, v133, v118, %[tl] bitop3:0xf6\n\t"
      "v_bitop3_b32 v133, v133, v120, %[tl] bitop3:0xf6\n\t"
      "v_bitop3_b32 v133, v133, v122, %[tl] bitop3:0xf6\n\t"
      "v_bitop3_b32 v133, v133, v124, %[tl] bitop3:0xf6\n\t"
      "v_bitop3_b32 v133, v133, v126, %[tl] bitop3:0xf6\n\t"
      "v_bitop3_b32 v133, v133, v128, %[tl] bitop3:0xf6\n\t"
      "v_bitop3_b32 v133, v133, v130, %[tl] bitop3:0xf6\n\t"
      "v_and_b32 v133, 15, v133\n\t"
      "v_or_b32 v132, v132, v133\n\t"
      "v_cmp_ne_u32 vcc, 0, v132\n\t"
      "s_cbranch_vccz 3f\n\t"
      "s_sub_u32 %[cnt], %[cnt], 1\n\t"
      "s_cbranch_scc0 1b\n\t"
      "2:\n\t"
      "v_and_b32 v132, 0xfffffff0, v100\n\t"
      "v_and_b32 v133, 0xfffffff0, v104\n\t"
      "v_add_f32 v132, v132, v133\n\t"
      "v_and_b32 v133, 0xfffffff0, v102\n\t"
      "v_and_b32 v136, 0xfffffff0, v106\n\t"
      "v_add_f32 v133, v133, v136\n\t"
      "v_cvt_pk_bf16_f32 v132, v132, v133\n\t"
      "ds_write_b32 %[qd], v132\n\t"
      "v_and_b32 v134, 0xfffffff0, v108\n\t"
      "v_and_b32 v135, 0xfffffff0, v112\n\t"
      "v_add_f32 v134, v134, v135\n\t"
      "v_and_b32 v135, 0xfffffff0, v110\n\t"
      "v_and_b32 v136, 0xfffffff0, v114\n\t"
      "v_add_f32 v135, v135, v136\n\t"
      "v_cvt_pk_bf16_f32 v134, v134, v135\n\t"
      "ds_write_b32 %[qd], v134 offset:256\n\t"
      "s_branch 4f\n\t"
      "3:\n\t"
      "v_and_b32 v132, 0xfffffff0, v116\n\t"
      "v_and_b32 v133, 0xfffffff0, v120\n\t"
      "v_add_f32 v132, v132, v133\n\t"
      "v_and_b32 v133, 0xfffffff0, v118\n\t"
      "v_and_b32 v136, 0xfffffff0, v122\n\t"
      "v_add_f32 v133, v133, v136\n\t"
      "v_cvt_pk_bf16_f32 v132, v132, v133\n\t"
      "ds_write_b32 %[qd], v132\n\t"
      "v_and_b32 v134, 0xfffffff0, v124\n\t"
      "v_and_b32 v135, 0xfffffff0, v128\n\t"
      "v_add_f32 v134, v134, v135\n\t"
      "v_and_b32 v135, 0xfffffff0, v126\n\t"
      "v_and_b32 v136, 0xfffffff0, v130\n\t"
      "v_add_f32 v135, v135, v136\n\t"
      "v_cvt_pk_bf16_f32 v134, v134, v135\n\t"
      "ds_write_b32 %[qd], v134 offset:256\n\t"
      "4:\n\t"
      "s_waitcnt lgkmcnt(0)\n\t"
      "s_barrier\n\t"
      "s_waitcnt vmcnt(0)"
      : [cnt] "=&s"(cnt)
      : [vo] "v"(voff), [rs] "s"(rw), [th] "s"(t.hi), [tl] "s"(t.lo), [qd] "v"(qdst), [cap] "n"(SPIN_CAP / 2), [stag] "n"(Q_STAGGER)
      : "vcc", "scc", "memory", "v100", "v101", "v102", "v103", "v104", "v105", "v106", "v107", "v108", "v109", "v110", "v111", "v112", "v113", "v114", "v115", "v116", "v117", "v118", "v119", "v120", "v121", "v122", "v123", "v124", "v125", "v126", "v127", "v128", "v129", "v130", "v131", "v132", "v133", "v134", "v135", "v136");
}
template <int N, class F> DEV void pretouch(LAS unsigned char* L, u64* ws, F idx) {
  const unsigned dump = (unsigned)(uintptr_t)(L + L_PF);
#pragma unroll
  for (int k = 0; k < N; ++k)
    asm volatile("s_mov_b32 m0, %0\n\ts_nop 0\n\tglobal_load_lds_dword %1, off sc1" :: "s"(dump), "v"((gu64*)ws + idx(k)) : "m0", "memory");
}
DEV f32x4 mfma(v4u a, v4u b, f32x4 c) {
  return __builtin_amdgcn_mfma_f32_16x16x32_bf16(__builtin_bit_cast(bf16x8, a), __builtin_bit_cast(bf16x8, b), c, 0, 0, 0);
}
DEV rsrc_t mk_rsrc(const void* p, unsigned bytes) { return __builtin_amdgcn_make_buffer_rsrc((void*)p, 0, (int)bytes, 0x00020000); }
DEV v4u ldb(rsrc_t r, unsigned voff, unsigned soff) { return __builtin_amdgcn_raw_buffer_load_b128(r, (int)voff, (int)soff, 2); }
DEV unsigned ldb16(rsrc_t r, unsigned voff) { return __builtin_amdgcn_raw_buffer_load_b16(r, (int)voff, 0, 0); }
DEV u64 load_state(u64* ws) {
  int zoff = G_EPOCH * 8;
  asm volatile("" : "+v"(zoff));
  typedef unsigned v2u_ __attribute__((ext_vector_type(2)));
  const v2u_ v = __builtin_amdgcn_raw_buffer_load_b64(mk_rsrc(ws, 64), zoff, 0, 16);
  return ((u64)v[1] << 32) | v[0];
}
DEV void bar() {
  asm volatile("s_waitcnt lgkmcnt(0)" ::: "memory");
  __builtin_amdgcn_s_barrier();
  asm volatile("" ::: "memory");
}
#define PIN(x) asm volatile("" : "+v"(x))
DEV void lds_wait() { asm volatile("s_waitcnt lgkmcnt(0)" ::: "memory"); }
template <int CTRL> DEV float dpp(float x) {
  return __builtin_bit_cast(float, __builtin_amdgcn_mov_dpp(__builtin_bit_cast(int, x), CTRL, 0xf, 0xf, true));
}
constexpr int ROR8 = 0x128, ROR4 = 0x124, ROR2 = 0x122, ROR1 = 0x121, XOR1 = 0xB1, XOR2 = 0x4E;
DEV float fmx(float a, float b) { float r; asm("v_max_f32 %0, %1, %2\n\ts_nop 1" : "=v"(r) : "v"(a), "v"(b)); return r; }
DEV float fmx3(float a, float b, float c) { float r; asm("v_max3_f32 %0, %1, %2, %3" : "=v"(r) : "v"(a), "v"(b), "v"(c)); return r; }
DEV float fmx3n(float a, float b, float c) { float r; asm("v_max3_f32 %0, %1, %2, %3\n\ts_nop 1" : "=v"(r) : "v"(a), "v"(b), "v"(c)); return r; }
DEV float x16_sum(float x) {
  auto s = __builtin_amdgcn_permlane16_swap(__float_as_uint(x), __float_as_uint(x), false, false);
  x = __uint_as_float(s[0]) + __uint_as_float(s[1]);
  auto t = __builtin_amdgcn_permlane32_swap(__float_as_uint(x), __float_as_uint(x), false, false);
  return __uint_as_float(t[0]) + __uint_as_float(t[1]);
}
DEV float x16_max(float x) {
  auto s = __builtin_amdgcn_permlane16_swap(__float_as_uint(x), __float_as_uint(x), false, false);
  x = fmaxf(__uint_as_float(s[0]), __uint_as_float(s[1]));
  auto t = __builtin_amdgcn_permlane32_swap(__float_as_uint(x), __float_as_uint(x), false, false);
  return fmaxf(__uint_as_float(t[0]), __uint_as_float(t[1]));
}
DEV float row_sum(float x) { x += dpp<ROR8>(x); x += dpp<ROR4>(x); x += dpp<ROR2>(x); x += dpp<ROR1>(x); return x; }
DEV float wave_sum(float x) { return x16_sum(row_sum(x)); }
constexpr float LOG2E = 1.4426950408889634f;
DEV float fadd(float a, float b) { float r; asm("v_add_f32 %0, %1, %2" : "=v"(r) : "v"(a), "v"(b)); return r; }
DEV float faddn(float a, float b) { float r; asm("v_add_f32 %0, %1, %2\n\ts_nop 1" : "=v"(r) : "v"(a), "v"(b)); return r; }
DEV float exp_f(float x) { return __builtin_amdgcn_exp2f(x * LOG2E); }
DEV float rcp_f(float x) { return __builtin_amdgcn_rcpf(x); }
DEV float pair_pick(float a, float b, int lane) {
  const float a2 = dpp<0x50>(a), b2 = dpp<0x50>(b);
  const float a3 = dpp<0xFA>(a), b3 = dpp<0xFA>(b);
  const float lo = (lane & 1) ? b2 : a2, hi = (lane & 1) ? b3 : a3;
  return (lane & 4) ? hi : lo;
}
DEV void gate_wait(u64 t0, unsigned ticks) { while (__builtin_amdgcn_s_memrealtime() - t0 < (u64)ticks) __builtin_amdgcn_s_sleep(2); }

struct Mask { unsigned m[4]; };
DEV Mask make_mask(int lane, bool on) {
  Mask k;
  const unsigned h = on ? ((lane & 1) ? 0xFFFF0000u : 0x0000FFFFu) : 0u;
#pragma unroll
  for (int r = 0; r < 4; ++r) k.m[r] = (((lane & 7) >> 1) == r) ? h : 0u;
  return k;
}
DEV v4u afrag(unsigned pp, const Mask& k) { return v4u{pp & k.m[0], pp & k.m[1], pp & k.m[2], pp & k.m[3]}; }

DEV unsigned ln_apply_pk(float x, float mean_b, float rden, float c1, float shift) {
  const float ln = rbf(rbf(x - mean_b) * rden);
  return pkr(rbf(ln * c1) + shift);
}
DEV float ln_rden(float var) { return rcp_f(rbf(__builtin_amdgcn_sqrtf(rbf(rbf(var) + 1e-5f)))); }

DEV unsigned red_off(int lane, int dump) {
  const int u = (lane >> 3) & 1;
  return 4 * ((lane >> 5) == u ? u * 64 + 8 * (lane & 7) + 4 * ((lane >> 4) & 1) : dump);
}
DEV unsigned silu_pk(unsigned craw) { const float v = bf2f(craw); return pkr(v * rcp_f(1.f + exp_f(-v))); }

struct WoAddr { unsigned tro, tri, obo, wro; gu64* dst; };
DEV WoAddr wo_prep(u64* ws, int pidx, int wave, int lane) {
  WoAddr a;
  a.tro = L_OL + wave * 640 + red_off(lane, 128); a.tri = L_OL + wave * 640 + lane * 4; a.obo = L_OB + (lane >> 3) * 16; a.wro = L_WO + wave * 4096 + lane * 16;
  a.dst = (gu64*)ws + G_WO + pidx * 576 + wave * 64 + lane;
  PIN(a.tro); PIN(a.tri); PIN(a.obo); PIN(a.wro); PIN(a.dst);
  return a;
}
DEV void wo_phase(LAS unsigned char* L, const WoAddr& a, const Tag& tag, const Mask& mk) {
  bar();
  v4u ob = *(const LAS v4u*)(L + a.obo);
  const LAS v4u* wr = (const LAS v4u*)(L + a.wro);
  v4u w0 = wr[0], w1 = wr[64], w2 = wr[128], w3 = wr[192];
  PIN(ob); PIN(w0); PIN(w1); PIN(w2); PIN(w3);
  __builtin_amdgcn_sched_barrier(0);
  f32x4 d0 = {0.f, 0.f, 0.f, 0.f}, d1 = {0.f, 0.f, 0.f, 0.f};
  d0 = mfma(afrag(ob[0], mk), w0, d0); d1 = mfma(afrag(ob[1], mk), w1, d1);
  d0 = mfma(afrag(ob[2], mk), w2, d0); d1 = mfma(afrag(ob[3], mk), w3, d1);
  *(LAS f32x4*)(L + a.tro) = d0 + d1;
  lds_wait();
  pubp(a.dst, fadd(*(const LAS float*)(L + a.tri), *(const LAS float*)(L + a.tri + 256)), tag);
}

struct ModA { v4u w[18]; unsigned c[3]; };
DEV void moda_load(const Params& p, int mt, int lane, ModA& m) {
  const rsrc_t rm = mk_rsrc(p.wmod, D * NMOD * 2), rc = mk_rsrc(p.c, D * 2);
  const int r0 = (mt & 3) * 144;
#pragma unroll
  for (int k = 0; k < 3; ++k) m.c[k] = ldb16(rc, (r0 + (k < 2 || lane < 16 ? lane + 64 * k : 143)) * 2);
  const unsigned v = (r0 + (lane >> 3)) * (NMOD * 2) + (lane & 7) * 16;
#pragma unroll
  for (int t = 0; t < 18; ++t) m.w[t] = ldb(rm, v, (mt >> 2) * 128 + t * 8 * (NMOD * 2));
}
DEV void moda_finish(LAS unsigned char* L, u64* ws, int mt, int lane, ModA& m, const Mask& mk, LAS float* tr, const Tag& tag) {
  unsigned sp[18];
  {
    const unsigned s0 = silu_pk(m.c[0]), s1 = silu_pk(m.c[1]), s2 = silu_pk(m.c[2]);
#pragma unroll
    for (int t = 0; t < 18; ++t) sp[t] = __builtin_amdgcn_ds_bpermute(((8 * t + (lane >> 3)) & 63) * 4, t < 8 ? s0 : t < 16 ? s1 : s2);
  }
  gu64* dst = (gu64*)ws + G_MOD + (mt & 3) * NMOD + (mt >> 2) * 64 + lane;
  unsigned tro = red_off(lane, 128);
  PIN(dst); PIN(tro);
#pragma unroll
  for (int t = 0; t < 18; ++t) PIN(sp[t]);
  __builtin_amdgcn_sched_barrier(0);
  f32x4 d0 = {0.f, 0.f, 0.f, 0.f}, d1 = {0.f, 0.f, 0.f, 0.f};
#pragma unroll
  for (int t = 0; t < 18; t += 2) {
    d0 = mfma(afrag(sp[t], mk), m.w[t], d0);
    __builtin_amdgcn_sched_barrier(0);
    d1 = mfma(afrag(sp[t + 1], mk), m.w[t + 1], d1);
    __builtin_amdgcn_sched_barrier(0);
  }
  *(LAS f32x4*)((LAS unsigned char*)tr + tro) = d0 + d1;
  lds_wait();
  pubp(dst, fadd(tr[lane], tr[64 + lane]), tag);
}

DEV void role_att(const Params& p, LAS unsigned char* L, int x, int i, int wave, int lane, u64 t0) {
  const int g = 2 * x + (i >= NSPLIT ? 1 : 0), j = i >= NSPLIT ? i - NSPLIT : i;
  const bool odd = g & 1;
  const int nm = odd ? 4 : 5;
  const bool moda = j < nm;
  const int ntile = !moda ? 24 : (!odd && j >= 3) ? 23 : 22;
  const int tile0 = !moda ? (odd ? 88 : 112) + 24 * (j - nm) : (!odd && j == 4) ? 89 : 22 * j;
  const bool owner = j < 8;
  int redc = -1;
  if (j >= 8) redc = 9 * x + (g & 1) * 3 + (j - 8);
  else if (j == 0) redc = 9 * x + 6 + (g & 1);
  else if (j == 1 && (g & 1) == 0) redc = 9 * x + 8;
  u64* ws = p.ws;
  const Mask mk = make_mask(lane, true);
  const int kap = lane >> 4, cc = lane & 15;

  if (wave < 8) {
    const size_t hb = ((size_t)g * SK + (SK - WIN)) * DH;
    const rsrc_t rk = mk_rsrc(p.kc + hb, WIN * DH * 2), rv = mk_rsrc(p.vc + hb, WIN * DH * 2);
    const bool three = wave + 16 < ntile;
    const unsigned tso = (tile0 + wave) * 4096;
    const unsigned ldsk = (unsigned)(uintptr_t)(L + L_K) + wave * 3 * 4096;
    unsigned vk[4];
#pragma unroll
    for (int q = 0; q < 4; ++q) vk[q] = (4 * q + kap) * 256 + ((cc ^ (4 * q + kap)) << 4);
#define KDMA(s, q) asm volatile("s_mov_b32 m0, %0\n\ts_nop 0\n\tbuffer_load_dwordx4 %1, %2, %3 offen nt lds" \
                     :: "s"(ldsk + (s) * 4096 + (q) * 1024), "v"(vk[q]), "s"(rk), "s"(tso + (s) * 32768) : "m0", "memory")
#define THR(n) do { if ((n) < ATT_N1 && (n) >= ATT_D1 && moda) asm volatile("s_waitcnt vmcnt(%0)" :: "n"(ATT_D1 - 1) : "memory"); } while (0)
    if (moda) __builtin_amdgcn_s_barrier();
    __builtin_amdgcn_s_setprio(2);
    KDMA(0, 0);
    gate_wait(t0, GATE_ATT);
    THR(1); KDMA(0, 1); THR(2); KDMA(0, 2); THR(3); KDMA(0, 3);
    THR(4); KDMA(1, 0); KDMA(1, 1); KDMA(1, 2); KDMA(1, 3);
    if (three) { KDMA(2, 0); KDMA(2, 1); KDMA(2, 2); KDMA(2, 3); }
#undef KDMA
#undef THR
    __builtin_amdgcn_s_setprio(0);
    v4u vr[12];
    const unsigned vv = kap * 1024 + cc * 16;
#pragma unroll
    for (int t = 0; t < 8; ++t) vr[t] = ldb(rv, vv + (t & 3) * 256, tso + (t >> 2) * 32768);
    if (three) {
#pragma unroll
      for (int t = 8; t < 12; ++t) vr[t] = ldb(rv, vv + (t & 3) * 256, tso + 65536);
    } else {
#pragma unroll
      for (int t = 8; t < 12; ++t) vr[t] = v4u{0u, 0u, 0u, 0u};
    }
    __builtin_amdgcn_sched_barrier(0);
    if (three) asm volatile("s_waitcnt vmcnt(12)" ::: "memory");
    else asm volatile("s_waitcnt vmcnt(8)" ::: "memory");
    v4u kf[12];
#pragma unroll
    for (int s = 0; s < 3; ++s)
#pragma unroll
      for (int ks = 0; ks < 4; ++ks)
        kf[s * 4 + ks] = *(const LAS v4u*)(L + L_K + (wave * 3 + (s < 2 || three ? s : 0)) * 4096 + cc * 256 + (((4 * ks + kap) ^ cc) << 4));
#pragma unroll
    for (int t = 0; t < 12; ++t) PIN(kf[t]);
    const int head = (lane >> 3) & 1;
    const int me = (wave & 3) * 64 + lane;
    unsigned qsrc = L_Q + head * 256 + kap * 16, mxw = L_MX + (head * 8 + wave) * 4, mxr = L_MX + head * 32, olw = L_OL + wave * 1024 + lane * 16;
    unsigned msrc = L_OL + ((16 * (((me >> 7) * 8 + (me & 7)) >> 2) + ((me & 127) >> 3)) * 4 + (me & 3)) * 4;
    gu64* mdst = (gu64*)ws + G_O + ((g * 8 + (me >> 5)) * NSPLIT + j) * 64 + (me & 31);
    bool lead = (lane & 0x37) == 0;
    PIN(qsrc); PIN(mxw); PIN(mxr); PIN(olw); PIN(msrc); PIN(mdst);
    bar();
    v4u qf[4];
#pragma unroll
    for (int ks = 0; ks < 4; ++ks) qf[ks] = *(const LAS v4u*)(L + qsrc + ks * 64);
#pragma unroll
    for (int ks = 0; ks < 4; ++ks) PIN(qf[ks]);
    __builtin_amdgcn_sched_barrier(0);
    f32x4 acc[3] = {{0.f, 0.f, 0.f, 0.f}, {0.f, 0.f, 0.f, 0.f}, {0.f, 0.f, 0.f, 0.f}};
#pragma unroll
    for (int ks = 0; ks < 4; ++ks)
#pragma unroll
      for (int s = 0; s < 3; ++s) acc[s] = mfma(kf[s * 4 + ks], qf[ks], acc[s]);
    __builtin_amdgcn_sched_barrier(0);
    float sc[12];
#pragma unroll
    for (int t = 0; t < 12; t += 2) {
      const unsigned u = cvt2(acc[t >> 2][t & 3], acc[t >> 2][(t & 3) + 1]);
      const unsigned w = cvt2(__uint_as_float(u << 16) * 0.08838834764831845f, __uint_as_float(u & 0xFFFF0000u) * 0.08838834764831845f);
      sc[t] = __uint_as_float(w << 16);
      sc[t + 1] = __uint_as_float(w & 0xFFFF0000u);
    }
    if (!three) {
#pragma unroll
      for (int t = 8; t < 12; ++t) sc[t] = -INFINITY;
    }
    float mx = fmx3n(fmx3(fmx3(sc[0], sc[1], sc[2]), fmx3(sc[3], sc[4], sc[5]), fmx3(sc[6], sc[7], sc[8])), fmx3(sc[9], sc[10], sc[11]), sc[0]);
    mx = x16_max(mx);
    if (lead) *(LAS float*)(L + mxw) = mx;
    lds_wait();
    if (lane == 0) __hip_atomic_fetch_add((LAS unsigned*)(L + L_CNT), 1u, __ATOMIC_RELAXED, __HIP_MEMORY_SCOPE_WORKGROUP);
    while (*(volatile LAS unsigned*)(L + L_CNT) < 8u) {}
    asm volatile("" ::: "memory");
    float m;
    {
      const f32x4 a = *(const LAS f32x4*)(L + mxr), b = *(const LAS f32x4*)(L + mxr + 16);
      m = fmx3(fmx3(a[0], a[1], a[2]), fmx3(a[3], b[0], b[1]), fmx(b[2], b[3]));
    }
    v4u af[12];
    float ls0 = 0.f, ls1 = 0.f;
    const float mneg = -m * LOG2E;
#pragma unroll
    for (int t = 0; t < 12; t += 2) {
      const float e0 = __builtin_amdgcn_exp2f(fmaf(sc[t], LOG2E, mneg)), e1 = __builtin_amdgcn_exp2f(fmaf(sc[t + 1], LOG2E, mneg));
      ls0 += e0; ls1 += e1;
      af[t] = afrag(pkr(e0), mk); af[t + 1] = afrag(pkr(e1), mk);
    }
    const float ls = x16_sum(ls0 + ls1);
    if (lead) *(LAS float*)(L + mxw + 64) = ls;
    const Tag tag = tag_get(L);
    __builtin_amdgcn_sched_barrier(0);
    f32x4 o0 = {0.f, 0.f, 0.f, 0.f}, o1 = {0.f, 0.f, 0.f, 0.f};
#pragma unroll
    for (int t = 0; t < 12; t += 2) { o0 = mfma(af[t], vr[t], o0); o1 = mfma(af[t + 1], vr[t + 1], o1); }
    *(LAS f32x4*)(L + olw) = o0 + o1;
    bar();
    if (wave < 4) {
      const LAS float* src = (const LAS float*)(L + msrc);
      float s[8];
#pragma unroll
      for (int w = 0; w < 8; ++w) s[w] = src[w * 256];
      pubp(mdst, fadd(fadd(fadd(s[0], s[1]), fadd(s[2], s[3])), fadd(fadd(s[4], s[5]), fadd(s[6], s[7]))), tag);
    } else if (wave == 4 && lane < 16) {
      const int u = lane >> 1, hh = u >> 2;
      const f32x4 a = *(const LAS f32x4*)(L + ((lane & 1) ? L_LS : L_MX) + hh * 32), b = *(const LAS f32x4*)(L + ((lane & 1) ? L_LS : L_MX) + hh * 32 + 16);
      const float vs = ((a[0] + a[1]) + (a[2] + a[3])) + ((b[0] + b[1]) + (b[2] + b[3]));
      const float vm = fmaxf(fmaxf(fmaxf(a[0], a[1]), fmaxf(a[2], a[3])), fmaxf(fmaxf(b[0], b[1]), fmaxf(b[2], b[3])));
      pub(ws, G_O + ((g * 8 + u) * NSPLIT + j) * 64 + 32 + (lane & 1), (lane & 1) ? vs : vm, tag);
    }
    if (owner) {
      wo_phase(L, wo_prep(ws, g * 8 + j, wave, lane), tag, mk);
    }
  } else {
    __builtin_amdgcn_s_setprio(3);
    Tag tag;
    {
      if (moda) {
        u64 st = load_state(ws);
        ModA ma;
        moda_load(p, 9 * x + (odd ? 5 : 0) + j, lane, ma);
        __builtin_amdgcn_sched_barrier(0);
        __builtin_amdgcn_s_barrier();
        asm volatile("" : "+v"(st));
        tag = tag_put(L, st, lane);
        moda_finish(L, ws, 9 * x + (odd ? 5 : 0) + j, lane, ma, mk, (LAS float*)(L + L_MA), tag);
      } else {
        tag = tag_put(L, load_state(ws), lane);
      }
    }
    float xr = 0.f;
    if (redc >= 0) xr = bf2f(p.x[redc * 8 + (lane & 7)]);
    gate_wait(t0, GATE_Q);
    const rsrc_t rw = mk_rsrc(ws, G_END * 8);
    if (lane == 0) *(volatile LAS unsigned*)(L + L_CNT) = 0u;
    q_handoff(rw, (G_Q + 2 * g * 256 + 2 * lane) * 8, (unsigned)(uintptr_t)(L + L_Q) + lane * 4, tag);
    if (owner) {
      const int ob = G_O + (g * 8 + j) * NSPLIT * 64;
      pretouch<1>(L, ws, [&](int) { const int q_ = lane < 33 ? lane : 32; return ob + (q_ / 3) * 64 + (q_ % 3) * 16; });
    }
    if (redc >= 0) {
      pretouch<2>(L, ws, [&](int s) { return G_WO + (s * 64 + lane) * 576 + redc * 8; });
      pretouch<2>(L, ws, [&](int s) { const int f_ = s * 64 + lane; return G_DN + (f_ < 72 ? f_ : 71) * 576 + redc * 8; });
    }
    if (owner) {
      const rsrc_t rwo = mk_rsrc(p.wo, 4096 * D * 2);
      const unsigned wo_v = (lane >> 3) * (D * 2) + (lane & 7) * 16;
      const unsigned wo_s = (g * 256 + j * 32) * (D * 2), ldsw = (unsigned)(uintptr_t)(L + L_WO);
#pragma unroll
      for (int w = 0; w < 9; ++w)
#pragma unroll
        for (int t = 0; t < 4; ++t)
          asm volatile("s_mov_b32 m0, %0\n\ts_nop 0\n\tbuffer_load_dwordx4 %1, %2, %3 offen nt lds"
                       :: "s"(ldsw + (w * 4 + t) * 1024), "v"(wo_v), "s"(rwo), "s"(wo_s + w * 128 + t * 8 * (D * 2)) : "m0", "memory");
    }
    bar();
    if (owner) {
      const int ob = G_O + (g * 8 + j) * NSPLIT * 64;
      const int k2 = kap < 3 ? 8 + kap : 10;
      unsigned obw = L_OB + (((2 * cc) & 7) * 4 + (cc >> 2)) * 4;
      float live = kap < 3 ? 1.f : 0.f;
      PIN(obw); PIN(live);
      const WoAddr wa = wo_prep(ws, g * 8 + j, 8, lane);
      G2 go[6];
      gather2<6>(rw, tag, go, [&](int k) { const int sp = k % 3 == 0 ? kap : k % 3 == 1 ? 4 + kap : k2; return ob + sp * 64 + (k < 3 ? 2 * cc : 32); });
      const float M = x16_max(fmaxf(fmaxf(go[3].lo(), go[4].lo()), go[5].lo()));
      const float Mn = -M * LOG2E;
      const float w0 = __builtin_amdgcn_exp2f(fmaf(go[3].lo(), LOG2E, Mn)), w1 = __builtin_amdgcn_exp2f(fmaf(go[4].lo(), LOG2E, Mn));
      const float w2 = __builtin_amdgcn_exp2f(fmaf(go[5].lo(), LOG2E, Mn)) * live;
      const float Ls = x16_sum(fmaf(w2, go[5].hi(), fmaf(w1, go[4].hi(), w0 * go[3].hi())));
      const float o0 = x16_sum(fmaf(w2, go[2].lo(), fmaf(w1, go[1].lo(), w0 * go[0].lo())));
      const float o1 = x16_sum(fmaf(w2, go[2].hi(), fmaf(w1, go[1].hi(), w0 * go[0].hi())));
      const float rl = rcp_f(Ls);
      *(LAS unsigned*)(L + obw) = pkr(o0 * rl);
      *(LAS unsigned*)(L + obw + 16) = pkr(o1 * rl);
      asm volatile("s_waitcnt vmcnt(0)" ::: "memory");
      wo_phase(L, wa, tag, mk);
    }
    if (redc >= 0) {
      u64 gg[8];
      gather<8>(ws, tag, gg, [&](int k) { return G_MOD + (k & 3) * NMOD + (k < 4 ? 2 : 5) * D + redc * 8 + (lane & 7); });
      float ga = rbf(fadd(fadd(gval(gg[0]), gval(gg[1])), fadd(gval(gg[2]), gval(gg[3]))));
      float gb = rbf(fadd(fadd(gval(gg[4]), gval(gg[5])), fadd(gval(gg[6]), gval(gg[7]))));
      gu64* xdst = (gu64*)ws + G_X2 + redc * 16 + (lane & 7);
      GAS u16* odst = (GAS u16*)p.out + redc * 8 + (lane & 7);
      PIN(ga); PIN(gb); PIN(xr); PIN(xdst); PIN(odst);
      float x2;
      {
        G2 gw[8];
        gather2<8>(rw, tag, gw, [&](int s) { return G_WO + (s * 16 + (lane >> 2)) * 576 + redc * 8 + 2 * (lane & 3); });
        float t0_ = faddn(fadd(fadd(gw[0].lo(), gw[1].lo()), fadd(gw[2].lo(), gw[3].lo())), fadd(fadd(gw[4].lo(), gw[5].lo()), fadd(gw[6].lo(), gw[7].lo())));
        float t1_ = faddn(fadd(fadd(gw[0].hi(), gw[1].hi()), fadd(gw[2].hi(), gw[3].hi())), fadd(fadd(gw[4].hi(), gw[5].hi()), fadd(gw[6].hi(), gw[7].hi())));
        t0_ += dpp<ROR8>(t0_); t1_ += dpp<ROR8>(t1_); t0_ += dpp<ROR4>(t0_); t1_ += dpp<ROR4>(t1_);
        t0_ = x16_sum(t0_); t1_ = x16_sum(t1_);
        const float ts = pair_pick(t0_, t1_, lane);
        x2 = rbf(xr + rbf(ga * rbf(ts)));
        if (lane < 8) pubp(xdst, x2, tag);
      }
      {
        G2 gd[5];
        float w4 = lane < 32 ? 1.f : 0.f;
        PIN(w4);
        gather2<5>(rw, tag, gd, [&](int s) { const int f_ = s * 16 + (lane >> 2); return G_DN + (f_ < 72 ? f_ : 71) * 576 + redc * 8 + 2 * (lane & 3); });
        float t0_ = fmaf(w4, gd[4].lo(), fadd(fadd(gd[0].lo(), gd[1].lo()), fadd(gd[2].lo(), gd[3].lo())));
        float t1_ = fmaf(w4, gd[4].hi(), fadd(fadd(gd[0].hi(), gd[1].hi()), fadd(gd[2].hi(), gd[3].hi())));
        t0_ += dpp<ROR8>(t0_); t1_ += dpp<ROR8>(t1_); t0_ += dpp<ROR4>(t0_); t1_ += dpp<ROR4>(t1_);
        t0_ = x16_sum(t0_); t1_ = x16_sum(t1_);
        const float ts = pair_pick(t0_, t1_, lane);
        const unsigned o = pkr(x2 + rbf(gb * rbf(ts)));
        if (lane < 8) *odst = (u16)o;
        if (redc == 0) {
          const u64 A = __hip_atomic_load((gu64*)ws + 300000, RLX_AGENT), B = __hip_atomic_load((gu64*)ws + 300016, RLX_AGENT);
          u64 gq_ = 0ull;
          gq_ = __builtin_amdgcn_readfirstlane((unsigned)(gq_ > 2000 ? 0 : gq_));
          const u64 tw = __builtin_amdgcn_s_memrealtime();
          while (__builtin_amdgcn_s_memrealtime() - tw < gq_ * 10) __builtin_amdgcn_s_sleep(1);
          if (lane == 0) __hip_atomic_store((gu64*)ws + 300016, __builtin_amdgcn_s_memrealtime(), RLX_AGENT);
        }
      }
    }
  }
}

DEV void role_ffn(const Params& p, LAS unsigned char* L, int x, int i, int wave, int lane, u64 t0) {
  const int f = 9 * x + i;
  u64* ws = p.ws;
  const Mask mk = make_mask(lane, true);
  LAS float* red = (LAS float*)(L + F_RED);
  const rsrc_t rm = mk_rsrc(p.wmod, D * NMOD * 2), rc = mk_rsrc(p.c, D * 2), rx = mk_rsrc(p.x, D * 2);
  const int hq = 4 * x + (i >> 1), qr = i & 1;
  const rsrc_t rw = mk_rsrc(ws, G_END * 8);
  Tag tag;
  if (i < 8) {
    v4u wa[12];
    if (wave >= 3) {
      const rsrc_t rq = mk_rsrc(p.wq, D * 4096 * 2);
      const unsigned v = (lane >> 4) * 8192 + (lane & 15) * 16, s = (qr * 288 + (wave - 3) * 48) * 8192 + hq * 256;
#pragma unroll
      for (int t = 0; t < 12; ++t) wa[t] = ldb(rq, v, s + t * 4 * 8192);
      __builtin_amdgcn_sched_barrier(0);
    } else {
      __builtin_amdgcn_s_setprio(3);
      const u64 st = load_state(ws);
      float xv[9];
#pragma unroll
      for (int k = 0; k < 9; ++k) xv[k] = bf2f(ldb16(rx, (lane + 64 * k) * 2));
      const int p0 = wave * 64 + lane, pr = p0 < 144 ? p0 : 143;
      const unsigned xo = __builtin_amdgcn_raw_buffer_load_b32(rx, (qr * 288 + 2 * pr) * 2, 0, 0);
      float s = 0.f;
#pragma unroll
      for (int k = 0; k < 9; ++k) s += xv[k];
      const float mf = wave_sum(s) * (1.f / D);
      float v = 0.f;
#pragma unroll
      for (int k = 0; k < 9; ++k) { const float d = xv[k] - mf; v = fmaf(d, d, v); }
      const float rden = ln_rden(wave_sum(v) * (1.f / D)), mean = rbf(mf);
      tag = wave == 0 ? tag_put(L, st, lane) : make_tag(st);
      const int hw = pr / 24, hr = 2 * pr - hw * 48;
      unsigned hdst = F_H + ((hw * 4 + (hr & 3)) * 12 + (hr >> 2)) * 4;
      float ln0 = rbf(rbf(__uint_as_float(xo << 16) - mean) * rden), ln1 = rbf(rbf(__uint_as_float(xo & 0xFFFF0000u) - mean) * rden);
      PIN(hdst); PIN(ln0); PIN(ln1);
      gate_wait(t0, GATE_H1);
      G2 ga[8];
      gather2<8>(rw, tag, ga, [&](int k) { return G_MOD + (k & 3) * NMOD + (k >> 2) * D + qr * 288 + 2 * pr; });
      const float sa0 = rbf(fadd(fadd(ga[0].lo(), ga[1].lo()), fadd(ga[2].lo(), ga[3].lo()))), sa1 = rbf(fadd(fadd(ga[0].hi(), ga[1].hi()), fadd(ga[2].hi(), ga[3].hi())));
      const float ca0 = rbf(fadd(fadd(ga[4].lo(), ga[5].lo()), fadd(ga[6].lo(), ga[7].lo()))), ca1 = rbf(fadd(fadd(ga[4].hi(), ga[5].hi()), fadd(ga[6].hi(), ga[7].hi())));
      *(LAS unsigned*)(L + hdst) = pkr(rbf(ln0 * rbf(1.f + ca0)) + sa0);
      *(LAS unsigned*)(L + hdst + 48) = pkr(rbf(ln1 * rbf(1.f + ca1)) + sa1);
      __builtin_amdgcn_s_setprio(0);
    }
    Mask mq = make_mask(lane, (lane & 8) == 0);
    const int qe = (wave & 1) * 64 + lane;
    unsigned hsrc = F_H + ((wave - 3) * 4 + (lane >> 4)) * 48, qst = F_RED + 4608 + (wave - 3) * 1024 + lane * 16;
    unsigned qsrc = F_RED + 4608 + ((16 * ((qe & 7) >> 2) + (qe >> 3)) * 4 + (qe & 3)) * 4;
    gu64* qdst = (gu64*)ws + G_Q + (hq * 2 + qr) * 128 + qe;
    PIN(mq.m[0]); PIN(mq.m[1]); PIN(mq.m[2]); PIN(mq.m[3]); PIN(hsrc); PIN(qst); PIN(qsrc); PIN(qdst);
    bar();
    if (wave >= 3) {
      const LAS v4u* hp = (const LAS v4u*)(L + hsrc);
      const v4u h0 = hp[0], h1 = hp[1], h2 = hp[2];
      f32x4 d0 = {0.f, 0.f, 0.f, 0.f}, d1 = {0.f, 0.f, 0.f, 0.f};
#pragma unroll
      for (int t = 0; t < 4; ++t) { d0 = mfma(afrag(h0[t], mq), wa[t], d0); d1 = mfma(afrag(h1[t], mq), wa[4 + t], d1); }
#pragma unroll
      for (int t = 0; t < 4; t += 2) { d0 = mfma(afrag(h2[t], mq), wa[8 + t], d0); d1 = mfma(afrag(h2[t + 1], mq), wa[9 + t], d1); }
      *(LAS f32x4*)(L + qst) = d0 + d1;
    }
    bar();
    if (wave < 2) {
      const LAS float* src = (const LAS float*)(L + qsrc);
      pubp(qdst, fadd(fadd(fadd(src[0], src[256]), fadd(src[512], src[768])), fadd(src[1024], src[1280])), tag);
    }
  } else {
    if (wave == 0) tag_put(L, load_state(ws), lane);
    gate_wait(t0, GATE_MODB2);
    ModA ma;
    if (wave < 6) moda_load(p, 72 + 6 * x + wave, lane, ma);
    __builtin_amdgcn_sched_barrier(0);
    bar();
    if (wave < 6) {
      tag = tag_get(L);
      moda_finish(L, ws, 72 + 6 * x + wave, lane, ma, mk, (LAS float*)(L + F_RED + wave * 1024), tag);
    }
  }
  tag = tag_get(L);
  pretouch<1>(L, ws, [&](int) { const int e = wave * 64 + lane; return G_X2 + (e >> 3) * 16 + (e & 7); });
  const bool slow_tile = ((((unsigned)(uintptr_t)p.wg >> 7) + f) & 7) == 3;
  v4u wg[9], wu[9], wd[8];
  if (!slow_tile) gate_wait(t0, GATE_FFN);
  if (wave < 8) {
    const rsrc_t rg = mk_rsrc(p.wg, D * FF * 2), ru = mk_rsrc(p.wu, D * FF * 2);
    const unsigned v = (lane >> 3) * (FF * 2) + (lane & 7) * 16, s = wave * 72 * (FF * 2) + f * 128;
#pragma unroll
    for (int t = 0; t < 9; ++t) { wg[t] = ldb(rg, v, s + t * 8 * (FF * 2)); wu[t] = ldb(ru, v, s + t * 8 * (FF * 2)); }
  }
  __builtin_amdgcn_sched_barrier(0);
  unsigned h2w, h2r, stw, str_, gur, guw0, actw, actr, trw, trr;
  gu64* ddst;
  {
    const int e = wave * 64 + lane, hw = e / 72, rem = e - hw * 72, c = (wave & 3) * 16 + (lane >> 2);
    h2w = F_H2 + ((hw * 8 + (rem & 7)) * 12 + (rem >> 3)) * 4;
    h2r = F_H2 + ((wave & 7) * 8 + (lane >> 3)) * 48;
    stw = F_ST + wave * 8;
    str_ = F_ST + ((lane & 15) < 9 ? (lane & 15) : 9) * 8;
    guw0 = F_RED + (wave & 7) * 1040 + red_off(lane, 256);
    gur = F_RED + ((lane & 3) * 520 + c) * 4;
    actw = F_ACT + ((lane & 3) == 0 ? (c & 7) * 8 + (c >> 3) : 64) * 4;
    actr = F_ACT + (lane >> 3) * 32;
    trw = F_TR + wave * 640 + red_off(lane, 128);
    trr = F_TR + wave * 640 + lane * 4;
    ddst = (gu64*)ws + G_DN + f * 576 + wave * 64 + lane;
    if (wave == 0 && lane == 0) *(LAS f32x2*)(L + F_ST + 72) = f32x2{0.f, 0.f};
  }
  PIN(h2w); PIN(h2r); PIN(stw); PIN(str_); PIN(guw0); PIN(gur); PIN(actw); PIN(actr); PIN(trw); PIN(trr); PIN(ddst);
  {
    const int e = wave * 64 + lane;
    u64 gs[9];
    bool have_x2 = false;
    for (int spin = 0; spin < SPIN_CAP; ++spin) {
#pragma unroll
      for (int k = 0; k < 8; ++k) gs[k] = peek(ws, G_MOD + (k & 3) * NMOD + (k < 4 ? 3 : 4) * D + e);
      gs[8] = peek(ws, G_X2 + (e >> 3) * 16 + (e & 7));
      Chk c, cx;
#pragma unroll
      for (int k = 0; k < 8; ++k) c.add(gs[k], tag);
      cx.add(gs[8], tag);
      have_x2 = cx.all_ok();
      if (c.all_ok()) break;
    }
    float sb = rbf(fadd(fadd(gval(gs[0]), gval(gs[1])), fadd(gval(gs[2]), gval(gs[3]))));
    float c1b = rbf(1.f + rbf(fadd(fadd(gval(gs[4]), gval(gs[5])), fadd(gval(gs[6]), gval(gs[7])))));
    float on = (lane & 15) < 9 ? 64.f : 0.f;
    PIN(sb); PIN(c1b); PIN(on);
    u64 gx[1] = {gs[8]};
    if (!have_x2) gather<1>(ws, tag, gx, [&](int) { return G_X2 + (e >> 3) * 16 + (e & 7); });
    const float xv = gval(gx[0]);
    const float sh = __uint_as_float(__builtin_amdgcn_readfirstlane(__float_as_uint(xv)));
    const float d = xv - sh;
    float s1 = d, s2 = d * d;
    s1 += dpp<ROR8>(s1); s2 += dpp<ROR8>(s2); s1 += dpp<ROR4>(s1); s2 += dpp<ROR4>(s2);
    s1 += dpp<ROR2>(s1); s2 += dpp<ROR2>(s2); s1 += dpp<ROR1>(s1); s2 += dpp<ROR1>(s2);
    s1 = x16_sum(s1); s2 = x16_sum(s2);
    const float dm = s1 * (1.f / 64.f);
    if (lane == 0) *(LAS f32x2*)(L + stw) = f32x2{sh + dm, fmaf(-dm, s1, s2)};
    bar();
    const f32x2 st = *(const LAS f32x2*)(L + str_);
    __builtin_amdgcn_sched_barrier(0);
    {
      const rsrc_t rd = mk_rsrc(p.wd, FF * D * 2);
      const unsigned v = (lane >> 3) * (D * 2) + (lane & 7) * 16, s = f * 64 * (D * 2) + wave * 128;
#pragma unroll
      for (int t = 0; t < DN_SECOND; ++t) wd[t] = ldb(rd, v, s + t * 8 * (D * 2));
      __builtin_amdgcn_sched_barrier(0);
    }
    const float mf = row_sum(st[0]) * (1.f / 9.f);
    const float dv = st[0] - mf;
    const float m2 = row_sum(fmaf(on * dv, dv, st[1]));
    *(LAS unsigned*)(L + h2w) = ln_apply_pk(xv, rbf(mf), ln_rden(m2 * (1.f / D)), c1b, sb);
  }
  bar();
  if (f == 0 && threadIdx.x == 0) __hip_atomic_store((gu64*)ws + G_EPOCH, ((u64)tag.hi << 32) | (u64)tag.rest, RLX_AGENT);
  v4u h0, h1;
  unsigned h8;
  {
    const LAS v4u* hp = (const LAS v4u*)(L + h2r);
    h0 = hp[0]; h1 = hp[1]; h8 = ((const LAS unsigned*)hp)[8];
  }
  __builtin_amdgcn_sched_barrier(0);
  {
    const rsrc_t rd = mk_rsrc(p.wd, FF * D * 2);
    const unsigned v = (lane >> 3) * (D * 2) + (lane & 7) * 16, s = f * 64 * (D * 2) + wave * 128;
#pragma unroll
    for (int t = DN_SECOND; t < 8; ++t) wd[t] = ldb(rd, v, s + t * 8 * (D * 2));
    __builtin_amdgcn_sched_barrier(0);
  }
  if (wave < 8) {
    PIN(h0); PIN(h1); PIN(h8);
    f32x4 dg = {0.f, 0.f, 0.f, 0.f}, du = {0.f, 0.f, 0.f, 0.f};
#pragma unroll
    for (int t = 0; t < 9; ++t) {
      const v4u a = afrag(t < 4 ? h0[t & 3] : t < 8 ? h1[t & 3] : h8, mk);
      dg = mfma(a, wg[t], dg);
      du = mfma(a, wu[t], du);
    }
    *(LAS f32x4*)(L + guw0) = dg;
    *(LAS f32x4*)(L + guw0 + ((lane >> 5) == ((lane >> 3) & 1) ? 512 : 0)) = du;
  }
  bar();
  if (wave < 4) {
    const LAS float* r0 = (const LAS float*)(L + gur);
    float g = faddn(fadd(r0[0], r0[64]), fadd(r0[260], r0[324])), u = faddn(fadd(r0[128], r0[192]), fadd(r0[388], r0[452]));
    g += dpp<XOR1>(g); u += dpp<XOR1>(u);
    g += dpp<XOR2>(g); u += dpp<XOR2>(u);
    g = rbf(g); u = rbf(u);
    const float y = g * fmaf(g * g, 0.044715f * 0.7978845608028654f * 2.f * LOG2E, 0.7978845608028654f * 2.f * LOG2E);
    const float th = fmaf(-2.f, rcp_f(__builtin_amdgcn_exp2f(y) + 1.f), 1.f);
    const float act = rbf(0.5f * g * (1.f + th)) * u;
    *(LAS unsigned*)(L + actw) = pkr(act);
  }
  bar();
  {
    const LAS v4u* ap = (const LAS v4u*)(L + actr);
    const v4u a0 = ap[0], a1 = ap[1];
    f32x4 d0 = {0.f, 0.f, 0.f, 0.f}, d1 = {0.f, 0.f, 0.f, 0.f};
#pragma unroll
    for (int t = 0; t < 4; ++t) { d0 = mfma(afrag(a0[t], mk), wd[t], d0); d1 = mfma(afrag(a1[t], mk), wd[4 + t], d1); }
    *(LAS f32x4*)(L + trw) = d0 + d1;
    lds_wait();
    pubp(ddst, fadd(*(const LAS float*)(L + trr), *(const LAS float*)(L + trr + 256)), tag);
  }
}

DEV void role_modb(const Params& p, LAS unsigned char* L, int x, int wave, int lane, u64 t0, u64 code0) {
  if (wave == 8) {
    const unsigned dump = (unsigned)(uintptr_t)(L + L_PF);
#pragma unroll
    for (int k = 0; k < (CODE_PF + 8191) / 8192; ++k) {
      const unsigned o = k * 8192 + lane * 128;
      asm volatile("s_mov_b32 m0, %0\n\ts_nop 0\n\tglobal_load_lds_dword %1, off" :: "s"(dump), "v"(code0 + (o < CODE_PF - 128 ? o : CODE_PF - 128)) : "m0", "memory");
    }
  }
  const Mask mk = make_mask(lane, true);
  if (wave == 0) tag_put(L, load_state(p.ws), lane);
  bar();
  const Tag tag = tag_get(L);
  gate_wait(t0, GATE_MODB);
  for (int k = wave; k < 12; k += 9) {
    ModA ma;
    moda_load(p, 120 + x + 8 * k, lane, ma);
    __builtin_amdgcn_sched_barrier(0);
    moda_finish(L, p.ws, 120 + x + 8 * k, lane, ma, mk, (LAS float*)(L + wave * 1024), tag);
  }
}

__global__ void __launch_bounds__(NTHR) adaln_block_kernel(Params p) {
  __shared__ __attribute__((aligned(16))) unsigned char smem[L_END];
  LAS unsigned char* L = (LAS unsigned char*)smem;
  const int lane = threadIdx.x & 63, wave = __builtin_amdgcn_readfirstlane(threadIdx.x >> 6);
  u64 code0;
  asm volatile("s_getpc_b64 %0" : "=s"(code0));
  code0 &= ~0xFFull;
  const u64 t0 = __builtin_amdgcn_s_memrealtime();
  const int x = blockIdx.x & 7, idx = blockIdx.x >> 3;
  if (idx < 22) role_att(p, L, x, idx, wave, lane, t0);
  else if (idx < 31) role_ffn(p, L, x, idx - 22, wave, lane, t0);
  else role_modb(p, L, x, wave, lane, t0, code0);
}

__global__ void k_null(u64* ws) { if (blockIdx.x == 0 && threadIdx.x == 0) ws[300000] = __builtin_amdgcn_s_memrealtime(); }
extern "C" void kernel_launch(void* const* d_in, const int* in_sizes, int n_in, void* d_out, int out_size, void* d_ws,
                              size_t ws_size, hipStream_t stream) {
  Params p;
  p.x = (const u16*)d_in[0];
  p.kc = (const u16*)d_in[1];
  p.vc = (const u16*)d_in[2];
  p.wq = (const u16*)d_in[3];
  p.wo = (const u16*)d_in[4];
  p.wg = (const u16*)d_in[6];
  p.wu = (const u16*)d_in[7];
  p.wd = (const u16*)d_in[8];
  p.c = (const u16*)d_in[9];
  p.wmod = (const u16*)d_in[10];
  p.out = (u16*)d_out;
  p.ws = (u64*)d_ws;
  hipLaunchKernelGGL(adaln_block_kernel, dim3(256), dim3(NTHR), 0, stream, p);
  hipLaunchKernelGGL(k_null, dim3(256), dim3(64), 0, stream, p.ws);
}
```

```cpp
#include <hip/hip_runtime.h>
#include <stdint.h>

typedef unsigned short u16;
typedef unsigned long long u64;
typedef unsigned v4u __attribute__((ext_vector_type(4)));
typedef float f32x4 __attribute__((ext_vector_type(4)));
typedef float f32x2 __attribute__((ext_vector_type(2)));
typedef __bf16 bf16x2 __attribute__((ext_vector_type(2)));
typedef short bf16x8 __attribute__((ext_vector_type(8)));
typedef __amdgpu_buffer_rsrc_t rsrc_t;
#define GAS __attribute__((address_space(1)))
#define LAS __attribute__((address_space(3)))
#define DEV __device__ __forceinline__
typedef GAS u64 gu64;
#define RLX_AGENT __ATOMIC_RELAXED, __HIP_MEMORY_SCOPE_AGENT

constexpr int D = 576, SK = 65536, WIN = 4096, DH = 128, FF = 4608, NMOD = 6 * D;
constexpr int NSPLIT = 11;
constexpr int NTHR = 576;
constexpr int SPIN_CAP = 1 << 16;
constexpr unsigned GATE_ATT = 140;
constexpr unsigned GATE_MODB = 200;
constexpr unsigned GATE_MODB2 = 120;
constexpr unsigned GATE_H1 = 260;
constexpr unsigned GATE_Q = 450;
constexpr unsigned GATE_FFN = 790;
constexpr int Q_STAGGER = 16;
constexpr int ATT_N1 = 5, ATT_D1 = 2;
constexpr int DN_SECOND = 4;
constexpr int CODE_PF = 21504;

constexpr int G_EPOCH = 0;
constexpr int G_MOD = 64;
constexpr int G_Q = G_MOD + 4 * NMOD;
constexpr int G_O = G_Q + 32 * 2 * 128;
constexpr int G_WO = G_O + 16 * 8 * NSPLIT * 64;
constexpr int G_X2 = G_WO + 128 * 576;
constexpr int G_DN = G_X2 + 72 * 16;
constexpr int G_END = G_DN + 72 * 576;

constexpr int L_K = 0;
constexpr int L_OL = 98304;
constexpr int L_Q = L_OL + 9216;
constexpr int L_MX = L_Q + 512;
constexpr int L_LS = L_MX + 64;
constexpr int L_OB = L_LS + 64;
constexpr int L_PF = L_OB + 128;
constexpr int L_WO = L_PF + 256;
constexpr int L_MA = L_WO + 36864;
constexpr int L_CNT = L_MA + 1536;
constexpr int L_TAG = L_MA + 1536 + 16;
constexpr int L_END = L_TAG + 16;
constexpr int F_RED = 0;
constexpr int F_H = 16384;
constexpr int F_H2 = 17664;
constexpr int F_ACT = 20736;
constexpr int F_TR = 21248;
constexpr int F_ST = 27136;

struct Params {
  const u16 *x, *kc, *vc, *wq, *wo, *wg, *wu, *wd, *c, *wmod;
  u16* out;
  u64* ws;
};

DEV float bf2f(unsigned b) { return __uint_as_float(b << 16); }
DEV unsigned cvt2(float lo, float hi) {
  const f32x2 v = {lo, hi};
  return __builtin_bit_cast(unsigned, __builtin_convertvector(v, bf16x2));
}
DEV unsigned pkr(float f) { return cvt2(f, f); }
DEV float rbf(float f) { return __uint_as_float(cvt2(f, f) & 0xFFFF0000u); }
constexpr int TAG_LO_BITS = 4;
constexpr unsigned TAGM = (1u << TAG_LO_BITS) - 1u;
struct Tag { unsigned hi, lo, rest; };
DEV Tag make_tag(u64 st) {
  const u64 nx = st * 6364136223846793005ull + 1442695040888963407ull;
  Tag t;
  t.hi = __builtin_amdgcn_readfirstlane((unsigned)(nx >> 32));
  t.rest = __builtin_amdgcn_readfirstlane((unsigned)nx);
  t.lo = t.rest >> (32 - TAG_LO_BITS);
  return t;
}
DEV u64 enc(float v, unsigned hi, unsigned lo) {
  const unsigned u = (__float_as_uint(v) + (TAGM + 1) / 2) & ~TAGM;
  return ((u64)hi << 32) | (u64)(u | lo);
}
DEV Tag tag_put(LAS unsigned char* L, u64 st, int lane) {
  const Tag t = make_tag(st);
  if (lane == 0) *(LAS v4u*)(L + L_TAG) = v4u{t.hi, t.lo, t.rest, 0u};
  return t;
}
DEV Tag tag_get(const LAS unsigned char* L) {
  const v4u v = *(const LAS v4u*)(L + L_TAG);
  Tag t; t.hi = __builtin_amdgcn_readfirstlane(v[0]); t.lo = __builtin_amdgcn_readfirstlane(v[1]); t.rest = __builtin_amdgcn_readfirstlane(v[2]);
  return t;
}
DEV unsigned or_xor(unsigned a, unsigned b, unsigned c) { return __builtin_amdgcn_bitop3_b32(a, b, c, 0xF6); }
struct Chk {
  unsigned a = 0, b = 0;
  DEV void add(u64 g, const Tag& t) { a = or_xor(a, (unsigned)(g >> 32), t.hi); b = or_xor(b, (unsigned)g, t.lo); }
  DEV bool all_ok() const { return __builtin_amdgcn_ballot_w64((a | (b & TAGM)) != 0u) == 0ull; }
};
DEV float gval(u64 g) { return __uint_as_float((unsigned)g & ~TAGM); }
DEV void pub(u64* ws, int idx, float v, const Tag& t) { __hip_atomic_store((gu64*)ws + idx, enc(v, t.hi, t.lo), RLX_AGENT); }
DEV void pubp(gu64* dst, float v, const Tag& t) { __hip_atomic_store(dst, enc(v, t.hi, t.lo), RLX_AGENT); }
DEV u64 peek(u64* ws, int idx) { return __hip_atomic_load((gu64*)ws + idx, RLX_AGENT); }
template <int N, class F> DEV void gather(u64* ws, const Tag& t, u64 (&g)[N], F idx) {
  for (int spin = 0; spin < SPIN_CAP; ++spin) {
#pragma unroll
    for (int k = 0; k < N; ++k) g[k] = peek(ws, idx(k));
    Chk c;
#pragma unroll
    for (int k = 0; k < N; ++k) c.add(g[k], t);
    if (c.all_ok()) break;
  }
}

struct G2 { v4u v; DEV float lo() const { return __uint_as_float(v[0] & ~TAGM); } DEV float hi() const { return __uint_as_float(v[2] & ~TAGM); } };
template <int N, class F> DEV void gather2(rsrc_t rw, const Tag& t, G2 (&g)[N], F idx) {
  for (int spin = 0; spin < SPIN_CAP; ++spin) {
    asm volatile("" ::: "memory");
#pragma unroll
    for (int k = 0; k < N; ++k) g[k].v = __builtin_amdgcn_raw_buffer_load_b128(rw, idx(k) * 8, 0, 16);
    unsigned a = 0, b = 0;
#pragma unroll
    for (int k = 0; k < N; ++k) { a = or_xor(or_xor(a, g[k].v[1], t.hi), g[k].v[3], t.hi); b = or_xor(or_xor(b, g[k].v[0], t.lo), g[k].v[2], t.lo); }
    if (__builtin_amdgcn_ballot_w64((a | (b & TAGM)) != 0u) == 0ull) break;
  }
}
DEV void q_handoff(rsrc_t rw, unsigned voff, unsigned qdst, const Tag& t) {
  static_assert(TAG_LO_BITS == 4, "masks below");
  unsigned cnt;
  asm volatile(
      "s_mov_b32 %[cnt], %[cap]\n\t"
      "buffer_load_dwordx4 v[100:103], %[vo], %[rs], 0 offen sc1\n\t"
      "buffer_load_dwordx4 v[104:107], %[vo], %[rs], 0 offen offset:1024 sc1\n\t"
      "buffer_load_dwordx4 v[108:111], %[vo], %[rs], 0 offen offset:2048 sc1\n\t"
      "buffer_load_dwordx4 v[112:115], %[vo], %[rs], 0 offen offset:3072 sc1\n\t"
      "s_sleep %[stag]\n\t"
      "1:\n\t"
      "buffer_load_dwordx4 v[116:119], %[vo], %[rs], 0 offen sc1\n\t"
      "buffer_load_dwordx4 v[120:123], %[vo], %[rs], 0 offen offset:1024 sc1\n\t"
      "buffer_load_dwordx4 v[124:127], %[vo], %[rs], 0 offen offset:2048 sc1\n\t"
      "buffer_load_dwordx4 v[128:131], %[vo], %[rs], 0 offen offset:3072 sc1\n\t"
      "s_waitcnt vmcnt(4)\n\t"
      "v_xor_b32 v132, %[th], v101\n\t"
      "v_bitop3_b32 v132, v132, v103, %[th] bitop3:0xf6\n\t"
      "v_bitop3_b32 v132, v132, v105, %[th] bitop3:0xf6\n\t"
      "v_bitop3_b32 v132, v132, v107, %[th] bitop3:0xf6\n\t"
      "v_bitop3_b32 v132, v132, v109, %[th] bitop3:0xf6\n\t"
      "v_bitop3_b32 v132, v132, v111, %[th] bitop3:0xf6\n\t"
      "v_bitop3_b32 v132, v132, v113, %[th] bitop3:0xf6\n\t"
      "v_bitop3_b32 v132, v132, v115, %[th] bitop3:0xf6\n\t"
      "v_xor_b32 v133, %[tl], v100\n\t"
      "v_bitop3_b32 v133, v133, v102, %[tl] bitop3:0xf6\n\t"
      "v_bitop3_b32 v133, v133, v104, %[tl] bitop3:0xf6\n\t"
      "v_bitop3_b32 v133, v133, v106, %[tl] bitop3:0xf6\n\t"
      "v_bitop3_b32 v133, v133, v108, %[tl] bitop3:0xf6\n\t"
      "v_bitop3_b32 v133, v133, v110, %[tl] bitop3:0xf6\n\t"
      "v_bitop3_b32 v133, v133, v112, %[tl] bitop3:0xf6\n\t"
      "v_bitop3_b32 v133, v133, v114, %[tl] bitop3:0xf6\n\t"
      "v_and_b32 v133, 15, v133\n\t"
      "v_or_b32 v132, v132, v133\n\t"
      "v_cmp_ne_u32 vcc, 0, v132\n\t"
      "s_cbranch_vccz 2f\n\t"
      "buffer_load_dwordx4 v[100:103], %[vo], %[rs], 0 offen sc1\n\t"
      "buffer_load_dwordx4 v[104:107], %[vo], %[rs], 0 offen offset:1024 sc1\n\t"
      "buffer_load_dwordx4 v[108:111], %[vo], %[rs], 0 offen offset:2048 sc1\n\t"
      "buffer_load_dwordx4 v[112:115], %[vo], %[rs], 0 offen offset:3072 sc1\n\t"
      "s_waitcnt vmcnt(4)\n\t"
      "v_xor_b32 v132, %[th], v117\n\t"
      "v_bitop3_b32 v132, v132, v119, %[th] bitop3:0xf6\n\t"
      "v_bitop3_b32 v132, v132, v121, %[th] bitop3:0xf6\n\t"
      "v_bitop3_b32 v132, v132, v123, %[th] bitop3:0xf6\n\t"
      "v_bitop3_b32 v132, v132, v125, %[th] bitop3:0xf6\n\t"
      "v_bitop3_b32 v132, v132, v127, %[th] bitop3:0xf6\n\t"
      "v_bitop3_b32 v132, v132, v129, %[th] bitop3:0xf6\n\t"
      "v_bitop3_b32 v132, v132, v131, %[th] bitop3:0xf6\n\t"
      "v_xor_b32 v133, %[tl], v116\n\t"
      "v_bitop3_b32 v133, v133, v118, %[tl] bitop3:0xf6\n\t"
      "v_bitop3_b32 v133, v133, v120, %[tl] bitop3:0xf6\n\t"
      "v_bitop3_b32 v133, v133, v122, %[tl] bitop3:0xf6\n\t"
      "v_bitop3_b32 v133, v133, v124, %[tl] bitop3:0xf6\n\t"
      "v_bitop3_b32 v133, v133, v126, %[tl] bitop3:0xf6\n\t"
      "v_bitop3_b32 v133, v133, v128, %[tl] bitop3:0xf6\n\t"
      "v_bitop3_b32 v133, v133, v130, %[tl] bitop3:0xf6\n\t"
      "v_and_b32 v133, 15, v133\n\t"
      "v_or_b32 v132, v132, v133\n\t"
      "v_cmp_ne_u32 vcc, 0, v132\n\t"
      "s_cbranch_vccz 3f\n\t"
      "s_sub_u32 %[cnt], %[cnt], 1\n\t"
      "s_cbranch_scc0 1b\n\t"
      "2:\n\t"
      "v_and_b32 v132, 0xfffffff0, v100\n\t"
      "v_and_b32 v133, 0xfffffff0, v104\n\t"
      "v_add_f32 v132, v132, v133\n\t"
      "v_and_b32 v133, 0xfffffff0, v102\n\t"
      "v_and_b32 v136, 0xfffffff0, v106\n\t"
      "v_add_f32 v133, v133, v136\n\t"
      "v_cvt_pk_bf16_f32 v132, v132, v133\n\t"
      "ds_write_b32 %[qd], v132\n\t"
      "v_and_b32 v134, 0xfffffff0, v108\n\t"
      "v_and_b32 v135, 0xfffffff0, v112\n\t"
      "v_add_f32 v134, v134, v135\n\t"
      "v_and_b32 v135, 0xfffffff0, v110\n\t"
      "v_and_b32 v136, 0xfffffff0, v114\n\t"
      "v_add_f32 v135, v135, v136\n\t"
      "v_cvt_pk_bf16_f32 v134, v134, v135\n\t"
      "ds_write_b32 %[qd], v134 offset:256\n\t"
      "s_branch 4f\n\t"
      "3:\n\t"
      "v_and_b32 v132, 0xfffffff0, v116\n\t"
      "v_and_b32 v133, 0xfffffff0, v120\n\t"
      "v_add_f32 v132, v132, v133\n\t"
      "v_and_b32 v133, 0xfffffff0, v118\n\t"
      "v_and_b32 v136, 0xfffffff0, v122\n\t"
      "v_add_f32 v133, v133, v136\n\t"
      "v_cvt_pk_bf16_f32 v132, v132, v133\n\t"
      "ds_write_b32 %[qd], v132\n\t"
      "v_and_b32 v134, 0xfffffff0, v124\n\t"
      "v_and_b32 v135, 0xfffffff0, v128\n\t"
      "v_add_f32 v134, v134, v135\n\t"
      "v_and_b32 v135, 0xfffffff0, v126\n\t"
      "v_and_b32 v136, 0xfffffff0, v130\n\t"
      "v_add_f32 v135, v135, v136\n\t"
      "v_cvt_pk_bf16_f32 v134, v134, v135\n\t"
      "ds_write_b32 %[qd], v134 offset:256\n\t"
      "4:\n\t"
      "s_waitcnt lgkmcnt(0)\n\t"
      "s_barrier\n\t"
      "s_waitcnt vmcnt(0)"
      : [cnt] "=&s"(cnt)
      : [vo] "v"(voff), [rs] "s"(rw), [th] "s"(t.hi), [tl] "s"(t.lo), [qd] "v"(qdst), [cap] "n"(SPIN_CAP / 2), [stag] "n"(Q_STAGGER)
      : "vcc", "scc", "memory", "v100", "v101", "v102", "v103", "v104", "v105", "v106", "v107", "v108", "v109", "v110", "v111", "v112", "v113", "v114", "v115", "v116", "v117", "v118", "v119", "v120", "v121", "v122", "v123", "v124", "v125", "v126", "v127", "v128", "v129", "v130", "v131", "v132", "v133", "v134", "v135", "v136");
}
template <int N, class F> DEV void pretouch(LAS unsigned char* L, u64* ws, F idx) {
  const unsigned dump = (unsigned)(uintptr_t)(L + L_PF);
#pragma unroll
  for (int k = 0; k < N; ++k)
    asm volatile("s_mov_b32 m0, %0\n\ts_nop 0\n\tglobal_load_lds_dword %1, off sc1" :: "s"(dump), "v"((gu64*)ws + idx(k)) : "m0", "memory");
}
DEV f32x4 mfma(v4u a, v4u b, f32x4 c) {
  return __builtin_amdgcn_mfma_f32_16x16x32_bf16(__builtin_bit_cast(bf16x8, a), __builtin_bit_cast(bf16x8, b), c, 0, 0, 0);
}
DEV rsrc_t mk_rsrc(const void* p, unsigned bytes) { return __builtin_amdgcn_make_buffer_rsrc((void*)p, 0, (int)bytes, 0x00020000); }
DEV v4u ldb(rsrc_t r, unsigned voff, unsigned soff) { return __builtin_amdgcn_raw_buffer_load_b128(r, (int)voff, (int)soff, 2); }
DEV unsigned ldb16(rsrc_t r, unsigned voff) { return __builtin_amdgcn_raw_buffer_load_b16(r, (int)voff, 0, 0); }
DEV u64 load_state(u64* ws) {
  int zoff = G_EPOCH * 8;
  asm volatile("" : "+v"(zoff));
  typedef unsigned v2u_ __attribute__((ext_vector_type(2)));
  const v2u_ v = __builtin_amdgcn_raw_buffer_load_b64(mk_rsrc(ws, 64), zoff, 0, 16);
  return ((u64)v[1] << 32) | v[0];
}
DEV void bar() {
  asm volatile("s_waitcnt lgkmcnt(0)" ::: "memory");
  __builtin_amdgcn_s_barrier();
  asm volatile("" ::: "memory");
}
#define PIN(x) asm volatile("" : "+v"(x))
DEV void lds_wait() { asm volatile("s_waitcnt lgkmcnt(0)" ::: "memory"); }
template <int CTRL> DEV float dpp(float x) {
  return __builtin_bit_cast(float, __builtin_amdgcn_mov_dpp(__builtin_bit_cast(int, x), CTRL, 0xf, 0xf, true));
}
constexpr int ROR8 = 0x128, ROR4 = 0x124, ROR2 = 0x122, ROR1 = 0x121, XOR1 = 0xB1, XOR2 = 0x4E;
DEV float fmx(float a, float b) { float r; asm("v_max_f32 %0, %1, %2\n\ts_nop 1" : "=v"(r) : "v"(a), "v"(b)); return r; }
DEV float fmx3(float a, float b, float c) { float r; asm("v_max3_f32 %0, %1, %2, %3" : "=v"(r) : "v"(a), "v"(b), "v"(c)); return r; }
DEV float fmx3n(float a, float b, float c) { float r; asm("v_max3_f32 %0, %1, %2, %3\n\ts_nop 1" : "=v"(r) : "v"(a), "v"(b), "v"(c)); return r; }
DEV float x16_sum(float x) {
  auto s = __builtin_amdgcn_permlane16_swap(__float_as_uint(x), __float_as_uint(x), false, false);
  x = __uint_as_float(s[0]) + __uint_as_float(s[1]);
  auto t = __builtin_amdgcn_permlane32_swap(__float_as_uint(x), __float_as_uint(x), false, false);
  return __uint_as_float(t[0]) + __uint_as_float(t[1]);
}
DEV float x16_max(float x) {
  auto s = __builtin_amdgcn_permlane16_swap(__float_as_uint(x), __float_as_uint(x), false, false);
  x = fmaxf(__uint_as_float(s[0]), __uint_as_float(s[1]));
  auto t = __builtin_amdgcn_permlane32_swap(__float_as_uint(x), __float_as_uint(x), false, false);
  return fmaxf(__uint_as_float(t[0]), __uint_as_float(t[1]));
}
DEV float row_sum(float x) { x += dpp<ROR8>(x); x += dpp<ROR4>(x); x += dpp<ROR2>(x); x += dpp<ROR1>(x); return x; }
DEV float wave_sum(float x) { return x16_sum(row_sum(x)); }
constexpr float LOG2E = 1.4426950408889634f;
DEV float fadd(float a, float b) { float r; asm("v_add_f32 %0, %1, %2" : "=v"(r) : "v"(a), "v"(b)); return r; }
DEV float faddn(float a, float b) { float r; asm("v_add_f32 %0, %1, %2\n\ts_nop 1" : "=v"(r) : "v"(a), "v"(b)); return r; }
DEV float exp_f(float x) { return __builtin_amdgcn_exp2f(x * LOG2E); }
DEV float rcp_f(float x) { return __builtin_amdgcn_rcpf(x); }
DEV float pair_pick(float a, float b, int lane) {
  const float a2 = dpp<0x50>(a), b2 = dpp<0x50>(b);
  const float a3 = dpp<0xFA>(a), b3 = dpp<0xFA>(b);
  const float lo = (lane & 1) ? b2 : a2, hi = (lane & 1) ? b3 : a3;
  return (lane & 4) ? hi : lo;
}
DEV void gate_wait(u64 t0, unsigned ticks) { while (__builtin_amdgcn_s_memrealtime() - t0 < (u64)ticks) __builtin_amdgcn_s_sleep(2); }

struct Mask { unsigned m[4]; };
DEV Mask make_mask(int lane, bool on) {
  Mask k;
  const unsigned h = on ? ((lane & 1) ? 0xFFFF0000u : 0x0000FFFFu) : 0u;
#pragma unroll
  for (int r = 0; r < 4; ++r) k.m[r] = (((lane & 7) >> 1) == r) ? h : 0u;
  return k;
}
DEV v4u afrag(unsigned pp, const Mask& k) { return v4u{pp & k.m[0], pp & k.m[1], pp & k.m[2], pp & k.m[3]}; }

DEV unsigned ln_apply_pk(float x, float mean_b, float rden, float c1, float shift) {
  const float ln = rbf(rbf(x - mean_b) * rden);
  return pkr(rbf(ln * c1) + shift);
}
DEV float ln_rden(float var) { return rcp_f(rbf(__builtin_amdgcn_sqrtf(rbf(rbf(var) + 1e-5f)))); }

DEV unsigned red_off(int lane, int dump) {
  const int u = (lane >> 3) & 1;
  return 4 * ((lane >> 5) == u ? u * 64 + 8 * (lane & 7) + 4 * ((lane >> 4) & 1) : dump);
}
DEV unsigned silu_pk(unsigned craw) { const float v = bf2f(craw); return pkr(v * rcp_f(1.f + exp_f(-v))); }

struct WoAddr { unsigned tro, tri, obo, wro; gu64* dst; };
DEV WoAddr wo_prep(u64* ws, int pidx, int wave, int lane) {
  WoAddr a;
  a.tro = L_OL + wave * 640 + red_off(lane, 128); a.tri = L_OL + wave * 640 + lane * 4; a.obo = L_OB + (lane >> 3) * 16; a.wro = L_WO + wave * 4096 + lane * 16;
  a.dst = (gu64*)ws + G_WO + pidx * 576 + wave * 64 + lane;
  PIN(a.tro); PIN(a.tri); PIN(a.obo); PIN(a.wro); PIN(a.dst);
  return a;
}
DEV void wo_phase(LAS unsigned char* L, const WoAddr& a, const Tag& tag, const Mask& mk) {
  bar();
  v4u ob = *(const LAS v4u*)(L + a.obo);
  const LAS v4u* wr = (const LAS v4u*)(L + a.wro);
  v4u w0 = wr[0], w1 = wr[64], w2 = wr[128], w3 = wr[192];
  PIN(ob); PIN(w0); PIN(w1); PIN(w2); PIN(w3);
  __builtin_amdgcn_sched_barrier(0);
  f32x4 d0 = {0.f, 0.f, 0.f, 0.f}, d1 = {0.f, 0.f, 0.f, 0.f};
  d0 = mfma(afrag(ob[0], mk), w0, d0); d1 = mfma(afrag(ob[1], mk), w1, d1);
  d0 = mfma(afrag(ob[2], mk), w2, d0); d1 = mfma(afrag(ob[3], mk), w3, d1);
  *(LAS f32x4*)(L + a.tro) = d0 + d1;
  lds_wait();
  pubp(a.dst, fadd(*(const LAS float*)(L + a.tri), *(const LAS float*)(L + a.tri + 256)), tag);
}

struct ModA { v4u w[18]; unsigned c[3]; };
DEV void moda_load(const Params& p, int mt, int lane, ModA& m) {
  const rsrc_t rm = mk_rsrc(p.wmod, D * NMOD * 2), rc = mk_rsrc(p.c, D * 2);
  const int r0 = (mt & 3) * 144;
#pragma unroll
  for (int k = 0; k < 3; ++k) m.c[k] = ldb16(rc, (r0 + (k < 2 || lane < 16 ? lane + 64 * k : 143)) * 2);
  const unsigned v = (r0 + (lane >> 3)) * (NMOD * 2) + (lane & 7) * 16;
#pragma unroll
  for (int t = 0; t < 18; ++t) m.w[t] = ldb(rm, v, (mt >> 2) * 128 + t * 8 * (NMOD * 2));
}
DEV void moda_finish(LAS unsigned char* L, u64* ws, int mt, int lane, ModA& m, const Mask& mk, LAS float* tr, const Tag& tag) {
  unsigned sp[18];
  {
    const unsigned s0 = silu_pk(m.c[0]), s1 = silu_pk(m.c[1]), s2 = silu_pk(m.c[2]);
#pragma unroll
    for (int t = 0; t < 18; ++t) sp[t] = __builtin_amdgcn_ds_bpermute(((8 * t + (lane >> 3)) & 63) * 4, t < 8 ? s0 : t < 16 ? s1 : s2);
  }
  gu64* dst = (gu64*)ws + G_MOD + (mt & 3) * NMOD + (mt >> 2) * 64 + lane;
  unsigned tro = red_off(lane, 128);
  PIN(dst); PIN(tro);
#pragma unroll
  for (int t = 0; t < 18; ++t) PIN(sp[t]);
  __builtin_amdgcn_sched_barrier(0);
  f32x4 d0 = {0.f, 0.f, 0.f, 0.f}, d1 = {0.f, 0.f, 0.f, 0.f};
#pragma unroll
  for (int t = 0; t < 18; t += 2) {
    d0 = mfma(afrag(sp[t], mk), m.w[t], d0);
    __builtin_amdgcn_sched_barrier(0);
    d1 = mfma(afrag(sp[t + 1], mk), m.w[t + 1], d1);
    __builtin_amdgcn_sched_barrier(0);
  }
  *(LAS f32x4*)((LAS unsigned char*)tr + tro) = d0 + d1;
  lds_wait();
  pubp(dst, fadd(tr[lane], tr[64 + lane]), tag);
}

DEV void role_att(const Params& p, LAS unsigned char* L, int x, int i, int wave, int lane, u64 t0) {
  const int g = 2 * x + (i >= NSPLIT ? 1 : 0), j = i >= NSPLIT ? i - NSPLIT : i;
  const bool odd = g & 1;
  const int nm = odd ? 4 : 5;
  const bool moda = j < nm;
  const int ntile = !moda ? 24 : (!odd && j >= 3) ? 23 : 22;
  const int tile0 = !moda ? (odd ? 88 : 112) + 24 * (j - nm) : (!odd && j == 4) ? 89 : 22 * j;
  const bool owner = j < 8;
  int redc = -1;
  if (j >= 8) redc = 9 * x + (g & 1) * 3 + (j - 8);
  else if (j == 0) redc = 9 * x + 6 + (g & 1);
  else if (j == 1 && (g & 1) == 0) redc = 9 * x + 8;
  u64* ws = p.ws;
  const Mask mk = make_mask(lane, true);
  const int kap = lane >> 4, cc = lane & 15;

  if (wave < 8) {
    const size_t hb = ((size_t)g * SK + (SK - WIN)) * DH;
    const rsrc_t rk = mk_rsrc(p.kc + hb, WIN * DH * 2), rv = mk_rsrc(p.vc + hb, WIN * DH * 2);
    const bool three = wave + 16 < ntile;
    const unsigned tso = (tile0 + wave) * 4096;
    const unsigned ldsk = (unsigned)(uintptr_t)(L + L_K) + wave * 3 * 4096;
    unsigned vk[4];
#pragma unroll
    for (int q = 0; q < 4; ++q) vk[q] = (4 * q + kap) * 256 + ((cc ^ (4 * q + kap)) << 4);
#define KDMA(s, q) asm volatile("s_mov_b32 m0, %0\n\ts_nop 0\n\tbuffer_load_dwordx4 %1, %2, %3 offen nt lds" \
                     :: "s"(ldsk + (s) * 4096 + (q) * 1024), "v"(vk[q]), "s"(rk), "s"(tso + (s) * 32768) : "m0", "memory")
#define THR(n) do { if ((n) < ATT_N1 && (n) >= ATT_D1 && moda) asm volatile("s_waitcnt vmcnt(%0)" :: "n"(ATT_D1 - 1) : "memory"); } while (0)
    if (moda) __builtin_amdgcn_s_barrier();
    __builtin_amdgcn_s_setprio(2);
    KDMA(0, 0);
    gate_wait(t0, GATE_ATT);
    THR(1); KDMA(0, 1); THR(2); KDMA(0, 2); THR(3); KDMA(0, 3);
    THR(4); KDMA(1, 0); KDMA(1, 1); KDMA(1, 2); KDMA(1, 3);
    if (three) { KDMA(2, 0); KDMA(2, 1); KDMA(2, 2); KDMA(2, 3); }
#undef KDMA
#undef THR
    __builtin_amdgcn_s_setprio(0);
    v4u vr[12];
    const unsigned vv = kap * 1024 + cc * 16;
#pragma unroll
    for (int t = 0; t < 8; ++t) vr[t] = ldb(rv, vv + (t & 3) * 256, tso + (t >> 2) * 32768);
    if (three) {
#pragma unroll
      for (int t = 8; t < 12; ++t) vr[t] = ldb(rv, vv + (t & 3) * 256, tso + 65536);
    } else {
#pragma unroll
      for (int t = 8; t < 12; ++t) vr[t] = v4u{0u, 0u, 0u, 0u};
    }
    __builtin_amdgcn_sched_barrier(0);
    if (three) asm volatile("s_waitcnt vmcnt(12)" ::: "memory");
    else asm volatile("s_waitcnt vmcnt(8)" ::: "memory");
    v4u kf[12];
#pragma unroll
    for (int s = 0; s < 3; ++s)
#pragma unroll
      for (int ks = 0; ks < 4; ++ks)
        kf[s * 4 + ks] = *(const LAS v4u*)(L + L_K + (wave * 3 + (s < 2 || three ? s : 0)) * 4096 + cc * 256 + (((4 * ks + kap) ^ cc) << 4));
#pragma unroll
    for (int t = 0; t < 12; ++t) PIN(kf[t]);
    const int head = (lane >> 3) & 1;
    const int me = (wave & 3) * 64 + lane;
    unsigned qsrc = L_Q + head * 256 + kap * 16, mxw = L_MX + (head * 8 + wave) * 4, mxr = L_MX + head * 32, olw = L_OL + wave * 1024 + lane * 16;
    unsigned msrc = L_OL + ((16 * (((me >> 7) * 8 + (me & 7)) >> 2) + ((me & 127) >> 3)) * 4 + (me & 3)) * 4;
    gu64* mdst = (gu64*)ws + G_O + ((g * 8 + (me >> 5)) * NSPLIT + j) * 64 + (me & 31);
    bool lead = (lane & 0x37) == 0;
    PIN(qsrc); PIN(mxw); PIN(mxr); PIN(olw); PIN(msrc); PIN(mdst);
    bar();
    v4u qf[4];
#pragma unroll
    for (int ks = 0; ks < 4; ++ks) qf[ks] = *(const LAS v4u*)(L + qsrc + ks * 64);
#pragma unroll
    for (int ks = 0; ks < 4; ++ks) PIN(qf[ks]);
    __builtin_amdgcn_sched_barrier(0);
    f32x4 acc[3] = {{0.f, 0.f, 0.f, 0.f}, {0.f, 0.f, 0.f, 0.f}, {0.f, 0.f, 0.f, 0.f}};
#pragma unroll
    for (int ks = 0; ks < 4; ++ks)
#pragma unroll
      for (int s = 0; s < 3; ++s) acc[s] = mfma(kf[s * 4 + ks], qf[ks], acc[s]);
    __builtin_amdgcn_sched_barrier(0);
    float sc[12];
#pragma unroll
    for (int t = 0; t < 12; t += 2) {
      const unsigned u = cvt2(acc[t >> 2][t & 3], acc[t >> 2][(t & 3) + 1]);
      const unsigned w = cvt2(__uint_as_float(u << 16) * 0.08838834764831845f, __uint_as_float(u & 0xFFFF0000u) * 0.08838834764831845f);
      sc[t] = __uint_as_float(w << 16);
      sc[t + 1] = __uint_as_float(w & 0xFFFF0000u);
    }
    if (!three) {
#pragma unroll
      for (int t = 8; t < 12; ++t) sc[t] = -INFINITY;
    }
    float mx = fmx3n(fmx3(fmx3(sc[0], sc[1], sc[2]), fmx3(sc[3], sc[4], sc[5]), fmx3(sc[6], sc[7], sc[8])), fmx3(sc[9], sc[10], sc[11]), sc[0]);
    mx = x16_max(mx);
    if (lead) *(LAS float*)(L + mxw) = mx;
    lds_wait();
    if (lane == 0) __hip_atomic_fetch_add((LAS unsigned*)(L + L_CNT), 1u, __ATOMIC_RELAXED, __HIP_MEMORY_SCOPE_WORKGROUP);
    while (*(volatile LAS unsigned*)(L + L_CNT) < 8u) {}
    asm volatile("" ::: "memory");
    float m;
    {
      const f32x4 a = *(const LAS f32x4*)(L + mxr), b = *(const LAS f32x4*)(L + mxr + 16);
      m = fmx3(fmx3(a[0], a[1], a[2]), fmx3(a[3], b[0], b[1]), fmx(b[2], b[3]));
    }
    v4u af[12];
    float ls0 = 0.f, ls1 = 0.f;
    const float mneg = -m * LOG2E;
#pragma unroll
    for (int t = 0; t < 12; t += 2) {
      const float e0 = __builtin_amdgcn_exp2f(fmaf(sc[t], LOG2E, mneg)), e1 = __builtin_amdgcn_exp2f(fmaf(sc[t + 1], LOG2E, mneg));
      ls0 += e0; ls1 += e1;
      af[t] = afrag(pkr(e0), mk); af[t + 1] = afrag(pkr(e1), mk);
    }
    const float ls = x16_sum(ls0 + ls1);
    if (lead) *(LAS float*)(L + mxw + 64) = ls;
    const Tag tag = tag_get(L);
    __builtin_amdgcn_sched_barrier(0);
    f32x4 o0 = {0.f, 0.f, 0.f, 0.f}, o1 = {0.f, 0.f, 0.f, 0.f};
#pragma unroll
    for (int t = 0; t < 12; t += 2) { o0 = mfma(af[t], vr[t], o0); o1 = mfma(af[t + 1], vr[t + 1], o1); }
    *(LAS f32x4*)(L + olw) = o0 + o1;
    bar();
    if (wave < 4) {
      const LAS float* src = (const LAS float*)(L + msrc);
      float s[8];
#pragma unroll
      for (int w = 0; w < 8; ++w) s[w] = src[w * 256];
      pubp(mdst, fadd(fadd(fadd(s[0], s[1]), fadd(s[2], s[3])), fadd(fadd(s[4], s[5]), fadd(s[6], s[7]))), tag);
    } else if (wave == 4 && lane < 16) {
      const int u = lane >> 1, hh = u >> 2;
      const f32x4 a = *(const LAS f32x4*)(L + ((lane & 1) ? L_LS : L_MX) + hh * 32), b = *(const LAS f32x4*)(L + ((lane & 1) ? L_LS : L_MX) + hh * 32 + 16);
      const float vs = ((a[0] + a[1]) + (a[2] + a[3])) + ((b[0] + b[1]) + (b[2] + b[3]));
      const float vm = fmaxf(fmaxf(fmaxf(a[0], a[1]), fmaxf(a[2], a[3])), fmaxf(fmaxf(b[0], b[1]), fmaxf(b[2], b[3])));
      pub(ws, G_O + ((g * 8 + u) * NSPLIT + j) * 64 + 32 + (lane & 1), (lane & 1) ? vs : vm, tag);
    }
    if (owner) {
      wo_phase(L, wo_prep(ws, g * 8 + j, wave, lane), tag, mk);
    }
  } else {
    __builtin_amdgcn_s_setprio(3);
    Tag tag;
    {
      if (moda) {
        u64 st = load_state(ws);
        ModA ma;
        moda_load(p, 9 * x + (odd ? 5 : 0) + j, lane, ma);
        __builtin_amdgcn_sched_barrier(0);
        __builtin_amdgcn_s_barrier();
        asm volatile("" : "+v"(st));
        tag = tag_put(L, st, lane);
        moda_finish(L, ws, 9 * x + (odd ? 5 : 0) + j, lane, ma, mk, (LAS float*)(L + L_MA), tag);
      } else {
        tag = tag_put(L, load_state(ws), lane);
      }
    }
    float xr = 0.f;
    if (redc >= 0) xr = bf2f(p.x[redc * 8 + (lane & 7)]);
    gate_wait(t0, GATE_Q);
    const rsrc_t rw = mk_rsrc(ws, G_END * 8);
    if (lane == 0) *(volatile LAS unsigned*)(L + L_CNT) = 0u;
    q_handoff(rw, (G_Q + 2 * g * 256 + 2 * lane) * 8, (unsigned)(uintptr_t)(L + L_Q) + lane * 4, tag);
    if (owner) {
      const int ob = G_O + (g * 8 + j) * NSPLIT * 64;
      pretouch<1>(L, ws, [&](int) { const int q_ = lane < 33 ? lane : 32; return ob + (q_ / 3) * 64 + (q_ % 3) * 16; });
    }
    if (redc >= 0) {
      pretouch<2>(L, ws, [&](int s) { return G_WO + (s * 64 + lane) * 576 + redc * 8; });
      pretouch<2>(L, ws, [&](int s) { const int f_ = s * 64 + lane; return G_DN + (f_ < 72 ? f_ : 71) * 576 + redc * 8; });
    }
    if (owner) {
      const rsrc_t rwo = mk_rsrc(p.wo, 4096 * D * 2);
      const unsigned wo_v = (lane >> 3) * (D * 2) + (lane & 7) * 16;
      const unsigned wo_s = (g * 256 + j * 32) * (D * 2), ldsw = (unsigned)(uintptr_t)(L + L_WO);
#pragma unroll
      for (int w = 0; w < 9; ++w)
#pragma unroll
        for (int t = 0; t < 4; ++t)
          asm volatile("s_mov_b32 m0, %0\n\ts_nop 0\n\tbuffer_load_dwordx4 %1, %2, %3 offen nt lds"
                       :: "s"(ldsw + (w * 4 + t) * 1024), "v"(wo_v), "s"(rwo), "s"(wo_s + w * 128 + t * 8 * (D * 2)) : "m0", "memory");
    }
    bar();
    if (owner) {
      const int ob = G_O + (g * 8 + j) * NSPLIT * 64;
      const int k2 = kap < 3 ? 8 + kap : 10;
      unsigned obw = L_OB + (((2 * cc) & 7) * 4 + (cc >> 2)) * 4;
      float live = kap < 3 ? 1.f : 0.f;
      PIN(obw); PIN(live);
      const WoAddr wa = wo_prep(ws, g * 8 + j, 8, lane);
      G2 go[6];
      gather2<6>(rw, tag, go, [&](int k) { const int sp = k % 3 == 0 ? kap : k % 3 == 1 ? 4 + kap : k2; return ob + sp * 64 + (k < 3 ? 2 * cc : 32); });
      const float M = x16_max(fmaxf(fmaxf(go[3].lo(), go[4].lo()), go[5].lo()));
      const float Mn = -M * LOG2E;
      const float w0 = __builtin_amdgcn_exp2f(fmaf(go[3].lo(), LOG2E, Mn)), w1 = __builtin_amdgcn_exp2f(fmaf(go[4].lo(), LOG2E, Mn));
      const float w2 = __builtin_amdgcn_exp2f(fmaf(go[5].lo(), LOG2E, Mn)) * live;
      const float Ls = x16_sum(fmaf(w2, go[5].hi(), fmaf(w1, go[4].hi(), w0 * go[3].hi())));
      const float o0 = x16_sum(fmaf(w2, go[2].lo(), fmaf(w1, go[1].lo(), w0 * go[0].lo())));
      const float o1 = x16_sum(fmaf(w2, go[2].hi(), fmaf(w1, go[1].hi(), w0 * go[0].hi())));
      const float rl = rcp_f(Ls);
      *(LAS unsigned*)(L + obw) = pkr(o0 * rl);
      *(LAS unsigned*)(L + obw + 16) = pkr(o1 * rl);
      asm volatile("s_waitcnt vmcnt(0)" ::: "memory");
      wo_phase(L, wa, tag, mk);
    }
    if (redc >= 0) {
      u64 gg[8];
      gather<8>(ws, tag, gg, [&](int k) { return G_MOD + (k & 3) * NMOD + (k < 4 ? 2 : 5) * D + redc * 8 + (lane & 7); });
      float ga = rbf(fadd(fadd(gval(gg[0]), gval(gg[1])), fadd(gval(gg[2]), gval(gg[3]))));
      float gb = rbf(fadd(fadd(gval(gg[4]), gval(gg[5])), fadd(gval(gg[6]), gval(gg[7]))));
      gu64* xdst = (gu64*)ws + G_X2 + redc * 16 + (lane & 7);
      GAS u16* odst = (GAS u16*)p.out + redc * 8 + (lane & 7);
      PIN(ga); PIN(gb); PIN(xr); PIN(xdst); PIN(odst);
      float x2;
      {
        G2 gw[8];
        gather2<8>(rw, tag, gw, [&](int s) { return G_WO + (s * 16 + (lane >> 2)) * 576 + redc * 8 + 2 * (lane & 3); });
        float t0_ = faddn(fadd(fadd(gw[0].lo(), gw[1].lo()), fadd(gw[2].lo(), gw[3].lo())), fadd(fadd(gw[4].lo(), gw[5].lo()), fadd(gw[6].lo(), gw[7].lo())));
        float t1_ = faddn(fadd(fadd(gw[0].hi(), gw[1].hi()), fadd(gw[2].hi(), gw[3].hi())), fadd(fadd(gw[4].hi(), gw[5].hi()), fadd(gw[6].hi(), gw[7].hi())));
        t0_ += dpp<ROR8>(t0_); t1_ += dpp<ROR8>(t1_); t0_ += dpp<ROR4>(t0_); t1_ += dpp<ROR4>(t1_);
        t0_ = x16_sum(t0_); t1_ = x16_sum(t1_);
        const float ts = pair_pick(t0_, t1_, lane);
        x2 = rbf(xr + rbf(ga * rbf(ts)));
        if (lane < 8) pubp(xdst, x2, tag);
      }
      {
        G2 gd[5];
        float w4 = lane < 32 ? 1.f : 0.f;
        PIN(w4);
        gather2<5>(rw, tag, gd, [&](int s) { const int f_ = s * 16 + (lane >> 2); return G_DN + (f_ < 72 ? f_ : 71) * 576 + redc * 8 + 2 * (lane & 3); });
        float t0_ = fmaf(w4, gd[4].lo(), fadd(fadd(gd[0].lo(), gd[1].lo()), fadd(gd[2].lo(), gd[3].lo())));
        float t1_ = fmaf(w4, gd[4].hi(), fadd(fadd(gd[0].hi(), gd[1].hi()), fadd(gd[2].hi(), gd[3].hi())));
        t0_ += dpp<ROR8>(t0_); t1_ += dpp<ROR8>(t1_); t0_ += dpp<ROR4>(t0_); t1_ += dpp<ROR4>(t1_);
        t0_ = x16_sum(t0_); t1_ = x16_sum(t1_);
        const float ts = pair_pick(t0_, t1_, lane);
        const unsigned o = pkr(x2 + rbf(gb * rbf(ts)));
        if (lane < 8) *odst = (u16)o;
      }
    }
  }
}

DEV void role_ffn(const Params& p, LAS unsigned char* L, int x, int i, int wave, int lane, u64 t0) {
  const int f = 9 * x + i;
  u64* ws = p.ws;
  const Mask mk = make_mask(lane, true);
  LAS float* red = (LAS float*)(L + F_RED);
  const rsrc_t rm = mk_rsrc(p.wmod, D * NMOD * 2), rc = mk_rsrc(p.c, D * 2), rx = mk_rsrc(p.x, D * 2);
  const int hq = 4 * x + (i >> 1), qr = i & 1;
  const rsrc_t rw = mk_rsrc(ws, G_END * 8);
  Tag tag;
  if (i < 8) {
    v4u wa[12];
    if (wave >= 3) {
      const rsrc_t rq = mk_rsrc(p.wq, D * 4096 * 2);
      const unsigned v = (lane >> 4) * 8192 + (lane & 15) * 16, s = (qr * 288 + (wave - 3) * 48) * 8192 + hq * 256;
#pragma unroll
      for (int t = 0; t < 12; ++t) wa[t] = ldb(rq, v, s + t * 4 * 8192);
      __builtin_amdgcn_sched_barrier(0);
    } else {
      __builtin_amdgcn_s_setprio(3);
      const u64 st = load_state(ws);
      float xv[9];
#pragma unroll
      for (int k = 0; k < 9; ++k) xv[k] = bf2f(ldb16(rx, (lane + 64 * k) * 2));
      const int p0 = wave * 64 + lane, pr = p0 < 144 ? p0 : 143;
      const unsigned xo = __builtin_amdgcn_raw_buffer_load_b32(rx, (qr * 288 + 2 * pr) * 2, 0, 0);
      float s = 0.f;
#pragma unroll
      for (int k = 0; k < 9; ++k) s += xv[k];
      const float mf = wave_sum(s) * (1.f / D);
      float v = 0.f;
#pragma unroll
      for (int k = 0; k < 9; ++k) { const float d = xv[k] - mf; v = fmaf(d, d, v); }
      const float rden = ln_rden(wave_sum(v) * (1.f / D)), mean = rbf(mf);
      tag = wave == 0 ? tag_put(L, st, lane) : make_tag(st);
      const int hw = pr / 24, hr = 2 * pr - hw * 48;
      unsigned hdst = F_H + ((hw * 4 + (hr & 3)) * 12 + (hr >> 2)) * 4;
      float ln0 = rbf(rbf(__uint_as_float(xo << 16) - mean) * rden), ln1 = rbf(rbf(__uint_as_float(xo & 0xFFFF0000u) - mean) * rden);
      PIN(hdst); PIN(ln0); PIN(ln1);
      gate_wait(t0, GATE_H1);
      G2 ga[8];
      gather2<8>(rw, tag, ga, [&](int k) { return G_MOD + (k & 3) * NMOD + (k >> 2) * D + qr * 288 + 2 * pr; });
      const float sa0 = rbf(fadd(fadd(ga[0].lo(), ga[1].lo()), fadd(ga[2].lo(), ga[3].lo()))), sa1 = rbf(fadd(fadd(ga[0].hi(), ga[1].hi()), fadd(ga[2].hi(), ga[3].hi())));
      const float ca0 = rbf(fadd(fadd(ga[4].lo(), ga[5].lo()), fadd(ga[6].lo(), ga[7].lo()))), ca1 = rbf(fadd(fadd(ga[4].hi(), ga[5].hi()), fadd(ga[6].hi(), ga[7].hi())));
      *(LAS unsigned*)(L + hdst) = pkr(rbf(ln0 * rbf(1.f + ca0)) + sa0);
      *(LAS unsigned*)(L + hdst + 48) = pkr(rbf(ln1 * rbf(1.f + ca1)) + sa1);
      __builtin_amdgcn_s_setprio(0);
    }
    Mask mq = make_mask(lane, (lane & 8) == 0);
    const int qe = (wave & 1) * 64 + lane;
    unsigned hsrc = F_H + ((wave - 3) * 4 + (lane >> 4)) * 48, qst = F_RED + 4608 + (wave - 3) * 1024 + lane * 16;
    unsigned qsrc = F_RED + 4608 + ((16 * ((qe & 7) >> 2) + (qe >> 3)) * 4 + (qe & 3)) * 4;
    gu64* qdst = (gu64*)ws + G_Q + (hq * 2 + qr) * 128 + qe;
    PIN(mq.m[0]); PIN(mq.m[1]); PIN(mq.m[2]); PIN(mq.m[3]); PIN(hsrc); PIN(qst); PIN(qsrc); PIN(qdst);
    bar();
    if (wave >= 3) {
      const LAS v4u* hp = (const LAS v4u*)(L + hsrc);
      const v4u h0 = hp[0], h1 = hp[1], h2 = hp[2];
      f32x4 d0 = {0.f, 0.f, 0.f, 0.f}, d1 = {0.f, 0.f, 0.f, 0.f};
#pragma unroll
      for (int t = 0; t < 4; ++t) { d0 = mfma(afrag(h0[t], mq), wa[t], d0); d1 = mfma(afrag(h1[t], mq), wa[4 + t], d1); }
#pragma unroll
      for (int t = 0; t < 4; t += 2) { d0 = mfma(afrag(h2[t], mq), wa[8 + t], d0); d1 = mfma(afrag(h2[t + 1], mq), wa[9 + t], d1); }
      *(LAS f32x4*)(L + qst) = d0 + d1;
    }
    bar();
    if (wave < 2) {
      const LAS float* src = (const LAS float*)(L + qsrc);
      pubp(qdst, fadd(fadd(fadd(src[0], src[256]), fadd(src[512], src[768])), fadd(src[1024], src[1280])), tag);
    }
  } else {
    if (wave == 0) tag_put(L, load_state(ws), lane);
    gate_wait(t0, GATE_MODB2);
    ModA ma;
    if (wave < 6) moda_load(p, 72 + 6 * x + wave, lane, ma);
    __builtin_amdgcn_sched_barrier(0);
    bar();
    if (wave < 6) {
      tag = tag_get(L);
      moda_finish(L, ws, 72 + 6 * x + wave, lane, ma, mk, (LAS float*)(L + F_RED + wave * 1024), tag);
    }
  }
  tag = tag_get(L);
  pretouch<1>(L, ws, [&](int) { const int e = wave * 64 + lane; return G_X2 + (e >> 3) * 16 + (e & 7); });
  const bool slow_tile = ((((unsigned)(uintptr_t)p.wg >> 7) + f) & 7) == 3;
  v4u wg[9], wu[9], wd[8];
  if (!slow_tile) gate_wait(t0, GATE_FFN);
  if (wave < 8) {
    const rsrc_t rg = mk_rsrc(p.wg, D * FF * 2), ru = mk_rsrc(p.wu, D * FF * 2);
    const unsigned v = (lane >> 3) * (FF * 2) + (lane & 7) * 16, s = wave * 72 * (FF * 2) + f * 128;
#pragma unroll
    for (int t = 0; t < 9; ++t) { wg[t] = ldb(rg, v, s + t * 8 * (FF * 2)); wu[t] = ldb(ru, v, s + t * 8 * (FF * 2)); }
  }
  __builtin_amdgcn_sched_barrier(0);
  unsigned h2w, h2r, stw, str_, gur, guw0, actw, actr, trw, trr;
  gu64* ddst;
  {
    const int e = wave * 64 + lane, hw = e / 72, rem = e - hw * 72, c = (wave & 3) * 16 + (lane >> 2);
    h2w = F_H2 + ((hw * 8 + (rem & 7)) * 12 + (rem >> 3)) * 4;
    h2r = F_H2 + ((wave & 7) * 8 + (lane >> 3)) * 48;
    stw = F_ST + wave * 8;
    str_ = F_ST + ((lane & 15) < 9 ? (lane & 15) : 9) * 8;
    guw0 = F_RED + (wave & 7) * 1040 + red_off(lane, 256);
    gur = F_RED + ((lane & 3) * 520 + c) * 4;
    actw = F_ACT + ((lane & 3) == 0 ? (c & 7) * 8 + (c >> 3) : 64) * 4;
    actr = F_ACT + (lane >> 3) * 32;
    trw = F_TR + wave * 640 + red_off(lane, 128);
    trr = F_TR + wave * 640 + lane * 4;
    ddst = (gu64*)ws + G_DN + f * 576 + wave * 64 + lane;
    if (wave == 0 && lane == 0) *(LAS f32x2*)(L + F_ST + 72) = f32x2{0.f, 0.f};
  }
  PIN(h2w); PIN(h2r); PIN(stw); PIN(str_); PIN(guw0); PIN(gur); PIN(actw); PIN(actr); PIN(trw); PIN(trr); PIN(ddst);
  {
    const int e = wave * 64 + lane;
    u64 gs[9];
    bool have_x2 = false;
    for (int spin = 0; spin < SPIN_CAP; ++spin) {
#pragma unroll
      for (int k = 0; k < 8; ++k) gs[k] = peek(ws, G_MOD + (k & 3) * NMOD + (k < 4 ? 3 : 4) * D + e);
      gs[8] = peek(ws, G_X2 + (e >> 3) * 16 + (e & 7));
      Chk c, cx;
#pragma unroll
      for (int k = 0; k < 8; ++k) c.add(gs[k], tag);
      cx.add(gs[8], tag);
      have_x2 = cx.all_ok();
      if (c.all_ok()) break;
    }
    float sb = rbf(fadd(fadd(gval(gs[0]), gval(gs[1])), fadd(gval(gs[2]), gval(gs[3]))));
    float c1b = rbf(1.f + rbf(fadd(fadd(gval(gs[4]), gval(gs[5])), fadd(gval(gs[6]), gval(gs[7])))));
    float on = (lane & 15) < 9 ? 64.f : 0.f;
    PIN(sb); PIN(c1b); PIN(on);
    u64 gx[1] = {gs[8]};
    if (!have_x2) gather<1>(ws, tag, gx, [&](int) { return G_X2 + (e >> 3) * 16 + (e & 7); });
    const float xv = gval(gx[0]);
    const float sh = __uint_as_float(__builtin_amdgcn_readfirstlane(__float_as_uint(xv)));
    const float d = xv - sh;
    float s1 = d, s2 = d * d;
    s1 += dpp<ROR8>(s1); s2 += dpp<ROR8>(s2); s1 += dpp<ROR4>(s1); s2 += dpp<ROR4>(s2);
    s1 += dpp<ROR2>(s1); s2 += dpp<ROR2>(s2); s1 += dpp<ROR1>(s1); s2 += dpp<ROR1>(s2);
    s1 = x16_sum(s1); s2 = x16_sum(s2);
    const float dm = s1 * (1.f / 64.f);
    if (lane == 0) *(LAS f32x2*)(L + stw) = f32x2{sh + dm, fmaf(-dm, s1, s2)};
    bar();
    const f32x2 st = *(const LAS f32x2*)(L + str_);
    __builtin_amdgcn_sched_barrier(0);
    {
      const rsrc_t rd = mk_rsrc(p.wd, FF * D * 2);
      const unsigned v = (lane >> 3) * (D * 2) + (lane & 7) * 16, s = f * 64 * (D * 2) + wave * 128;
#pragma unroll
      for (int t = 0; t < DN_SECOND; ++t) wd[t] = ldb(rd, v, s + t * 8 * (D * 2));
      __builtin_amdgcn_sched_barrier(0);
    }
    const float mf = row_sum(st[0]) * (1.f / 9.f);
    const float dv = st[0] - mf;
    const float m2 = row_sum(fmaf(on * dv, dv, st[1]));
    *(LAS unsigned*)(L + h2w) = ln_apply_pk(xv, rbf(mf), ln_rden(m2 * (1.f / D)), c1b, sb);
  }
  bar();
  if (f == 0 && threadIdx.x == 0) __hip_atomic_store((gu64*)ws + G_EPOCH, ((u64)tag.hi << 32) | (u64)tag.rest, RLX_AGENT);
  v4u h0, h1;
  unsigned h8;
  {
    const LAS v4u* hp = (const LAS v4u*)(L + h2r);
    h0 = hp[0]; h1 = hp[1]; h8 = ((const LAS unsigned*)hp)[8];
  }
  __builtin_amdgcn_sched_barrier(0);
  {
    const rsrc_t rd = mk_rsrc(p.wd, FF * D * 2);
    const unsigned v = (lane >> 3) * (D * 2) + (lane & 7) * 16, s = f * 64 * (D * 2) + wave * 128;
#pragma unroll
    for (int t = DN_SECOND; t < 8; ++t) wd[t] = ldb(rd, v, s + t * 8 * (D * 2));
    __builtin_amdgcn_sched_barrier(0);
  }
  if (wave < 8) {
    PIN(h0); PIN(h1); PIN(h8);
    f32x4 dg = {0.f, 0.f, 0.f, 0.f}, du = {0.f, 0.f, 0.f, 0.f};
#pragma unroll
    for (int t = 0; t < 9; ++t) {
      const v4u a = afrag(t < 4 ? h0[t & 3] : t < 8 ? h1[t & 3] : h8, mk);
      dg = mfma(a, wg[t], dg);
      du = mfma(a, wu[t], du);
    }
    *(LAS f32x4*)(L + guw0) = dg;
    *(LAS f32x4*)(L + guw0 + ((lane >> 5) == ((lane >> 3) & 1) ? 512 : 0)) = du;
  }
  bar();
  if (wave < 4) {
    const LAS float* r0 = (const LAS float*)(L + gur);
    float g = faddn(fadd(r0[0], r0[64]), fadd(r0[260], r0[324])), u = faddn(fadd(r0[128], r0[192]), fadd(r0[388], r0[452]));
    g += dpp<XOR1>(g); u += dpp<XOR1>(u);
    g += dpp<XOR2>(g); u += dpp<XOR2>(u);
    g = rbf(g); u = rbf(u);
    const float y = g * fmaf(g * g, 0.044715f * 0.7978845608028654f * 2.f * LOG2E, 0.7978845608028654f * 2.f * LOG2E);
    const float th = fmaf(-2.f, rcp_f(__builtin_amdgcn_exp2f(y) + 1.f), 1.f);
    const float act = rbf(0.5f * g * (1.f + th)) * u;
    *(LAS unsigned*)(L + actw) = pkr(act);
  }
  bar();
  {
    const LAS v4u* ap = (const LAS v4u*)(L + actr);
    const v4u a0 = ap[0], a1 = ap[1];
    f32x4 d0 = {0.f, 0.f, 0.f, 0.f}, d1 = {0.f, 0.f, 0.f, 0.f};
#pragma unroll
    for (int t = 0; t < 4; ++t) { d0 = mfma(afrag(a0[t], mk), wd[t], d0); d1 = mfma(afrag(a1[t], mk), wd[4 + t], d1); }
    *(LAS f32x4*)(L + trw) = d0 + d1;
    lds_wait();
    pubp(ddst, fadd(*(const LAS float*)(L + trr), *(const LAS float*)(L + trr + 256)), tag);
  }
}

DEV void role_modb(const Params& p, LAS unsigned char* L, int x, int wave, int lane, u64 t0, u64 code0) {
  if (wave == 8) {
    const unsigned dump = (unsigned)(uintptr_t)(L + L_PF);
#pragma unroll
    for (int k = 0; k < (CODE_PF + 8191) / 8192; ++k) {
      const unsigned o = k * 8192 + lane * 128;
      asm volatile("s_mov_b32 m0, %0\n\ts_nop 0\n\tglobal_load_lds_dword %1, off" :: "s"(dump), "v"(code0 + (o < CODE_PF - 128 ? o : CODE_PF - 128)) : "m0", "memory");
    }
  }
  const Mask mk = make_mask(lane, true);
  if (wave == 0) tag_put(L, load_state(p.ws), lane);
  bar();
  const Tag tag = tag_get(L);
  gate_wait(t0, GATE_MODB);
  for (int k = wave; k < 12; k += 9) {
    ModA ma;
    moda_load(p, 120 + x + 8 * k, lane, ma);
    __builtin_amdgcn_sched_barrier(0);
    moda_finish(L, p.ws, 120 + x + 8 * k, lane, ma, mk, (LAS float*)(L + wave * 1024), tag);
  }
}

__global__ void __launch_bounds__(NTHR) adaln_block_kernel(Params p) {
  __shared__ __attribute__((aligned(16))) unsigned char smem[L_END];
  LAS unsigned char* L = (LAS unsigned char*)smem;
  const int lane = threadIdx.x & 63, wave = __builtin_amdgcn_readfirstlane(threadIdx.x >> 6);
  u64 code0;
  asm volatile("s_getpc_b64 %0" : "=s"(code0));
  code0 &= ~0xFFull;
  const u64 t0 = __builtin_amdgcn_s_memrealtime();
  const int x = blockIdx.x & 7, idx = blockIdx.x >> 3;
  if (idx < 22) role_att(p, L, x, idx, wave, lane, t0);
  else if (idx < 31) role_ffn(p, L, x, idx - 22, wave, lane, t0);
  else role_modb(p, L, x, wave, lane, t0, code0);
}

extern "C" void kernel_launch(void* const* d_in, const int* in_sizes, int n_in, void* d_out, int out_size, void* d_ws,
                              size_t ws_size, hipStream_t stream) {
  Params p;
  p.x = (const u16*)d_in[0];
  p.kc = (const u16*)d_in[1];
  p.vc = (const u16*)d_in[2];
  p.wq = (const u16*)d_in[3];
  p.wo = (const u16*)d_in[4];
  p.wg = (const u16*)d_in[6];
  p.wu = (const u16*)d_in[7];
  p.wd = (const u16*)d_in[8];
  p.c = (const u16*)d_in[9];
  p.wmod = (const u16*)d_in[10];
  p.out = (u16*)d_out;
  p.ws = (u64*)d_ws;
  hipLaunchKernelGGL(adaln_block_kernel, dim3(256), dim3(NTHR), 0, stream, p);
}
```
